```python
import torch

def _softmax(x, axis=-1):
    return torch.softmax(x, dim=axis)

def _gelu(x):
    return torch.nn.functional.gelu(x, approximate='tanh')

def _silu(x):
    return torch.nn.functional.silu(x)

def _sigmoid(x):
    return torch.sigmoid(x)

def _layernorm(x, axis=-1, eps=1e-05):
    m = x.mean(dim=axis, keepdim=True)
    v = x.var(dim=axis, keepdim=True, unbiased=False)
    return (x - m) / torch.sqrt(v + eps)

def _scatter_add(tgt, idx, src, axis=0):
    return tgt.clone().index_add_(axis, idx, src)

def _segment_sum(src, seg, n):
    out = torch.zeros((n,) + src.shape[1:], dtype=src.dtype)
    return out.index_add_(0, seg, src)

def _pad(x, pw):
    flat = tuple((v for pair in reversed(pw) for v in pair))
    return torch.nn.functional.pad(x, flat)
kernel_name = 'parallel_sum__memory_aug_recurrent_mem_tr__dense_activation_gelu__prompt'

def setup_inputs():

    _w = lambda a: (lambda t: t.to(torch.bfloat16) if t.is_floating_point() else t)(torch.as_tensor(a))

    def _A_setup():
        B, S, Dm = (1, 2048, 1024)
        H, Dh = (32, 128)
        M, seg = (8, 32)
        seg = min(seg, S)
        S = S // seg * seg
        return {'x': _w((lambda *s: torch.randn(*s, dtype=torch.bfloat16) * 0.05)(B, S, Dm)), 'mem0': _w((lambda *s: torch.randn(*s, dtype=torch.bfloat16) * 0.2)(B, M, Dm)), 'Wq': _w((lambda *s: torch.randn(*s, dtype=torch.bfloat16) * 1.6)(Dm, H, Dh) * Dm ** (-0.5)), 'Wk': _w((lambda *s: torch.randn(*s, dtype=torch.bfloat16) * 1.6)(Dm, H, Dh) * Dm ** (-0.5)), 'Wv': _w((lambda *s: torch.randn(*s, dtype=torch.bfloat16) * 0.2)(Dm, H, Dh) * Dm ** (-0.5)), 'Wo': _w((lambda *s: torch.randn(*s, dtype=torch.bfloat16) * 0.2)(H, Dh, Dm) * (H * Dh) ** (-0.5)), 'seg_len': seg}
    _A_in = _A_setup()

    def _B_setup():
        B, S, D, F = (1, 2048, 1024, 3584)
        return {'x': _w((lambda *s: torch.randn(*s, dtype=torch.bfloat16) * 0.05)(B, S, D)), 'w1': _w((lambda *s: torch.randn(*s, dtype=torch.bfloat16) * 0.4)(D, F) * D ** (-0.5)), 'b1': _w((lambda *s: torch.randn(*s, dtype=torch.bfloat16) * 12.8)(F) * 0.01), 'w2': _w((lambda *s: torch.randn(*s, dtype=torch.bfloat16) * 0.1)(F, D) * F ** (-0.5)), 'b2': _w((lambda *s: torch.randn(*s, dtype=torch.bfloat16) * 1.6)(D) * 0.01)}
    _B_in = _B_setup()
    return {'A_x': _A_in['x'], 'A_mem0': _A_in['mem0'], 'A_Wq': _A_in['Wq'], 'A_Wk': _A_in['Wk'], 'A_Wv': _A_in['Wv'], 'A_Wo': _A_in['Wo'], 'A_seg_len': _A_in['seg_len'], 'B_x': _B_in['x'], 'B_w1': _B_in['w1'], 'B_b1': _B_in['b1'], 'B_w2': _B_in['w2'], 'B_b2': _B_in['b2']}

def reference(A_x, A_mem0, A_Wq, A_Wk, A_Wv, A_Wo, A_seg_len, B_x, B_w1, B_b1, B_w2, B_b2):

    def _A_ref(x, mem0, Wq, Wk, Wv, Wo, seg_len):
        B, S, Dm = x.shape
        H, Dh = (Wq.shape[1], Wq.shape[2])
        M = mem0.shape[1]
        scale = Dh ** (-0.5)
        n_seg = S // seg_len
        L = M + seg_len
        idx = torch.arange(L)
        mask = (idx[:, None] < M) | (idx[:, None] >= idx[None, :])
        mem = mem0
        outs = []
        for s in range(n_seg):
            xs = x[:, s * seg_len:(s + 1) * seg_len, :].clone()
            h = torch.concatenate([mem, xs], axis=1)
            q = torch.einsum('btd,dhe->bhte', h, Wq)
            k = torch.einsum('btd,dhe->bhte', h, Wk)
            v = torch.einsum('btd,dhe->bhte', h, Wv)
            scores = torch.einsum('bhqe,bhke->bhqk', q, k) * scale
            scores = torch.where(mask[None, None], scores, -1000000000.0)
            p = _softmax(scores, axis=-1)
            o = torch.einsum('bhqk,bhke->bhqe', p, v)
            o = torch.einsum('bhte,hed->btd', o, Wo)
            mem = o[:, :M, :].clone()
            outs.append(o[:, M:, :])
        return torch.concatenate(outs, axis=1)

    def _B_ref(x, w1, b1, w2, b2):
        h = torch.einsum('bsd,df->bsf', x, w1) + b1
        h = _gelu(h)
        return torch.einsum('bsf,fd->bsd', h, w2) + b2
    x = A_x
    xn = _layernorm(x, axis=-1)
    return x + _A_ref(xn, A_mem0, A_Wq, A_Wk, A_Wv, A_Wo, A_seg_len) + _B_ref(xn, B_w1, B_b1, B_w2, B_b2)
```

```cpp
#include <hip/hip_runtime.h>
#include <hip/hip_bf16.h>
#include <stdint.h>

typedef unsigned short u16;
typedef unsigned int u32;
typedef unsigned long long u64;
#define GAS __attribute__((address_space(1)))
typedef GAS u64 gu64;
typedef GAS u32 gu32;
typedef __attribute__((ext_vector_type(8))) short bf16x8;
typedef __attribute__((ext_vector_type(4))) float f32x4;
typedef __attribute__((ext_vector_type(4))) unsigned int u32x4;
#define RLX_AGENT __ATOMIC_RELAXED, __HIP_MEMORY_SCOPE_AGENT

constexpr int S_ = 2048, DM = 1024, NH = 32, DH = 128, HD = 4096, MM = 8, SEG = 32, NSEG = 64, FF = 3584;

__device__ __forceinline__ float bf2f(u16 v) { return __uint_as_float(((u32)v) << 16); }
__device__ __forceinline__ u16 f2bf(float f) { u32 u = __float_as_uint(f); u += 0x7fffu + ((u >> 16) & 1u); return (u16)(u >> 16); }
__device__ __forceinline__ float rbf(float f) { return bf2f(f2bf(f)); }

__global__ __launch_bounds__(256) void ln_kernel(const u16* __restrict__ x, u16* __restrict__ xn) {
  __shared__ float red[8];
  const int row = blockIdx.x, t = threadIdx.x, lane = t & 63, w = t >> 6;
  const ushort4 v = *(const ushort4*)(x + (size_t)row * DM + t * 4);
  float f0 = bf2f(v.x), f1 = bf2f(v.y), f2 = bf2f(v.z), f3 = bf2f(v.w);
  float s = (f0 + f1) + (f2 + f3);
#pragma unroll
  for (int m = 32; m >= 1; m >>= 1) s += __shfl_xor(s, m);
  if (lane == 0) red[w] = s;
  __syncthreads();
  const float mean = ((red[0] + red[1]) + (red[2] + red[3])) * (1.0f / DM);
  float d0 = f0 - mean, d1 = f1 - mean, d2 = f2 - mean, d3 = f3 - mean;
  float q = (d0 * d0 + d1 * d1) + (d2 * d2 + d3 * d3);
#pragma unroll
  for (int m = 32; m >= 1; m >>= 1) q += __shfl_xor(q, m);
  if (lane == 0) red[4 + w] = q;
  __syncthreads();
  const float var = ((red[4] + red[5]) + (red[6] + red[7])) * (1.0f / DM);
  const float mb = rbf(mean), vb = rbf(var);
  const float den = rbf(sqrtf(rbf(vb + 1e-5f)));
  ushort4 o;
  o.x = f2bf(rbf(f0 - mb) / den); o.y = f2bf(rbf(f1 - mb) / den);
  o.z = f2bf(rbf(f2 - mb) / den); o.w = f2bf(rbf(f3 - mb) / den);
  *(ushort4*)(xn + (size_t)row * DM + t * 4) = o;
}

__device__ __forceinline__ float gelu_tanh(float x) {
  const float k0 = 0.7978845608028654f, k1 = 0.044715f;
  const float u = k0 * (x + k1 * x * x * x);
  return 0.5f * x * (1.0f + tanhf(u));
}

struct GemmP {
  const u16* A; const u16* B0; const u16* B1; const u16* B2; const u16* B3;
  u16* C0; u16* C1; u16* C2; u16* C3;
  const u16* bias; const u16* res; const u16* res2;
};

template <int MODE>
__global__ __launch_bounds__(256) void gemm_kernel(GemmP p) {
  constexpr int BK = 32, LDT = 40;
  __shared__ __attribute__((aligned(16))) u16 As[128 * LDT];
  __shared__ __attribute__((aligned(16))) u16 Bs[128 * LDT];
  const int t = threadIdx.x, lane = t & 63, w = t >> 6, wr = w >> 1, wc = w & 1;
  const int mt = blockIdx.y, nt = blockIdx.x;
  const u16* B; u16* C; int ldb, K, lda, ncol0;
  if (MODE == 0) {
    K = DM; lda = DM;
    if (nt < 32) { B = p.B0; C = p.C0; ldb = HD; ncol0 = nt * 128; }
    else if (nt < 64) { B = p.B1; C = p.C1; ldb = HD; ncol0 = (nt - 32) * 128; }
    else if (nt < 96) { B = p.B2; C = p.C2; ldb = HD; ncol0 = (nt - 64) * 128; }
    else { B = p.B3; C = p.C3; ldb = FF; ncol0 = (nt - 96) * 128; }
  } else if (MODE == 1) { K = FF; lda = FF; B = p.B0; C = p.C0; ldb = DM; ncol0 = nt * 128; }
  else { K = HD; lda = HD; B = p.B0; C = p.C0; ldb = DM; ncol0 = nt * 128; }
  const u16* A = p.A + (size_t)mt * 128 * lda;
  f32x4 acc[4][4];
#pragma unroll
  for (int i = 0; i < 4; ++i)
#pragma unroll
    for (int j = 0; j < 4; ++j) acc[i][j] = (f32x4){0.f, 0.f, 0.f, 0.f};
  for (int k0 = 0; k0 < K; k0 += BK) {
#pragma unroll
    for (int i = 0; i < 2; ++i) {
      const int e = t + i * 256;
      const int r = e >> 2, c = (e & 3) * 8;
      *(u32x4*)(&As[r * LDT + c]) = *(const u32x4*)(A + (size_t)r * lda + k0 + c);
      const int kr = e >> 4, nc = (e & 15) * 8;
      const u32x4 bv = *(const u32x4*)(B + (size_t)(k0 + kr) * ldb + ncol0 + nc);
      const u16* bp = (const u16*)&bv;
#pragma unroll
      for (int q = 0; q < 8; ++q) Bs[(nc + q) * LDT + kr] = bp[q];
    }
    __syncthreads();
    bf16x8 af[4], bfr[4];
#pragma unroll
    for (int i = 0; i < 4; ++i) af[i] = *(const bf16x8*)(&As[(wr * 64 + i * 16 + (lane & 15)) * LDT + (lane >> 4) * 8]);
#pragma unroll
    for (int j = 0; j < 4; ++j) bfr[j] = *(const bf16x8*)(&Bs[(wc * 64 + j * 16 + (lane & 15)) * LDT + (lane >> 4) * 8]);
#pragma unroll
    for (int i = 0; i < 4; ++i)
#pragma unroll
      for (int j = 0; j < 4; ++j) acc[i][j] = __builtin_amdgcn_mfma_f32_16x16x32_bf16(af[i], bfr[j], acc[i][j], 0, 0, 0);
    __syncthreads();
  }
  const int ldc = (MODE == 0) ? ((nt < 96) ? HD : FF) : DM;
#pragma unroll
  for (int i = 0; i < 4; ++i)
#pragma unroll
    for (int j = 0; j < 4; ++j)
#pragma unroll
      for (int r = 0; r < 4; ++r) {
        const int row = mt * 128 + wr * 64 + i * 16 + (lane >> 4) * 4 + r;
        const int col = ncol0 + wc * 64 + j * 16 + (lane & 15);
        float v = acc[i][j][r];
        if (MODE == 0) {
          if (nt >= 96) { v = rbf(rbf(v) + bf2f(p.bias[col])); v = gelu_tanh(v); }
        } else if (MODE == 1) {
          v = rbf(v) + bf2f(p.bias[col]);
        } else {
          v = rbf(bf2f(p.res[(size_t)row * DM + col]) + rbf(v)) + bf2f(p.res2[(size_t)row * DM + col]);
        }
        C[(size_t)row * ldc + col] = f2bf(v);
      }
}

struct ChainP {
  const u16* mem0; const u16* Wq; const u16* Wk; const u16* Wv; const u16* Wo;
  const u16* Kx; const u16* Vx; u16* KM; u16* VM;
  u64* memg; u64* qkvg; u64* partg; u32* tmo;
};
constexpr int NG_MEM = 4096;
constexpr int NG_QKV = 192;
constexpr u32 SPIN_CAP = 400000u;

__device__ __forceinline__ void st_gran(u64* g, u32 tag, u32 val) {
  __hip_atomic_store((gu64*)g, ((u64)tag << 32) | (u64)val, RLX_AGENT);
}
__device__ __forceinline__ u64 ld_gran(const u64* g) { return __hip_atomic_load((gu64*)g, RLX_AGENT); }

template <int N>
__device__ __forceinline__ bool sweep(const u64* g, int stride, u32 tag, u32 (&v)[N], u32* tmo) {
  for (u32 spins = 0;; ++spins) {
    bool ok = true;
#pragma unroll
    for (int k = 0; k < N; ++k) { const u64 x = ld_gran(g + (size_t)k * stride); v[k] = (u32)x; ok &= ((u32)(x >> 32) == tag); }
    if (__all(ok)) return true;
    if (spins > SPIN_CAP) { __hip_atomic_store((gu32*)tmo, 1u, RLX_AGENT); return false; }
    if ((spins & 1023u) == 1023u) { if (__builtin_amdgcn_readfirstlane(__hip_atomic_load((gu32*)tmo, RLX_AGENT))) return false; }
    __builtin_amdgcn_s_sleep(1);
  }
}

__global__ __launch_bounds__(256, 1) void chain_kernel(ChainP p) {
  constexpr int LM = 1032;
  constexpr int LQ = 136;
  constexpr int LV = 72;
  __shared__ __attribute__((aligned(16))) u16 memS[8 * LM];
  __shared__ __attribute__((aligned(16))) float red[4 * 8 * 48];
  __shared__ __attribute__((aligned(16))) u16 qS[8 * LQ];
  __shared__ __attribute__((aligned(16))) u16 kS[48 * LQ];
  __shared__ __attribute__((aligned(16))) u16 vT[128 * LV];
  __shared__ __attribute__((aligned(16))) u16 pS[4 * 8 * LV];
  __shared__ __attribute__((aligned(16))) u16 oS[8 * LQ];
  __shared__ __attribute__((aligned(16))) float red2[4 * 32];

  const int t = threadIdx.x, lane = t & 63, w = __builtin_amdgcn_readfirstlane(t >> 6);
  const int b = blockIdx.x, xcd = b & 7, ib = b >> 3;
  const int h = xcd * 4 + (ib >> 3), j = ib & 7;
  const int fr = lane & 15, fq = lane >> 4;

  bf16x8 wf[3][8];
  {
    const u16* Ws[3] = {p.Wq, p.Wk, p.Wv};
#pragma unroll
    for (int m = 0; m < 3; ++m)
#pragma unroll
      for (int s = 0; s < 8; ++s) {
        const u16* src = Ws[m] + (size_t)(256 * w + 32 * s + 8 * fq) * HD + h * DH + 16 * j + fr;
#pragma unroll
        for (int i = 0; i < 8; ++i) wf[m][s][i] = (short)src[(size_t)i * HD];
      }
  }
  bf16x8 wo[2][4];
#pragma unroll
  for (int c = 0; c < 2; ++c)
#pragma unroll
    for (int s = 0; s < 4; ++s) {
      const u16* src = p.Wo + (size_t)(h * DH + 32 * s + 8 * fq) * DM + 128 * j + 16 * (2 * w + c) + fr;
#pragma unroll
      for (int i = 0; i < 8; ++i) wo[c][s][i] = (short)src[(size_t)i * DM];
    }
  for (int e = t; e < 128 * LV; e += 256) vT[e] = 0;
  for (int e = t; e < 48 * LQ; e += 256) kS[e] = 0;
  __syncthreads();

  for (int s = 0; s < NSEG; ++s) {
    const int par = s & 1;
    const u32 epoch = (u32)s + 1u;
    {
#pragma unroll
      for (int i = 0; i < 2; ++i) {
        const int e = t + i * 256, r = e >> 4, c = (e & 15) * 8;
        const u32x4 kv = *(const u32x4*)(p.Kx + (size_t)(s * SEG + r) * HD + h * DH + c);
        *(u32x4*)(&kS[r * LQ + c]) = kv;
        const u32x4 vv = *(const u32x4*)(p.Vx + (size_t)(s * SEG + r) * HD + h * DH + c);
        const u16* vp = (const u16*)&vv;
#pragma unroll
        for (int q = 0; q < 8; ++q) vT[(c + q) * LV + r] = vp[q];
      }
    }
    if (s == 0) {
#pragma unroll
      for (int i = 0; i < 4; ++i) {
        const int e = t + i * 256, r = e >> 7, c = (e & 127) * 8;
        *(u32x4*)(&memS[r * LM + c]) = *(const u32x4*)(p.mem0 + r * DM + c);
      }
    } else {
      u32 v[16];
      const u64* src = p.memg + (size_t)(par ^ 1) * NG_MEM + t;
      if (!sweep<16>(src, 256, (u32)s, v, p.tmo)) return;
#pragma unroll
      for (int k = 0; k < 16; ++k) {
        const int g = t + k * 256;
        *(u32*)(&memS[(g >> 9) * LM + (g & 511) * 2]) = v[k];
      }
    }
    __syncthreads();
    {
      f32x4 acc[3] = {{0.f, 0.f, 0.f, 0.f}, {0.f, 0.f, 0.f, 0.f}, {0.f, 0.f, 0.f, 0.f}};
#pragma unroll
      for (int ks = 0; ks < 8; ++ks) {
        const bf16x8 a = *(const bf16x8*)(&memS[(lane & 7) * LM + 256 * w + 32 * ks + 8 * fq]);
#pragma unroll
        for (int m = 0; m < 3; ++m) acc[m] = __builtin_amdgcn_mfma_f32_16x16x32_bf16(a, wf[m][ks], acc[m], 0, 0, 0);
      }
      if (fq < 2) {
#pragma unroll
        for (int m = 0; m < 3; ++m)
#pragma unroll
          for (int r = 0; r < 4; ++r) red[(w * 8 + fq * 4 + r) * 48 + m * 16 + fr] = acc[m][r];
      }
    }
    __syncthreads();
    if (t < NG_QKV) {
      const int r = t / 24, cp = t % 24;
      float v0 = 0.f, v1 = 0.f;
#pragma unroll
      for (int ww = 0; ww < 4; ++ww) { v0 += red[(ww * 8 + r) * 48 + cp * 2]; v1 += red[(ww * 8 + r) * 48 + cp * 2 + 1]; }
      const u32 pk = (u32)f2bf(v0) | ((u32)f2bf(v1) << 16);
      const int m = cp >> 3, c = (cp & 7) * 2;
      if (s < NSEG - 1) st_gran(p.qkvg + ((size_t)(par * NH + h) * 8 + j) * NG_QKV + t, epoch, pk);
      if (m == 1) *(u32*)(p.KM + (size_t)(s * MM + r) * HD + h * DH + 16 * j + c) = pk;
      if (m == 2) *(u32*)(p.VM + (size_t)(s * MM + r) * HD + h * DH + 16 * j + c) = pk;
    }
    if (s == NSEG - 1) break;
    {
      u32 v[6];
      const u64* src = p.qkvg + (size_t)(par * NH + h) * 8 * NG_QKV + t;
      if (!sweep<6>(src, 256, epoch, v, p.tmo)) return;
#pragma unroll
      for (int k = 0; k < 6; ++k) {
        const int g = t + k * 256, jj = g / NG_QKV, e = g % NG_QKV;
        const int r = e / 24, cp = e % 24, m = cp >> 3, c = 16 * jj + (cp & 7) * 2;
        if (m == 0) *(u32*)(&qS[r * LQ + c]) = v[k];
        else if (m == 1) *(u32*)(&kS[(32 + r) * LQ + c]) = v[k];
        else { vT[c * LV + 32 + r] = (u16)(v[k] & 0xffffu); vT[(c + 1) * LV + 32 + r] = (u16)(v[k] >> 16); }
      }
    }
    __syncthreads();
    {
      f32x4 sc[3] = {{0.f, 0.f, 0.f, 0.f}, {0.f, 0.f, 0.f, 0.f}, {0.f, 0.f, 0.f, 0.f}};
#pragma unroll
      for (int ks = 0; ks < 4; ++ks) {
        const bf16x8 a = *(const bf16x8*)(&qS[(lane & 7) * LQ + 32 * ks + 8 * fq]);
#pragma unroll
        for (int n = 0; n < 3; ++n) {
          const bf16x8 bk = *(const bf16x8*)(&kS[(n * 16 + fr) * LQ + 32 * ks + 8 * fq]);
          sc[n] = __builtin_amdgcn_mfma_f32_16x16x32_bf16(a, bk, sc[n], 0, 0, 0);
        }
      }
      const float scale = 0.08838834764831845f;
      u16* pw = &pS[w * 8 * LV];
#pragma unroll
      for (int r = 0; r < 4; ++r) {
        float x0 = rbf(rbf(sc[0][r]) * scale), x1 = rbf(rbf(sc[1][r]) * scale), x2 = rbf(rbf(sc[2][r]) * scale);
        const bool live2 = fr < 8;
        float mx = fmaxf(x0, x1); if (live2) mx = fmaxf(mx, x2);
#pragma unroll
        for (int m = 8; m >= 1; m >>= 1) mx = fmaxf(mx, __shfl_xor(mx, m));
        const float e0 = __expf(x0 - mx), e1 = __expf(x1 - mx), e2 = live2 ? __expf(x2 - mx) : 0.f;
        float sm = e0 + e1 + e2;
#pragma unroll
        for (int m = 8; m >= 1; m >>= 1) sm += __shfl_xor(sm, m);
        const float inv = 1.0f / sm;
        if (fq < 2) {
          const int row = fq * 4 + r;
          pw[row * LV + fr] = f2bf(e0 * inv);
          pw[row * LV + 16 + fr] = f2bf(e1 * inv);
          pw[row * LV + 32 + fr] = f2bf(e2 * inv);
          pw[row * LV + 48 + fr] = 0;
        }
      }
      f32x4 oc[2] = {{0.f, 0.f, 0.f, 0.f}, {0.f, 0.f, 0.f, 0.f}};
#pragma unroll
      for (int ks = 0; ks < 2; ++ks) {
        const bf16x8 a = *(const bf16x8*)(&pw[(lane & 7) * LV + 32 * ks + 8 * fq]);
#pragma unroll
        for (int c = 0; c < 2; ++c) {
          const bf16x8 bv = *(const bf16x8*)(&vT[(32 * w + 16 * c + fr) * LV + 32 * ks + 8 * fq]);
          oc[c] = __builtin_amdgcn_mfma_f32_16x16x32_bf16(a, bv, oc[c], 0, 0, 0);
        }
      }
      if (fq < 2) {
#pragma unroll
        for (int c = 0; c < 2; ++c)
#pragma unroll
          for (int r = 0; r < 4; ++r) oS[(fq * 4 + r) * LQ + 32 * w + 16 * c + fr] = f2bf(oc[c][r]);
      }
    }
    __syncthreads();
    {
      f32x4 pc[2] = {{0.f, 0.f, 0.f, 0.f}, {0.f, 0.f, 0.f, 0.f}};
#pragma unroll
      for (int ks = 0; ks < 4; ++ks) {
        const bf16x8 a = *(const bf16x8*)(&oS[(lane & 7) * LQ + 32 * ks + 8 * fq]);
#pragma unroll
        for (int c = 0; c < 2; ++c) pc[c] = __builtin_amdgcn_mfma_f32_16x16x32_bf16(a, wo[c][ks], pc[c], 0, 0, 0);
      }
      if (fq < 2) {
#pragma unroll
        for (int c = 0; c < 2; ++c) {
          const int col = 128 * j + 16 * (2 * w + c) + fr;
          u64* dst = p.partg + ((size_t)(par * NH + h) * 256 + (col >> 2)) * 32 + (col & 3);
#pragma unroll
          for (int r = 0; r < 4; ++r) st_gran(dst + (fq * 4 + r) * 4, epoch, __float_as_uint(pc[c][r]));
        }
      }
    }
    {
      u32 v[4];
      const int hh = t >> 3, e0 = (t & 7) * 4;
      const u64* src = p.partg + ((size_t)(par * NH + hh) * 256 + b) * 32 + e0;
      if (!sweep<4>(src, 1, epoch, v, p.tmo)) return;
      float f[4];
#pragma unroll
      for (int k = 0; k < 4; ++k) {
        f[k] = __uint_as_float(v[k]);
        f[k] += __shfl_xor(f[k], 8); f[k] += __shfl_xor(f[k], 16); f[k] += __shfl_xor(f[k], 32);
      }
      if (lane < 8) {
#pragma unroll
        for (int k = 0; k < 4; ++k) red2[w * 32 + e0 + k] = f[k];
      }
    }
    __syncthreads();
    if (t < 16) {
      const int r = t >> 1, cp = t & 1;
      const int e = r * 4 + cp * 2;
      const float v0 = (red2[e] + red2[32 + e]) + (red2[64 + e] + red2[96 + e]);
      const float v1 = (red2[e + 1] + red2[32 + e + 1]) + (red2[64 + e + 1] + red2[96 + e + 1]);
      const u32 pk = (u32)f2bf(v0) | ((u32)f2bf(v1) << 16);
      st_gran(p.memg + (size_t)par * NG_MEM + r * 512 + b * 2 + cp, epoch, pk);
    }
  }
}

__global__ __launch_bounds__(128) void attn_x_kernel(const u16* __restrict__ Q, const u16* __restrict__ Kx, const u16* __restrict__ Vx,
                                                     const u16* __restrict__ KM, const u16* __restrict__ VM, u16* __restrict__ O) {
  __shared__ float qs[32][129];
  __shared__ float ks[40][129];
  __shared__ float vs[40][128];
  __shared__ float ps[32][41];
  const int s = blockIdx.x >> 5, h = blockIdx.x & 31, t = threadIdx.x;
  for (int e = t; e < 32 * 128; e += 128) {
    const int r = e >> 7, c = e & 127;
    qs[r][c] = bf2f(Q[(size_t)(s * SEG + r) * HD + h * DH + c]);
    ks[8 + r][c] = bf2f(Kx[(size_t)(s * SEG + r) * HD + h * DH + c]);
    vs[8 + r][c] = bf2f(Vx[(size_t)(s * SEG + r) * HD + h * DH + c]);
  }
  for (int e = t; e < 8 * 128; e += 128) {
    const int r = e >> 7, c = e & 127;
    ks[r][c] = bf2f(KM[(size_t)(s * MM + r) * HD + h * DH + c]);
    vs[r][c] = bf2f(VM[(size_t)(s * MM + r) * HD + h * DH + c]);
  }
  __syncthreads();
  const float scale = 0.08838834764831845f;
  for (int e = t; e < 32 * 40; e += 128) {
    const int qi = e / 40, ki = e % 40;
    float d = 0.f;
    if (ki < 8 + qi + 1) {
#pragma unroll 8
      for (int c = 0; c < 128; ++c) d += qs[qi][c] * ks[ki][c];
      d = rbf(rbf(d) * scale);
    } else d = -1e9f;
    ps[qi][ki] = d;
  }
  __syncthreads();
  if (t < 32) {
    float mx = -3e38f;
    for (int k = 0; k < 40; ++k) mx = fmaxf(mx, ps[t][k]);
    float sm = 0.f;
    for (int k = 0; k < 40; ++k) { const float e = __expf(ps[t][k] - mx); ps[t][k] = e; sm += e; }
    const float inv = 1.0f / sm;
    for (int k = 0; k < 40; ++k) ps[t][k] = rbf(ps[t][k] * inv);
  }
  __syncthreads();
  for (int qi = 0; qi < 32; ++qi) {
    float a = 0.f;
#pragma unroll 8
    for (int k = 0; k < 40; ++k) a += ps[qi][k] * vs[k][t];
    O[(size_t)(s * SEG + qi) * HD + h * DH + t] = f2bf(a);
  }
}

extern "C" void kernel_launch(void* const* d_in, const int* in_sizes, int n_in,
                              void* d_out, int out_size, void* d_ws, size_t ws_size,
                              hipStream_t stream) {
  const u16* x = (const u16*)d_in[0];
  const u16* mem0 = (const u16*)d_in[1];
  const u16* Wq = (const u16*)d_in[2];
  const u16* Wk = (const u16*)d_in[3];
  const u16* Wv = (const u16*)d_in[4];
  const u16* Wo = (const u16*)d_in[5];
  const u16* w1 = (const u16*)d_in[8];
  const u16* b1 = (const u16*)d_in[9];
  const u16* w2 = (const u16*)d_in[10];
  const u16* b2 = (const u16*)d_in[11];
  u16* out = (u16*)d_out;

  char* ws = (char*)d_ws;
  size_t off = 0;
  auto carve = [&](size_t bytes) { char* q = ws + off; off += (bytes + 255) & ~(size_t)255; return q; };
  u64* memg = (u64*)carve((size_t)2 * NG_MEM * 8);
  u64* qkvg = (u64*)carve((size_t)2 * NH * 8 * NG_QKV * 8);
  u64* partg = (u64*)carve((size_t)2 * NH * 256 * 32 * 8);
  u32* tmo = (u32*)carve(256);
  const size_t sync_bytes = off;
  u16* xn = (u16*)carve((size_t)S_ * DM * 2);
  u16* Qx = (u16*)carve((size_t)S_ * HD * 2);
  u16* Kx = (u16*)carve((size_t)S_ * HD * 2);
  u16* Vx = (u16*)carve((size_t)S_ * HD * 2);
  u16* Hb = (u16*)carve((size_t)S_ * FF * 2);
  u16* Ox = (u16*)carve((size_t)S_ * HD * 2);
  u16* KM = (u16*)carve((size_t)NSEG * MM * HD * 2);
  u16* VM = (u16*)carve((size_t)NSEG * MM * HD * 2);
  u16* Bm = (u16*)carve((size_t)S_ * DM * 2);

  hipMemsetAsync(ws, 0, sync_bytes, stream);
  ln_kernel<<<S_, 256, 0, stream>>>(x, xn);
  {
    GemmP g{xn, Wq, Wk, Wv, w1, Qx, Kx, Vx, Hb, b1, nullptr, nullptr};
    gemm_kernel<0><<<dim3(124, 16), 256, 0, stream>>>(g);
  }
  {
    ChainP c{mem0, Wq, Wk, Wv, Wo, Kx, Vx, KM, VM, memg, qkvg, partg, tmo};
    chain_kernel<<<256, 256, 0, stream>>>(c);
  }
  attn_x_kernel<<<NSEG * NH, 128, 0, stream>>>(Qx, Kx, Vx, KM, VM, Ox);
  {
    GemmP g{Hb, w2, nullptr, nullptr, nullptr, Bm, nullptr, nullptr, nullptr, b2, nullptr, nullptr};
    gemm_kernel<1><<<dim3(8, 16), 256, 0, stream>>>(g);
  }
  {
    GemmP g{Ox, Wo, nullptr, nullptr, nullptr, out, nullptr, nullptr, nullptr, nullptr, x, Bm};
    gemm_kernel<2><<<dim3(8, 16), 256, 0, stream>>>(g);
  }
}
```

```cpp
#include <hip/hip_runtime.h>
#include <hip/hip_bf16.h>
#include <stdint.h>

typedef unsigned short u16;
typedef unsigned int u32;
typedef unsigned long long u64;
#define GAS __attribute__((address_space(1)))
typedef GAS u64 gu64;
typedef GAS u32 gu32;
typedef __attribute__((ext_vector_type(8))) short bf16x8;
typedef __attribute__((ext_vector_type(4))) float f32x4;
typedef __attribute__((ext_vector_type(4))) unsigned int u32x4;
#define RLX_AGENT __ATOMIC_RELAXED, __HIP_MEMORY_SCOPE_AGENT

constexpr int S_ = 2048, DM = 1024, NH = 32, DH = 128, HD = 4096, MM = 8, SEG = 32, NSEG = 64, FF = 3584;

__device__ __forceinline__ float bf2f(u16 v) { return __uint_as_float(((u32)v) << 16); }
__device__ __forceinline__ u16 f2bf(float f) { u32 u = __float_as_uint(f); u += 0x7fffu + ((u >> 16) & 1u); return (u16)(u >> 16); }
__device__ __forceinline__ float rbf(float f) { return bf2f(f2bf(f)); }
typedef __bf16 hbf2 __attribute__((ext_vector_type(2)));
typedef float f32x2 __attribute__((ext_vector_type(2)));
__device__ __forceinline__ u32 cvtpk(float a, float b) { f32x2 v = {a, b}; return __builtin_bit_cast(u32, __builtin_convertvector(v, hbf2)); }
__device__ __forceinline__ float lo2f(u32 v) { return __uint_as_float(v << 16); }
__device__ __forceinline__ float hi2f(u32 v) { return __uint_as_float(v & 0xffff0000u); }
typedef __attribute__((ext_vector_type(4))) unsigned int u32x4b;

__global__ __launch_bounds__(256) void ln_kernel(const u16* __restrict__ x, u16* __restrict__ xn) {
  __shared__ float red[8];
  const int row = blockIdx.x, t = threadIdx.x, lane = t & 63, w = t >> 6;
  const ushort4 v = *(const ushort4*)(x + (size_t)row * DM + t * 4);
  float f0 = bf2f(v.x), f1 = bf2f(v.y), f2 = bf2f(v.z), f3 = bf2f(v.w);
  float s = (f0 + f1) + (f2 + f3);
#pragma unroll
  for (int m = 32; m >= 1; m >>= 1) s += __shfl_xor(s, m);
  if (lane == 0) red[w] = s;
  __syncthreads();
  const float mean = ((red[0] + red[1]) + (red[2] + red[3])) * (1.0f / DM);
  float d0 = f0 - mean, d1 = f1 - mean, d2 = f2 - mean, d3 = f3 - mean;
  float q = (d0 * d0 + d1 * d1) + (d2 * d2 + d3 * d3);
#pragma unroll
  for (int m = 32; m >= 1; m >>= 1) q += __shfl_xor(q, m);
  if (lane == 0) red[4 + w] = q;
  __syncthreads();
  const float var = ((red[4] + red[5]) + (red[6] + red[7])) * (1.0f / DM);
  const float mb = rbf(mean), vb = rbf(var);
  const float den = rbf(sqrtf(rbf(vb + 1e-5f)));
  ushort4 o;
  o.x = f2bf(rbf(f0 - mb) / den); o.y = f2bf(rbf(f1 - mb) / den);
  o.z = f2bf(rbf(f2 - mb) / den); o.w = f2bf(rbf(f3 - mb) / den);
  *(ushort4*)(xn + (size_t)row * DM + t * 4) = o;
}

__device__ __forceinline__ float gelu_tanh(float x) {
  const float k0 = 0.7978845608028654f, k1 = 0.044715f;
  const float u = k0 * (x + k1 * x * x * x);
  return 0.5f * x * (1.0f + tanhf(u));
}

struct GemmP {
  const u16* A; const u16* B0; const u16* B1; const u16* B2; const u16* B3;
  u16* C0; u16* C1; u16* C2; u16* C3;
  const u16* bias; const u16* res; const u16* res2;
};

template <int MODE>
__global__ __launch_bounds__(256) void gemm_kernel(GemmP p) {
  constexpr int BK = 32, LDT = 40;
  __shared__ __attribute__((aligned(16))) u16 As[128 * LDT];
  __shared__ __attribute__((aligned(16))) u16 Bs[128 * LDT];
  const int t = threadIdx.x, lane = t & 63, w = t >> 6, wr = w >> 1, wc = w & 1;
  const int mt = blockIdx.y, nt = blockIdx.x;
  const u16* B; u16* C; int ldb, K, lda, ncol0;
  if (MODE == 0) {
    K = DM; lda = DM;
    if (nt < 32) { B = p.B0; C = p.C0; ldb = HD; ncol0 = nt * 128; }
    else if (nt < 64) { B = p.B1; C = p.C1; ldb = HD; ncol0 = (nt - 32) * 128; }
    else if (nt < 96) { B = p.B2; C = p.C2; ldb = HD; ncol0 = (nt - 64) * 128; }
    else { B = p.B3; C = p.C3; ldb = FF; ncol0 = (nt - 96) * 128; }
  } else if (MODE == 1) { K = FF; lda = FF; B = p.B0; C = p.C0; ldb = DM; ncol0 = nt * 128; }
  else { K = HD; lda = HD; B = p.B0; C = p.C0; ldb = DM; ncol0 = nt * 128; }
  const u16* A = p.A + (size_t)mt * 128 * lda;
  f32x4 acc[4][4];
#pragma unroll
  for (int i = 0; i < 4; ++i)
#pragma unroll
    for (int j = 0; j < 4; ++j) acc[i][j] = (f32x4){0.f, 0.f, 0.f, 0.f};
  for (int k0 = 0; k0 < K; k0 += BK) {
#pragma unroll
    for (int i = 0; i < 2; ++i) {
      const int e = t + i * 256;
      const int r = e >> 2, c = (e & 3) * 8;
      *(u32x4*)(&As[r * LDT + c]) = *(const u32x4*)(A + (size_t)r * lda + k0 + c);
      const int kr = e >> 4, nc = (e & 15) * 8;
      const u32x4 bv = *(const u32x4*)(B + (size_t)(k0 + kr) * ldb + ncol0 + nc);
      const u16* bp = (const u16*)&bv;
#pragma unroll
      for (int q = 0; q < 8; ++q) Bs[(nc + q) * LDT + kr] = bp[q];
    }
    __syncthreads();
    bf16x8 af[4], bfr[4];
#pragma unroll
    for (int i = 0; i < 4; ++i) af[i] = *(const bf16x8*)(&As[(wr * 64 + i * 16 + (lane & 15)) * LDT + (lane >> 4) * 8]);
#pragma unroll
    for (int j = 0; j < 4; ++j) bfr[j] = *(const bf16x8*)(&Bs[(wc * 64 + j * 16 + (lane & 15)) * LDT + (lane >> 4) * 8]);
#pragma unroll
    for (int i = 0; i < 4; ++i)
#pragma unroll
      for (int j = 0; j < 4; ++j) acc[i][j] = __builtin_amdgcn_mfma_f32_16x16x32_bf16(af[i], bfr[j], acc[i][j], 0, 0, 0);
    __syncthreads();
  }
  const int ldc = (MODE == 0) ? ((nt < 96) ? HD : FF) : DM;
#pragma unroll
  for (int i = 0; i < 4; ++i)
#pragma unroll
    for (int j = 0; j < 4; ++j)
#pragma unroll
      for (int r = 0; r < 4; ++r) {
        const int row = mt * 128 + wr * 64 + i * 16 + (lane >> 4) * 4 + r;
        const int col = ncol0 + wc * 64 + j * 16 + (lane & 15);
        float v = acc[i][j][r];
        if (MODE == 0) {
          if (nt >= 96) { v = rbf(rbf(v) + bf2f(p.bias[col])); v = gelu_tanh(v); }
        } else if (MODE == 1) {
          v = rbf(v) + bf2f(p.bias[col]);
        } else {
          v = rbf(bf2f(p.res[(size_t)row * DM + col]) + rbf(v)) + bf2f(p.res2[(size_t)row * DM + col]);
        }
        C[(size_t)row * ldc + col] = f2bf(v);
      }
}

struct ChainP {
  const u16* mem0; const u16* Wq; const u16* Wk; const u16* Wv; const u16* Wo;
  const u16* Kx; const u16* Vx; u16* KM; u16* VM;
  u64* memg; u64* qkvg; u64* partg; u32* tmo;
};
constexpr int NG_MEM = 4096;
constexpr int NG_QKV = 192;
constexpr u32 SPIN_CAP = 400000u;
#define H1_DELAY 16
constexpr int NREP = 8;

__device__ __forceinline__ void st_gran(u64* g, u32 tag, u32 val) {
  __hip_atomic_store((gu64*)g, ((u64)tag << 32) | (u64)val, RLX_AGENT);
}
__device__ __forceinline__ u64 ld_gran(const u64* g) { return __hip_atomic_load((gu64*)g, RLX_AGENT); }
__device__ __forceinline__ void st_gran2(u64* g, u32 tag, u32 v0, u32 v1) {
  const u32x4 d = {v0, tag, v1, tag};
  asm volatile("global_store_dwordx4 %0, %1, off sc1\n\ts_nop 1" :: "v"((gu64*)g), "v"(d) : "memory");
}

__device__ __forceinline__ bool spin_fail(u32& spins, u32* tmo) {
  ++spins;
  if (spins > SPIN_CAP) { __hip_atomic_store((gu32*)tmo, 1u, RLX_AGENT); return true; }
  if ((spins & 1023u) == 0u) { if (__builtin_amdgcn_readfirstlane(__hip_atomic_load((gu32*)tmo, RLX_AGENT))) return true; }
  return false;
}
__device__ __forceinline__ bool wait_sentinel(const u64* g, bool active, u32 tag, u32* tmo) {
  for (u32 spins = 0;;) {
    bool ok = true;
    if (active) ok = ((u32)(ld_gran(g) >> 32) == tag);
    if (__all(ok)) return true;
    if (spin_fail(spins, tmo)) return false;
    __builtin_amdgcn_s_sleep(1);
  }
}
template <int N>
__device__ __forceinline__ bool sweep(const u64* g, int stride, u32 tag, u32 (&v)[N], u32* tmo) {
  for (u32 spins = 0;;) {
    bool ok = true;
#pragma unroll
    for (int k = 0; k < N; ++k) { const u64 x = ld_gran(g + (size_t)k * stride); v[k] = (u32)x; ok &= ((u32)(x >> 32) == tag); }
    if (__all(ok)) return true;
    if (spin_fail(spins, tmo)) return false;
  }
}
template <int N>
__device__ __forceinline__ bool sweep2(__amdgpu_buffer_rsrc_t rs, u32 byte_off, u32 byte_stride, u32 tag, u32 (&v)[2 * N], u32* tmo) {
  for (u32 spins = 0;;) {
    bool ok = true;
#pragma unroll
    for (int k = 0; k < N; ++k) {
      const u32x4 x = __builtin_bit_cast(u32x4, __builtin_amdgcn_raw_buffer_load_b128(rs, byte_off + k * byte_stride, 0, 16));
      v[2 * k] = x[0]; v[2 * k + 1] = x[2]; ok &= (x[1] == tag) & (x[3] == tag);
    }
    if (__all(ok)) return true;
    if (spin_fail(spins, tmo)) return false;
  }
}

#define STAMP(i) do { } while (0)
__device__ __forceinline__ float xrow_max(float v) {
  auto a = __builtin_amdgcn_permlane16_swap(__float_as_uint(v), __float_as_uint(v), false, false);
  v = fmaxf(__uint_as_float(a[0]), __uint_as_float(a[1]));
  auto c = __builtin_amdgcn_permlane32_swap(__float_as_uint(v), __float_as_uint(v), false, false);
  return fmaxf(__uint_as_float(c[0]), __uint_as_float(c[1]));
}
__device__ __forceinline__ float xrow_sum(float v) {
  auto a = __builtin_amdgcn_permlane16_swap(__float_as_uint(v), __float_as_uint(v), false, false);
  v = __uint_as_float(a[0]) + __uint_as_float(a[1]);
  auto c = __builtin_amdgcn_permlane32_swap(__float_as_uint(v), __float_as_uint(v), false, false);
  return __uint_as_float(c[0]) + __uint_as_float(c[1]);
}

__global__ __launch_bounds__(256, 1) void chain_kernel(ChainP p) {
  constexpr int LM = 1032;
  constexpr int LQ = 136;
  constexpr int LV = 72;
  __shared__ __attribute__((aligned(16))) u16 memS[8 * LM];
  __shared__ __attribute__((aligned(16))) float red[4 * 8 * 48];
  __shared__ __attribute__((aligned(16))) u16 qS[8 * LQ];
  __shared__ __attribute__((aligned(16))) u16 kS[48 * LQ];
  __shared__ __attribute__((aligned(16))) u16 vT[128 * LV];

  const int t = threadIdx.x, lane = t & 63, w = __builtin_amdgcn_readfirstlane(t >> 6);
  const int b = blockIdx.x, xcd = b & 7, ib = b >> 3;
  const int h = xcd * 4 + (ib >> 3), j = ib & 7;
  const int R = h * 8 + j;
  const int fr = lane & 15, fq = lane >> 4;
  const __amdgpu_buffer_rsrc_t rs_mem = __builtin_amdgcn_make_buffer_rsrc((void*)p.memg, 0, 2 * NREP * NG_MEM * 8, 0x00020000);
  const __amdgpu_buffer_rsrc_t rs_qkv = __builtin_amdgcn_make_buffer_rsrc((void*)p.qkvg, 0, 2 * NH * 8 * NG_QKV * 8, 0x00020000);
  const __amdgpu_buffer_rsrc_t rs_part = __builtin_amdgcn_make_buffer_rsrc((void*)p.partg, 0, 2 * 256 * NH * 32 * 8, 0x00020000);

  bf16x8 wf[3][8];
  {
    const u16* Ws[3] = {p.Wq, p.Wk, p.Wv};
#pragma unroll
    for (int m = 0; m < 3; ++m)
#pragma unroll
      for (int s = 0; s < 8; ++s) {
        const u16* src = Ws[m] + (size_t)(256 * w + 32 * s + 8 * fq) * HD + h * DH + 16 * j + fr;
#pragma unroll
        for (int i = 0; i < 8; ++i) wf[m][s][i] = (short)src[(size_t)i * HD];
      }
  }
  bf16x8 wo[2][4];
#pragma unroll
  for (int c = 0; c < 2; ++c)
#pragma unroll
    for (int s = 0; s < 4; ++s)
#pragma unroll
      for (int i = 0; i < 8; ++i) {
        const int d = 32 * s + ((i < 4) ? (4 * fq + i) : (16 + 4 * fq + i - 4));
        wo[c][s][i] = (short)p.Wo[(size_t)(h * DH + d) * DM + 128 * j + 16 * (2 * w + c) + fr];
      }
  for (int e = t; e < 128 * LV; e += 256) vT[e] = 0;
  for (int e = t; e < 48 * LQ; e += 256) kS[e] = 0;
  u32x4 kpre[2], vpre[2];
#pragma unroll
  for (int i = 0; i < 2; ++i) {
    const int e = t + i * 256, r = e >> 4, c = (e & 15) * 8;
    kpre[i] = *(const u32x4*)(p.Kx + (size_t)r * HD + h * DH + c);
    vpre[i] = *(const u32x4*)(p.Vx + (size_t)r * HD + h * DH + c);
  }
  __syncthreads();

  for (int s = 0; s < NSEG; ++s) {
    const int par = s & 1;
    const u32 epoch = (u32)s + 1u;
    STAMP(0);
    if (s == 0) {
#pragma unroll
      for (int i = 0; i < 4; ++i) {
        const int e = t + i * 256, r = e >> 7, c = (e & 127) * 8;
        *(u32x4*)(&memS[r * LM + c]) = *(const u32x4*)(p.mem0 + r * DM + c);
      }
    } else {
      u32 v[16];
      const u32 gbase = (u32)(((par ^ 1) * NREP + (b & (NREP - 1))) * NG_MEM + w * 1024);
      __builtin_amdgcn_s_sleep(H1_DELAY);
      if (!sweep2<8>(rs_mem, (gbase + lane * 2) * 8, 128 * 8, (u32)s, v, p.tmo)) return;
#pragma unroll
      for (int k = 0; k < 8; ++k) {
        const int u = lane + k * 64;
        *(uint2*)(&memS[(u & 7) * LM + (w * 64 + (u >> 3)) * 4]) = make_uint2(v[2 * k], v[2 * k + 1]);
      }
    }
#pragma unroll
    for (int i = 0; i < 2; ++i) {
      const int e = t + i * 256, r = e >> 4, c = (e & 15) * 8;
      *(u32x4*)(&kS[r * LQ + c]) = kpre[i];
      const u16* vp = (const u16*)&vpre[i];
      const int pos = (r < 16) ? (8 * (r >> 2) + (r & 3)) : (8 * ((r - 16) >> 2) + 4 + (r & 3));
#pragma unroll
      for (int q = 0; q < 8; ++q) vT[(c + q) * LV + pos] = vp[q];
    }
    __syncthreads();
    STAMP(1);
    {
      f32x4 acc[3] = {{0.f, 0.f, 0.f, 0.f}, {0.f, 0.f, 0.f, 0.f}, {0.f, 0.f, 0.f, 0.f}};
      bf16x8 a[8];
#pragma unroll
      for (int ks = 0; ks < 8; ++ks) a[ks] = *(const bf16x8*)(&memS[(lane & 7) * LM + 256 * w + 32 * ks + 8 * fq]);
#pragma unroll
      for (int ks = 0; ks < 8; ++ks)
#pragma unroll
        for (int m = 0; m < 3; ++m) acc[m] = __builtin_amdgcn_mfma_f32_16x16x32_bf16(a[ks], wf[m][ks], acc[m], 0, 0, 0);
      if (fq < 2) {
#pragma unroll
        for (int m = 0; m < 3; ++m)
#pragma unroll
          for (int r = 0; r < 4; ++r) red[(w * 8 + fq * 4 + r) * 48 + m * 16 + fr] = acc[m][r];
      }
    }
    __syncthreads();
    if (t < NG_QKV) {
      const int r = t / 24, cp = t % 24;
      float v0 = 0.f, v1 = 0.f;
#pragma unroll
      for (int ww = 0; ww < 4; ++ww) { const float2 q2 = *(const float2*)(&red[(ww * 8 + r) * 48 + cp * 2]); v0 += q2.x; v1 += q2.y; }
      const u32 pk = cvtpk(v0, v1);
      const int m = cp >> 3, c = (cp & 7) * 2;
      if (s < NSEG - 1) st_gran(p.qkvg + ((size_t)(par * NH + h) * 8 + j) * NG_QKV + t, epoch, pk);
      if (m == 1) *(u32*)(p.KM + (size_t)(s * MM + r) * HD + h * DH + 16 * j + c) = pk;
      if (m == 2) *(u32*)(p.VM + (size_t)(s * MM + r) * HD + h * DH + 16 * j + c) = pk;
    }
    STAMP(2);
    if (s == NSEG - 1) break;
    bf16x8 kx[2][4], vx[8];
#pragma unroll
    for (int ks = 0; ks < 4; ++ks)
#pragma unroll
      for (int n = 0; n < 2; ++n) kx[n][ks] = *(const bf16x8*)(&kS[(n * 16 + fr) * LQ + 32 * ks + 8 * fq]);
#pragma unroll
    for (int c = 0; c < 8; ++c) vx[c] = *(const bf16x8*)(&vT[(16 * c + fr) * LV + 8 * fq]);
    __builtin_amdgcn_sched_barrier(0);
    {
      u32 v[6];
      const u32 gbase = (u32)(((par * NH + h) * 8 + 2 * w) * NG_QKV);
      if (!sweep2<3>(rs_qkv, (gbase + lane * 2) * 8, 128 * 8, epoch, v, p.tmo)) return;
#pragma unroll
      for (int k = 0; k < 3; ++k) {
        const int g = (lane + k * 64) * 2, jj = 2 * w + g / NG_QKV, e = g % NG_QKV;
        const int r = e / 24, cp = e % 24, m = cp >> 3, c = 16 * jj + (cp & 7) * 2;
        if (m == 0) *(uint2*)(&qS[r * LQ + c]) = make_uint2(v[2 * k], v[2 * k + 1]);
        else if (m == 1) *(uint2*)(&kS[(32 + r) * LQ + c]) = make_uint2(v[2 * k], v[2 * k + 1]);
        else {
          const int pos = 32 + 8 * (r >> 2) + (r & 3);
          vT[c * LV + pos] = (u16)(v[2 * k] & 0xffffu); vT[(c + 1) * LV + pos] = (u16)(v[2 * k] >> 16);
          vT[(c + 2) * LV + pos] = (u16)(v[2 * k + 1] & 0xffffu); vT[(c + 3) * LV + pos] = (u16)(v[2 * k + 1] >> 16);
        }
      }
    }
    __syncthreads();
    STAMP(3);
    {
      f32x4 sc[3] = {{0.f, 0.f, 0.f, 0.f}, {0.f, 0.f, 0.f, 0.f}, {0.f, 0.f, 0.f, 0.f}};
      bf16x8 qf[4], km[4];
      uint2 vm[8];
#pragma unroll
      for (int ks = 0; ks < 4; ++ks) {
        qf[ks] = *(const bf16x8*)(&qS[(lane & 7) * LQ + 32 * ks + 8 * fq]);
        km[ks] = *(const bf16x8*)(&kS[(32 + fr) * LQ + 32 * ks + 8 * fq]);
      }
#pragma unroll
      for (int c = 0; c < 8; ++c) vm[c] = *(const uint2*)(&vT[(16 * c + fr) * LV + 32 + 8 * fq]);
      __builtin_amdgcn_sched_barrier(0);
#pragma unroll
      for (int ks = 0; ks < 4; ++ks) {
        sc[0] = __builtin_amdgcn_mfma_f32_16x16x32_bf16(kx[0][ks], qf[ks], sc[0], 0, 0, 0);
        sc[1] = __builtin_amdgcn_mfma_f32_16x16x32_bf16(kx[1][ks], qf[ks], sc[1], 0, 0, 0);
        sc[2] = __builtin_amdgcn_mfma_f32_16x16x32_bf16(km[ks], qf[ks], sc[2], 0, 0, 0);
      }
      const float scale = 0.08838834764831845f;
      const bool live2 = fq < 2;
      float xs[3][4];
#pragma unroll
      for (int n = 0; n < 3; ++n)
#pragma unroll
        for (int r = 0; r < 4; r += 2) {
          const u32 a = cvtpk(sc[n][r], sc[n][r + 1]);
          const u32 c2 = cvtpk(lo2f(a) * scale, hi2f(a) * scale);
          xs[n][r] = lo2f(c2); xs[n][r + 1] = hi2f(c2);
        }
      if (!live2) { xs[2][0] = xs[2][1] = xs[2][2] = xs[2][3] = -3.0e38f; }
      float mx = fmaxf(fmaxf(fmaxf(xs[0][0], xs[0][1]), fmaxf(xs[0][2], xs[0][3])), fmaxf(fmaxf(xs[1][0], xs[1][1]), fmaxf(xs[1][2], xs[1][3])));
      mx = fmaxf(mx, fmaxf(fmaxf(xs[2][0], xs[2][1]), fmaxf(xs[2][2], xs[2][3])));
      mx = xrow_max(mx);
      float sm = 0.f;
#pragma unroll
      for (int n = 0; n < 3; ++n)
#pragma unroll
        for (int r = 0; r < 4; ++r) { xs[n][r] = __expf(xs[n][r] - mx); sm += xs[n][r]; }
      sm = xrow_sum(sm);
      const float inv = 1.0f / sm;
      u32x4b pw0, pw1;
      pw0[0] = cvtpk(xs[0][0] * inv, xs[0][1] * inv); pw0[1] = cvtpk(xs[0][2] * inv, xs[0][3] * inv);
      pw0[2] = cvtpk(xs[1][0] * inv, xs[1][1] * inv); pw0[3] = cvtpk(xs[1][2] * inv, xs[1][3] * inv);
      pw1[0] = cvtpk(xs[2][0] * inv, xs[2][1] * inv); pw1[1] = cvtpk(xs[2][2] * inv, xs[2][3] * inv);
      pw1[2] = 0u; pw1[3] = 0u;
      const bf16x8 pf0 = __builtin_bit_cast(bf16x8, pw0), pf1 = __builtin_bit_cast(bf16x8, pw1);
      u32x4b ow[4];
#pragma unroll
      for (int c = 0; c < 8; ++c) {
        const u32x4b vmw = {vm[c].x, vm[c].y, 0u, 0u};
        f32x4 oc = {0.f, 0.f, 0.f, 0.f};
        oc = __builtin_amdgcn_mfma_f32_16x16x32_bf16(vx[c], pf0, oc, 0, 0, 0);
        oc = __builtin_amdgcn_mfma_f32_16x16x32_bf16(__builtin_bit_cast(bf16x8, vmw), pf1, oc, 0, 0, 0);
        ow[c >> 1][(c & 1) * 2] = cvtpk(oc[0], oc[1]); ow[c >> 1][(c & 1) * 2 + 1] = cvtpk(oc[2], oc[3]);
      }
      f32x4 pc[2] = {{0.f, 0.f, 0.f, 0.f}, {0.f, 0.f, 0.f, 0.f}};
#pragma unroll
      for (int ks = 0; ks < 4; ++ks)
#pragma unroll
        for (int c = 0; c < 2; ++c) pc[c] = __builtin_amdgcn_mfma_f32_16x16x32_bf16(wo[c][ks], __builtin_bit_cast(bf16x8, ow[ks]), pc[c], 0, 0, 0);
      if (fr < 8) {
#pragma unroll
        for (int c = 0; c < 2; ++c) {
          u64* dst = p.partg + ((size_t)(par * 256 + 32 * j + 8 * w + 4 * c + fq) * NH + h) * 32 + fr * 4;
          st_gran2(dst, epoch, __float_as_uint(pc[c][0]), __float_as_uint(pc[c][1]));
          st_gran2(dst + 2, epoch, __float_as_uint(pc[c][2]), __float_as_uint(pc[c][3]));
        }
      }
    }
    STAMP(4);
    __syncthreads();
#pragma unroll
    for (int i = 0; i < 2; ++i) {
      const int e = t + i * 256, r = e >> 4, c = (e & 15) * 8;
      kpre[i] = *(const u32x4*)(p.Kx + (size_t)((s + 1) * SEG + r) * HD + h * DH + c);
      vpre[i] = *(const u32x4*)(p.Vx + (size_t)((s + 1) * SEG + r) * HD + h * DH + c);
    }
    if (w == 0) {
      u32 v[16];
      const u32 gbase = (u32)(((par * 256 + R) * NH + 8 * fq) * 32 + fr * 2);
      if (!sweep2<8>(rs_part, gbase * 8, 32 * 8, epoch, v, p.tmo)) return;
      float f0 = 0.f, f1 = 0.f;
#pragma unroll
      for (int k = 0; k < 8; ++k) { f0 += __uint_as_float(v[2 * k]); f1 += __uint_as_float(v[2 * k + 1]); }
      f0 = xrow_sum(f0); f1 = xrow_sum(f1);
      const u32 pk = cvtpk(f0, f1);
#pragma unroll
      for (int q = 0; q < NREP / 4; ++q) st_gran(p.memg + (size_t)(par * NREP + q * 4 + fq) * NG_MEM + R * 16 + fr, epoch, pk);
      STAMP(5);
    }
    __syncthreads();
  }
}

__global__ __launch_bounds__(128) void attn_x_kernel(const u16* __restrict__ Q, const u16* __restrict__ Kx, const u16* __restrict__ Vx,
                                                     const u16* __restrict__ KM, const u16* __restrict__ VM, u16* __restrict__ O) {
  __shared__ float qs[32][129];
  __shared__ float ks[40][129];
  __shared__ float vs[40][128];
  __shared__ float ps[32][41];
  const int s = blockIdx.x >> 5, h = blockIdx.x & 31, t = threadIdx.x;
  for (int e = t; e < 32 * 128; e += 128) {
    const int r = e >> 7, c = e & 127;
    qs[r][c] = bf2f(Q[(size_t)(s * SEG + r) * HD + h * DH + c]);
    ks[8 + r][c] = bf2f(Kx[(size_t)(s * SEG + r) * HD + h * DH + c]);
    vs[8 + r][c] = bf2f(Vx[(size_t)(s * SEG + r) * HD + h * DH + c]);
  }
  for (int e = t; e < 8 * 128; e += 128) {
    const int r = e >> 7, c = e & 127;
    ks[r][c] = bf2f(KM[(size_t)(s * MM + r) * HD + h * DH + c]);
    vs[r][c] = bf2f(VM[(size_t)(s * MM + r) * HD + h * DH + c]);
  }
  __syncthreads();
  const float scale = 0.08838834764831845f;
  for (int e = t; e < 32 * 40; e += 128) {
    const int qi = e / 40, ki = e % 40;
    float d = 0.f;
    if (ki < 8 + qi + 1) {
#pragma unroll 8
      for (int c = 0; c < 128; ++c) d += qs[qi][c] * ks[ki][c];
      d = rbf(rbf(d) * scale);
    } else d = -1e9f;
    ps[qi][ki] = d;
  }
  __syncthreads();
  if (t < 32) {
    float mx = -3e38f;
    for (int k = 0; k < 40; ++k) mx = fmaxf(mx, ps[t][k]);
    float sm = 0.f;
    for (int k = 0; k < 40; ++k) { const float e = __expf(ps[t][k] - mx); ps[t][k] = e; sm += e; }
    const float inv = 1.0f / sm;
    for (int k = 0; k < 40; ++k) ps[t][k] = rbf(ps[t][k] * inv);
  }
  __syncthreads();
  for (int qi = 0; qi < 32; ++qi) {
    float a = 0.f;
#pragma unroll 8
    for (int k = 0; k < 40; ++k) a += ps[qi][k] * vs[k][t];
    O[(size_t)(s * SEG + qi) * HD + h * DH + t] = f2bf(a);
  }
}

extern "C" void kernel_launch(void* const* d_in, const int* in_sizes, int n_in,
                              void* d_out, int out_size, void* d_ws, size_t ws_size,
                              hipStream_t stream) {
  const u16* x = (const u16*)d_in[0];
  const u16* mem0 = (const u16*)d_in[1];
  const u16* Wq = (const u16*)d_in[2];
  const u16* Wk = (const u16*)d_in[3];
  const u16* Wv = (const u16*)d_in[4];
  const u16* Wo = (const u16*)d_in[5];
  const u16* w1 = (const u16*)d_in[8];
  const u16* b1 = (const u16*)d_in[9];
  const u16* w2 = (const u16*)d_in[10];
  const u16* b2 = (const u16*)d_in[11];
  u16* out = (u16*)d_out;

  char* ws = (char*)d_ws;
  size_t off = 0;
  auto carve = [&](size_t bytes) { char* q = ws + off; off += (bytes + 255) & ~(size_t)255; return q; };
  u64* memg = (u64*)carve((size_t)2 * NREP * NG_MEM * 8);
  u64* qkvg = (u64*)carve((size_t)2 * NH * 8 * NG_QKV * 8);
  u64* partg = (u64*)carve((size_t)2 * 256 * NH * 32 * 8);
  u32* tmo = (u32*)carve(256);
  const size_t sync_bytes = off;
  u16* xn = (u16*)carve((size_t)S_ * DM * 2);
  u16* Qx = (u16*)carve((size_t)S_ * HD * 2);
  u16* Kx = (u16*)carve((size_t)S_ * HD * 2);
  u16* Vx = (u16*)carve((size_t)S_ * HD * 2);
  u16* Hb = (u16*)carve((size_t)S_ * FF * 2);
  u16* Ox = (u16*)carve((size_t)S_ * HD * 2);
  u16* KM = (u16*)carve((size_t)NSEG * MM * HD * 2);
  u16* VM = (u16*)carve((size_t)NSEG * MM * HD * 2);
  u16* Bm = (u16*)carve((size_t)S_ * DM * 2);

  hipMemsetAsync(ws, 0, sync_bytes, stream);
  ln_kernel<<<S_, 256, 0, stream>>>(x, xn);
  {
    GemmP g{xn, Wq, Wk, Wv, w1, Qx, Kx, Vx, Hb, b1, nullptr, nullptr};
    gemm_kernel<0><<<dim3(124, 16), 256, 0, stream>>>(g);
  }
  {
    ChainP c{mem0, Wq, Wk, Wv, Wo, Kx, Vx, KM, VM, memg, qkvg, partg, tmo};
    chain_kernel<<<256, 256, 0, stream>>>(c);
  }
  attn_x_kernel<<<NSEG * NH, 128, 0, stream>>>(Qx, Kx, Vx, KM, VM, Ox);
  {
    GemmP g{Hb, w2, nullptr, nullptr, nullptr, Bm, nullptr, nullptr, nullptr, b2, nullptr, nullptr};
    gemm_kernel<1><<<dim3(8, 16), 256, 0, stream>>>(g);
  }
  {
    GemmP g{Ox, Wo, nullptr, nullptr, nullptr, out, nullptr, nullptr, nullptr, nullptr, x, Bm};
    gemm_kernel<2><<<dim3(8, 16), 256, 0, stream>>>(g);
  }
}
```

```cpp
#include <hip/hip_runtime.h>
#include <hip/hip_bf16.h>
#include <stdint.h>

typedef unsigned short u16;
typedef unsigned int u32;
typedef unsigned long long u64;
#define GAS __attribute__((address_space(1)))
typedef GAS u64 gu64;
typedef GAS u32 gu32;
typedef __attribute__((ext_vector_type(8))) short bf16x8;
typedef __attribute__((ext_vector_type(4))) float f32x4;
typedef __attribute__((ext_vector_type(4))) unsigned int u32x4;
#define RLX_AGENT __ATOMIC_RELAXED, __HIP_MEMORY_SCOPE_AGENT

constexpr int S_ = 2048, DM = 1024, NH = 32, DH = 128, HD = 4096, MM = 8, SEG = 32, NSEG = 64, FF = 3584;

__device__ __forceinline__ float bf2f(u16 v) { return __uint_as_float(((u32)v) << 16); }
__device__ __forceinline__ u16 f2bf(float f) { u32 u = __float_as_uint(f); u += 0x7fffu + ((u >> 16) & 1u); return (u16)(u >> 16); }
__device__ __forceinline__ float rbf(float f) { return bf2f(f2bf(f)); }
typedef __bf16 hbf2 __attribute__((ext_vector_type(2)));
typedef float f32x2 __attribute__((ext_vector_type(2)));
__device__ __forceinline__ u32 cvtpk(float a, float b) { f32x2 v = {a, b}; return __builtin_bit_cast(u32, __builtin_convertvector(v, hbf2)); }
__device__ __forceinline__ float lo2f(u32 v) { return __uint_as_float(v << 16); }
__device__ __forceinline__ float hi2f(u32 v) { return __uint_as_float(v & 0xffff0000u); }
typedef __attribute__((ext_vector_type(4))) unsigned int u32x4b;

__global__ __launch_bounds__(256) void ln_kernel(const u16* __restrict__ x, u16* __restrict__ xn) {
  __shared__ float red[8];
  const int row = blockIdx.x, t = threadIdx.x, lane = t & 63, w = t >> 6;
  const ushort4 v = *(const ushort4*)(x + (size_t)row * DM + t * 4);
  float f0 = bf2f(v.x), f1 = bf2f(v.y), f2 = bf2f(v.z), f3 = bf2f(v.w);
  float s = (f0 + f1) + (f2 + f3);
#pragma unroll
  for (int m = 32; m >= 1; m >>= 1) s += __shfl_xor(s, m);
  if (lane == 0) red[w] = s;
  __syncthreads();
  const float mean = ((red[0] + red[1]) + (red[2] + red[3])) * (1.0f / DM);
  float d0 = f0 - mean, d1 = f1 - mean, d2 = f2 - mean, d3 = f3 - mean;
  float q = (d0 * d0 + d1 * d1) + (d2 * d2 + d3 * d3);
#pragma unroll
  for (int m = 32; m >= 1; m >>= 1) q += __shfl_xor(q, m);
  if (lane == 0) red[4 + w] = q;
  __syncthreads();
  const float var = ((red[4] + red[5]) + (red[6] + red[7])) * (1.0f / DM);
  const float mb = rbf(mean), vb = rbf(var);
  const float den = rbf(sqrtf(rbf(vb + 1e-5f)));
  ushort4 o;
  o.x = f2bf(rbf(f0 - mb) / den); o.y = f2bf(rbf(f1 - mb) / den);
  o.z = f2bf(rbf(f2 - mb) / den); o.w = f2bf(rbf(f3 - mb) / den);
  *(ushort4*)(xn + (size_t)row * DM + t * 4) = o;
}

__device__ __forceinline__ float gelu_tanh(float x) {
  const float k0 = 0.7978845608028654f, k1 = 0.044715f;
  const float u = k0 * (x + k1 * x * x * x);
  return 0.5f * x * (1.0f + tanhf(u));
}

struct GemmP {
  const u16* A0; const u16* A1; const u16* B0; const u16* B1; const u16* B2; const u16* B3;
  u16* C0; u16* C1; u16* C2; u16* C3;
  const u16* bias;
};
#define LAS __attribute__((address_space(3)))
typedef __attribute__((ext_vector_type(4))) short s16x4;

constexpr int gemm_lds(int BM, int BN, int WM, int WN) {
  const int st = 2 * (BM * 64 * 2 + 64 * BN * 2), ep = WM * WN * (BM / WM) * (BN / WN + 8) * 2;
  return st > ep ? st : ep;
}
template <int BM, int BN, int WM, int WN, int MODE>
__global__ __launch_bounds__(WM * WN * 64) void gemm_kernel(GemmP p) {
  constexpr int NW = WM * WN, NT = NW * 64, BK = 64;
  constexpr int TM = BM / WM / 16, TN = BN / WN / 16;
  constexpr int A_BYTES = BM * BK * 2, B_BYTES = BK * BN * 2, STAGE = A_BYTES + B_BYTES;
  constexpr int NSUB = BN / 16;
  extern __shared__ __attribute__((aligned(16))) char smem[];
  const int t = threadIdx.x, lane = t & 63, w = __builtin_amdgcn_readfirstlane(t >> 6);
  const int wr = w / WN, wc = w % WN, fr = lane & 15, fq = lane >> 4;

  const int nbx = gridDim.x, nby = gridDim.y, nwg = nbx * nby;
  int id = blockIdx.y * nbx + blockIdx.x;
  { const int q = nwg / 8, r = nwg % 8, x = id % 8, o = id / 8; id = (x < r ? x * (q + 1) : r * (q + 1) + (x - r) * q) + o; }
  const int mt = id / nbx, nt = id % nbx;

  const u16* A; const u16* B; u16* C; int lda, ldb, ldc, K, ncol0; bool act = false;
  if (MODE == 0) {
    constexpr int PER = HD / BN;
    A = p.A0; lda = DM; K = DM;
    if (nt < PER) { B = p.B0; C = p.C0; ldb = HD; ncol0 = nt * BN; }
    else if (nt < 2 * PER) { B = p.B1; C = p.C1; ldb = HD; ncol0 = (nt - PER) * BN; }
    else if (nt < 3 * PER) { B = p.B2; C = p.C2; ldb = HD; ncol0 = (nt - 2 * PER) * BN; }
    else { B = p.B3; C = p.C3; ldb = FF; ncol0 = (nt - 3 * PER) * BN; act = true; }
    ldc = ldb;
  } else {
    if (blockIdx.z == 0) { A = p.A0; lda = HD; K = HD; B = p.B0; C = p.C0; }
    else { A = p.A1; lda = FF; K = FF; B = p.B1; C = p.C1; act = true; }
    ldb = DM; ldc = DM; ncol0 = nt * BN;
  }
  const int m0 = mt * BM;

  constexpr int AU = BM * 8 / NT, BU = BK * BN / 8 / NT;
  const u16* asrc[AU]; const u16* bsrc[BU];
#pragma unroll
  for (int i = 0; i < AU; ++i) {
    const int u = i * NT + t, row = u >> 3, c = (u & 7) ^ (row & 7);
    asrc[i] = A + (size_t)(m0 + row) * lda + c * 8;
  }
#pragma unroll
  for (int i = 0; i < BU; ++i) {
    const int u = i * NT + t, hf = u & 1, rp = (u >> 1) & 7, st = u >> 4, kb = st / NSUB, nb = st % NSUB;
    const int r = rp ^ (4 * (kb & 1));
    bsrc[i] = B + (size_t)(kb * 8 + r) * ldb + ncol0 + nb * 16 + hf * 8;
  }
  auto stage = [&](int kt, int buf) {
    char* base = smem + buf * STAGE;
#pragma unroll
    for (int i = 0; i < AU; ++i)
      __builtin_amdgcn_global_load_lds((const GAS u32*)(asrc[i] + kt * BK), (LAS u32*)(base + (i * NT + t) * 16), 16, 0, 0);
#pragma unroll
    for (int i = 0; i < BU; ++i)
      __builtin_amdgcn_global_load_lds((const GAS u32*)(bsrc[i] + (size_t)kt * BK * ldb), (LAS u32*)(base + A_BYTES + (i * NT + t) * 16), 16, 0, 0);
  };

  f32x4 acc[TM][TN];
#pragma unroll
  for (int i = 0; i < TM; ++i)
#pragma unroll
    for (int j = 0; j < TN; ++j) acc[i][j] = (f32x4){0.f, 0.f, 0.f, 0.f};

  int aoff[TM];
#pragma unroll
  for (int i = 0; i < TM; ++i) { const int row = wr * (BM / WM) + i * 16 + fr; aoff[i] = row * 128; }
  const int arx = fr & 7;
  const int tq = (lane & 15) >> 2, tp = lane & 3;

  const int nk = K / BK;
  stage(0, 0);
  for (int kt = 0; kt < nk; ++kt) {
    const int buf = kt & 1;
    asm volatile("s_waitcnt vmcnt(0)" ::: "memory");
    __syncthreads();
    if (kt + 1 < nk) stage(kt + 1, buf ^ 1);
    const char* sa = smem + buf * STAGE;
    const char* sb = sa + A_BYTES;
#pragma unroll
    for (int ks = 0; ks < 2; ++ks) {
      bf16x8 af[TM], bfr[TN];
#pragma unroll
      for (int i = 0; i < TM; ++i) af[i] = *(const bf16x8*)(sa + aoff[i] + (((4 * ks + fq) ^ arx) * 16));
      const int kb = 4 * ks + fq;
      const int rsw = 4 * (kb & 1);
#pragma unroll
      for (int j = 0; j < TN; ++j) {
        const int nb = wc * TN + j;
        const char* sub = sb + (kb * NSUB + nb) * 256 + tp * 8;
        const s16x4 lo = __builtin_amdgcn_ds_read_tr16_b64_v4i16((LAS s16x4*)(sub + ((tq) ^ rsw) * 32));
        const s16x4 hi = __builtin_amdgcn_ds_read_tr16_b64_v4i16((LAS s16x4*)(sub + ((4 + tq) ^ rsw) * 32));
        bfr[j] = (bf16x8){lo[0], lo[1], lo[2], lo[3], hi[0], hi[1], hi[2], hi[3]};
      }
#pragma unroll
      for (int i = 0; i < TM; ++i)
#pragma unroll
        for (int j = 0; j < TN; ++j) acc[i][j] = __builtin_amdgcn_mfma_f32_16x16x32_bf16(bfr[j], af[i], acc[i][j], 0, 0, 0);
    }
  }
  __syncthreads();
  constexpr int WROWS = BM / WM, WCOLS = BN / WN, LDW = WCOLS + 8;
  static_assert(NW * WROWS * LDW * 2 <= gemm_lds(BM, BN, WM, WN), "epilogue staging");
  u16* ew = (u16*)smem + w * WROWS * LDW;
#pragma unroll
  for (int j = 0; j < TN; ++j) {
    float bv[4] = {0.f, 0.f, 0.f, 0.f};
    if (act) {
      const ushort4 bb = *(const ushort4*)(p.bias + ncol0 + wc * WCOLS + j * 16 + fq * 4);
      bv[0] = bf2f(bb.x); bv[1] = bf2f(bb.y); bv[2] = bf2f(bb.z); bv[3] = bf2f(bb.w);
    }
#pragma unroll
    for (int i = 0; i < TM; ++i) {
      float v[4];
#pragma unroll
      for (int r = 0; r < 4; ++r) v[r] = acc[i][j][r];
      if (act) {
        const u32 a01 = cvtpk(v[0], v[1]), a23 = cvtpk(v[2], v[3]);
        v[0] = lo2f(a01) + bv[0]; v[1] = hi2f(a01) + bv[1]; v[2] = lo2f(a23) + bv[2]; v[3] = hi2f(a23) + bv[3];
        if (MODE == 0) {
          const u32 b01 = cvtpk(v[0], v[1]), b23 = cvtpk(v[2], v[3]);
          v[0] = gelu_tanh(lo2f(b01)); v[1] = gelu_tanh(hi2f(b01)); v[2] = gelu_tanh(lo2f(b23)); v[3] = gelu_tanh(hi2f(b23));
        }
      }
      *(uint2*)(&ew[(i * 16 + fr) * LDW + j * 16 + fq * 4]) = make_uint2(cvtpk(v[0], v[1]), cvtpk(v[2], v[3]));
    }
  }
  asm volatile("s_waitcnt lgkmcnt(0)" ::: "memory");
  __builtin_amdgcn_wave_barrier();
  constexpr int CPR = WCOLS / 8;
#pragma unroll
  for (int e = lane; e < WROWS * CPR; e += 64) {
    const int r = e / CPR, c = e % CPR;
    const u32x4 v = *(const u32x4*)(&ew[r * LDW + c * 8]);
    *(u32x4*)(C + (size_t)(m0 + wr * WROWS + r) * ldc + ncol0 + wc * WCOLS + c * 8) = v;
  }
}

__global__ __launch_bounds__(256) void combine_kernel(const u16* __restrict__ x, const u16* __restrict__ a, const u16* __restrict__ bm, u16* __restrict__ out) {
  const size_t i = ((size_t)blockIdx.x * 256 + threadIdx.x) * 8;
  const u32x4 xv = *(const u32x4*)(x + i), av = *(const u32x4*)(a + i), bv = *(const u32x4*)(bm + i);
  u32x4 o;
#pragma unroll
  for (int q = 0; q < 4; ++q) {
    const u32 s1 = cvtpk(lo2f(xv[q]) + lo2f(av[q]), hi2f(xv[q]) + hi2f(av[q]));
    o[q] = cvtpk(lo2f(s1) + lo2f(bv[q]), hi2f(s1) + hi2f(bv[q]));
  }
  *(u32x4*)(out + i) = o;
}

struct ChainP {
  const u16* mem0; const u16* Wq; const u16* Wk; const u16* Wv; const u16* Wo;
  const u16* Kx; const u16* Vx; u16* KM; u16* VM;
  u64* memg; u64* qkvg; u64* partg; u32* tmo;
};
constexpr int NG_MEM = 4096;
constexpr int NG_QKV = 192;
constexpr u32 SPIN_CAP = 400000u;
#define H1_DELAY 16
constexpr int NREP = 8;

__device__ __forceinline__ void st_gran(u64* g, u32 tag, u32 val) {
  __hip_atomic_store((gu64*)g, ((u64)tag << 32) | (u64)val, RLX_AGENT);
}
__device__ __forceinline__ u64 ld_gran(const u64* g) { return __hip_atomic_load((gu64*)g, RLX_AGENT); }
__device__ __forceinline__ void st_gran2(u64* g, u32 tag, u32 v0, u32 v1) {
  const u32x4 d = {v0, tag, v1, tag};
  asm volatile("global_store_dwordx4 %0, %1, off sc1\n\ts_nop 1" :: "v"((gu64*)g), "v"(d) : "memory");
}

__device__ __forceinline__ bool spin_fail(u32& spins, u32* tmo) {
  ++spins;
  if (spins > SPIN_CAP) { __hip_atomic_store((gu32*)tmo, 1u, RLX_AGENT); return true; }
  if ((spins & 1023u) == 0u) { if (__builtin_amdgcn_readfirstlane(__hip_atomic_load((gu32*)tmo, RLX_AGENT))) return true; }
  return false;
}
__device__ __forceinline__ bool wait_sentinel(const u64* g, bool active, u32 tag, u32* tmo) {
  for (u32 spins = 0;;) {
    bool ok = true;
    if (active) ok = ((u32)(ld_gran(g) >> 32) == tag);
    if (__all(ok)) return true;
    if (spin_fail(spins, tmo)) return false;
    __builtin_amdgcn_s_sleep(1);
  }
}
template <int N>
__device__ __forceinline__ bool sweep(const u64* g, int stride, u32 tag, u32 (&v)[N], u32* tmo) {
  for (u32 spins = 0;;) {
    bool ok = true;
#pragma unroll
    for (int k = 0; k < N; ++k) { const u64 x = ld_gran(g + (size_t)k * stride); v[k] = (u32)x; ok &= ((u32)(x >> 32) == tag); }
    if (__all(ok)) return true;
    if (spin_fail(spins, tmo)) return false;
  }
}
template <int N>
__device__ __forceinline__ bool sweep2(__amdgpu_buffer_rsrc_t rs, u32 byte_off, u32 byte_stride, u32 tag, u32 (&v)[2 * N], u32* tmo) {
  for (u32 spins = 0;;) {
    bool ok = true;
#pragma unroll
    for (int k = 0; k < N; ++k) {
      const u32x4 x = __builtin_bit_cast(u32x4, __builtin_amdgcn_raw_buffer_load_b128(rs, byte_off + k * byte_stride, 0, 16));
      v[2 * k] = x[0]; v[2 * k + 1] = x[2]; ok &= (x[1] == tag) & (x[3] == tag);
    }
    if (__all(ok)) return true;
    if (spin_fail(spins, tmo)) return false;
  }
}

#define STAMP(i) do { } while (0)
__device__ __forceinline__ float xrow_max(float v) {
  auto a = __builtin_amdgcn_permlane16_swap(__float_as_uint(v), __float_as_uint(v), false, false);
  v = fmaxf(__uint_as_float(a[0]), __uint_as_float(a[1]));
  auto c = __builtin_amdgcn_permlane32_swap(__float_as_uint(v), __float_as_uint(v), false, false);
  return fmaxf(__uint_as_float(c[0]), __uint_as_float(c[1]));
}
__device__ __forceinline__ float xrow_sum(float v) {
  auto a = __builtin_amdgcn_permlane16_swap(__float_as_uint(v), __float_as_uint(v), false, false);
  v = __uint_as_float(a[0]) + __uint_as_float(a[1]);
  auto c = __builtin_amdgcn_permlane32_swap(__float_as_uint(v), __float_as_uint(v), false, false);
  return __uint_as_float(c[0]) + __uint_as_float(c[1]);
}

__global__ __launch_bounds__(256, 1) void chain_kernel(ChainP p) {
  constexpr int LM = 1032;
  constexpr int LQ = 136;
  constexpr int LV = 72;
  __shared__ __attribute__((aligned(16))) u16 memS[8 * LM];
  __shared__ __attribute__((aligned(16))) float red[4 * 8 * 48];
  __shared__ __attribute__((aligned(16))) u16 qS[8 * LQ];
  __shared__ __attribute__((aligned(16))) u16 kS[48 * LQ];
  __shared__ __attribute__((aligned(16))) u16 vT[128 * LV];

  const int t = threadIdx.x, lane = t & 63, w = __builtin_amdgcn_readfirstlane(t >> 6);
  const int b = blockIdx.x, xcd = b & 7, ib = b >> 3;
  const int h = xcd * 4 + (ib >> 3), j = ib & 7;
  const int R = h * 8 + j;
  const int fr = lane & 15, fq = lane >> 4;
  const __amdgpu_buffer_rsrc_t rs_mem = __builtin_amdgcn_make_buffer_rsrc((void*)p.memg, 0, 2 * NREP * NG_MEM * 8, 0x00020000);
  const __amdgpu_buffer_rsrc_t rs_qkv = __builtin_amdgcn_make_buffer_rsrc((void*)p.qkvg, 0, 2 * NH * 8 * NG_QKV * 8, 0x00020000);
  const __amdgpu_buffer_rsrc_t rs_part = __builtin_amdgcn_make_buffer_rsrc((void*)p.partg, 0, 2 * 256 * NH * 32 * 8, 0x00020000);

  bf16x8 wf[3][8];
  {
    const u16* Ws[3] = {p.Wq, p.Wk, p.Wv};
#pragma unroll
    for (int m = 0; m < 3; ++m)
#pragma unroll
      for (int s = 0; s < 8; ++s) {
        const u16* src = Ws[m] + (size_t)(256 * w + 32 * s + 8 * fq) * HD + h * DH + 16 * j + fr;
#pragma unroll
        for (int i = 0; i < 8; ++i) wf[m][s][i] = (short)src[(size_t)i * HD];
      }
  }
  bf16x8 wo[2][4];
#pragma unroll
  for (int c = 0; c < 2; ++c)
#pragma unroll
    for (int s = 0; s < 4; ++s)
#pragma unroll
      for (int i = 0; i < 8; ++i) {
        const int d = 32 * s + ((i < 4) ? (4 * fq + i) : (16 + 4 * fq + i - 4));
        wo[c][s][i] = (short)p.Wo[(size_t)(h * DH + d) * DM + 128 * j + 16 * (2 * w + c) + fr];
      }
  for (int e = t; e < 128 * LV; e += 256) vT[e] = 0;
  for (int e = t; e < 48 * LQ; e += 256) kS[e] = 0;
  u32x4 kpre[2], vpre[2];
#pragma unroll
  for (int i = 0; i < 2; ++i) {
    const int e = t + i * 256, r = e >> 4, c = (e & 15) * 8;
    kpre[i] = *(const u32x4*)(p.Kx + (size_t)r * HD + h * DH + c);
    vpre[i] = *(const u32x4*)(p.Vx + (size_t)r * HD + h * DH + c);
  }
  __syncthreads();

  for (int s = 0; s < NSEG; ++s) {
    const int par = s & 1;
    const u32 epoch = (u32)s + 1u;
    STAMP(0);
    if (s == 0) {
#pragma unroll
      for (int i = 0; i < 4; ++i) {
        const int e = t + i * 256, r = e >> 7, c = (e & 127) * 8;
        *(u32x4*)(&memS[r * LM + c]) = *(const u32x4*)(p.mem0 + r * DM + c);
      }
    } else {
      u32 v[16];
      const u32 gbase = (u32)(((par ^ 1) * NREP + (b & (NREP - 1))) * NG_MEM + w * 1024);
      __builtin_amdgcn_s_sleep(H1_DELAY);
      if (!sweep2<8>(rs_mem, (gbase + lane * 2) * 8, 128 * 8, (u32)s, v, p.tmo)) return;
#pragma unroll
      for (int k = 0; k < 8; ++k) {
        const int u = lane + k * 64;
        *(uint2*)(&memS[(u & 7) * LM + (w * 64 + (u >> 3)) * 4]) = make_uint2(v[2 * k], v[2 * k + 1]);
      }
    }
#pragma unroll
    for (int i = 0; i < 2; ++i) {
      const int e = t + i * 256, r = e >> 4, c = (e & 15) * 8;
      *(u32x4*)(&kS[r * LQ + c]) = kpre[i];
      const u16* vp = (const u16*)&vpre[i];
      const int pos = (r < 16) ? (8 * (r >> 2) + (r & 3)) : (8 * ((r - 16) >> 2) + 4 + (r & 3));
#pragma unroll
      for (int q = 0; q < 8; ++q) vT[(c + q) * LV + pos] = vp[q];
    }
    __syncthreads();
    STAMP(1);
    {
      f32x4 acc[3] = {{0.f, 0.f, 0.f, 0.f}, {0.f, 0.f, 0.f, 0.f}, {0.f, 0.f, 0.f, 0.f}};
      bf16x8 a[8];
#pragma unroll
      for (int ks = 0; ks < 8; ++ks) a[ks] = *(const bf16x8*)(&memS[(lane & 7) * LM + 256 * w + 32 * ks + 8 * fq]);
#pragma unroll
      for (int ks = 0; ks < 8; ++ks)
#pragma unroll
        for (int m = 0; m < 3; ++m) acc[m] = __builtin_amdgcn_mfma_f32_16x16x32_bf16(a[ks], wf[m][ks], acc[m], 0, 0, 0);
      if (fq < 2) {
#pragma unroll
        for (int m = 0; m < 3; ++m)
#pragma unroll
          for (int r = 0; r < 4; ++r) red[(w * 8 + fq * 4 + r) * 48 + m * 16 + fr] = acc[m][r];
      }
    }
    __syncthreads();
    if (t < NG_QKV) {
      const int r = t / 24, cp = t % 24;
      float v0 = 0.f, v1 = 0.f;
#pragma unroll
      for (int ww = 0; ww < 4; ++ww) { const float2 q2 = *(const float2*)(&red[(ww * 8 + r) * 48 + cp * 2]); v0 += q2.x; v1 += q2.y; }
      const u32 pk = cvtpk(v0, v1);
      const int m = cp >> 3, c = (cp & 7) * 2;
      if (s < NSEG - 1) st_gran(p.qkvg + ((size_t)(par * NH + h) * 8 + j) * NG_QKV + t, epoch, pk);
      if (m == 1) *(u32*)(p.KM + (size_t)(s * MM + r) * HD + h * DH + 16 * j + c) = pk;
      if (m == 2) *(u32*)(p.VM + (size_t)(s * MM + r) * HD + h * DH + 16 * j + c) = pk;
    }
    STAMP(2);
    if (s == NSEG - 1) break;
    bf16x8 kx[2][4], vx[8];
#pragma unroll
    for (int ks = 0; ks < 4; ++ks)
#pragma unroll
      for (int n = 0; n < 2; ++n) kx[n][ks] = *(const bf16x8*)(&kS[(n * 16 + fr) * LQ + 32 * ks + 8 * fq]);
#pragma unroll
    for (int c = 0; c < 8; ++c) vx[c] = *(const bf16x8*)(&vT[(16 * c + fr) * LV + 8 * fq]);
    __builtin_amdgcn_sched_barrier(0);
    {
      u32 v[6];
      const u32 gbase = (u32)(((par * NH + h) * 8 + 2 * w) * NG_QKV);
      if (!sweep2<3>(rs_qkv, (gbase + lane * 2) * 8, 128 * 8, epoch, v, p.tmo)) return;
#pragma unroll
      for (int k = 0; k < 3; ++k) {
        const int g = (lane + k * 64) * 2, jj = 2 * w + g / NG_QKV, e = g % NG_QKV;
        const int r = e / 24, cp = e % 24, m = cp >> 3, c = 16 * jj + (cp & 7) * 2;
        if (m == 0) *(uint2*)(&qS[r * LQ + c]) = make_uint2(v[2 * k], v[2 * k + 1]);
        else if (m == 1) *(uint2*)(&kS[(32 + r) * LQ + c]) = make_uint2(v[2 * k], v[2 * k + 1]);
        else {
          const int pos = 32 + 8 * (r >> 2) + (r & 3);
          vT[c * LV + pos] = (u16)(v[2 * k] & 0xffffu); vT[(c + 1) * LV + pos] = (u16)(v[2 * k] >> 16);
          vT[(c + 2) * LV + pos] = (u16)(v[2 * k + 1] & 0xffffu); vT[(c + 3) * LV + pos] = (u16)(v[2 * k + 1] >> 16);
        }
      }
    }
    __syncthreads();
    STAMP(3);
    {
      f32x4 sc[3] = {{0.f, 0.f, 0.f, 0.f}, {0.f, 0.f, 0.f, 0.f}, {0.f, 0.f, 0.f, 0.f}};
      bf16x8 qf[4], km[4];
      uint2 vm[8];
#pragma unroll
      for (int ks = 0; ks < 4; ++ks) {
        qf[ks] = *(const bf16x8*)(&qS[(lane & 7) * LQ + 32 * ks + 8 * fq]);
        km[ks] = *(const bf16x8*)(&kS[(32 + fr) * LQ + 32 * ks + 8 * fq]);
      }
#pragma unroll
      for (int c = 0; c < 8; ++c) vm[c] = *(const uint2*)(&vT[(16 * c + fr) * LV + 32 + 8 * fq]);
      __builtin_amdgcn_sched_barrier(0);
#pragma unroll
      for (int ks = 0; ks < 4; ++ks) {
        sc[0] = __builtin_amdgcn_mfma_f32_16x16x32_bf16(kx[0][ks], qf[ks], sc[0], 0, 0, 0);
        sc[1] = __builtin_amdgcn_mfma_f32_16x16x32_bf16(kx[1][ks], qf[ks], sc[1], 0, 0, 0);
        sc[2] = __builtin_amdgcn_mfma_f32_16x16x32_bf16(km[ks], qf[ks], sc[2], 0, 0, 0);
      }
      const float scale = 0.08838834764831845f;
      const bool live2 = fq < 2;
      float xs[3][4];
#pragma unroll
      for (int n = 0; n < 3; ++n)
#pragma unroll
        for (int r = 0; r < 4; r += 2) {
          const u32 a = cvtpk(sc[n][r], sc[n][r + 1]);
          const u32 c2 = cvtpk(lo2f(a) * scale, hi2f(a) * scale);
          xs[n][r] = lo2f(c2); xs[n][r + 1] = hi2f(c2);
        }
      if (!live2) { xs[2][0] = xs[2][1] = xs[2][2] = xs[2][3] = -3.0e38f; }
      float mx = fmaxf(fmaxf(fmaxf(xs[0][0], xs[0][1]), fmaxf(xs[0][2], xs[0][3])), fmaxf(fmaxf(xs[1][0], xs[1][1]), fmaxf(xs[1][2], xs[1][3])));
      mx = fmaxf(mx, fmaxf(fmaxf(xs[2][0], xs[2][1]), fmaxf(xs[2][2], xs[2][3])));
      mx = xrow_max(mx);
      float sm = 0.f;
#pragma unroll
      for (int n = 0; n < 3; ++n)
#pragma unroll
        for (int r = 0; r < 4; ++r) { xs[n][r] = __expf(xs[n][r] - mx); sm += xs[n][r]; }
      sm = xrow_sum(sm);
      const float inv = 1.0f / sm;
      u32x4b pw0, pw1;
      pw0[0] = cvtpk(xs[0][0] * inv, xs[0][1] * inv); pw0[1] = cvtpk(xs[0][2] * inv, xs[0][3] * inv);
      pw0[2] = cvtpk(xs[1][0] * inv, xs[1][1] * inv); pw0[3] = cvtpk(xs[1][2] * inv, xs[1][3] * inv);
      pw1[0] = cvtpk(xs[2][0] * inv, xs[2][1] * inv); pw1[1] = cvtpk(xs[2][2] * inv, xs[2][3] * inv);
      pw1[2] = 0u; pw1[3] = 0u;
      const bf16x8 pf0 = __builtin_bit_cast(bf16x8, pw0), pf1 = __builtin_bit_cast(bf16x8, pw1);
      u32x4b ow[4];
#pragma unroll
      for (int c = 0; c < 8; ++c) {
        const u32x4b vmw = {vm[c].x, vm[c].y, 0u, 0u};
        f32x4 oc = {0.f, 0.f, 0.f, 0.f};
        oc = __builtin_amdgcn_mfma_f32_16x16x32_bf16(vx[c], pf0, oc, 0, 0, 0);
        oc = __builtin_amdgcn_mfma_f32_16x16x32_bf16(__builtin_bit_cast(bf16x8, vmw), pf1, oc, 0, 0, 0);
        ow[c >> 1][(c & 1) * 2] = cvtpk(oc[0], oc[1]); ow[c >> 1][(c & 1) * 2 + 1] = cvtpk(oc[2], oc[3]);
      }
      f32x4 pc[2] = {{0.f, 0.f, 0.f, 0.f}, {0.f, 0.f, 0.f, 0.f}};
#pragma unroll
      for (int ks = 0; ks < 4; ++ks)
#pragma unroll
        for (int c = 0; c < 2; ++c) pc[c] = __builtin_amdgcn_mfma_f32_16x16x32_bf16(wo[c][ks], __builtin_bit_cast(bf16x8, ow[ks]), pc[c], 0, 0, 0);
      if (fr < 8) {
#pragma unroll
        for (int c = 0; c < 2; ++c) {
          u64* dst = p.partg + ((size_t)(par * 256 + 32 * j + 8 * w + 4 * c + fq) * NH + h) * 32 + fr * 4;
          st_gran2(dst, epoch, __float_as_uint(pc[c][0]), __float_as_uint(pc[c][1]));
          st_gran2(dst + 2, epoch, __float_as_uint(pc[c][2]), __float_as_uint(pc[c][3]));
        }
      }
    }
    STAMP(4);
    __syncthreads();
#pragma unroll
    for (int i = 0; i < 2; ++i) {
      const int e = t + i * 256, r = e >> 4, c = (e & 15) * 8;
      kpre[i] = *(const u32x4*)(p.Kx + (size_t)((s + 1) * SEG + r) * HD + h * DH + c);
      vpre[i] = *(const u32x4*)(p.Vx + (size_t)((s + 1) * SEG + r) * HD + h * DH + c);
    }
    if (w == 0) {
      u32 v[16];
      const u32 gbase = (u32)(((par * 256 + R) * NH + 8 * fq) * 32 + fr * 2);
      if (!sweep2<8>(rs_part, gbase * 8, 32 * 8, epoch, v, p.tmo)) return;
      float f0 = 0.f, f1 = 0.f;
#pragma unroll
      for (int k = 0; k < 8; ++k) { f0 += __uint_as_float(v[2 * k]); f1 += __uint_as_float(v[2 * k + 1]); }
      f0 = xrow_sum(f0); f1 = xrow_sum(f1);
      const u32 pk = cvtpk(f0, f1);
#pragma unroll
      for (int q = 0; q < NREP / 4; ++q) st_gran(p.memg + (size_t)(par * NREP + q * 4 + fq) * NG_MEM + R * 16 + fr, epoch, pk);
      STAMP(5);
    }
    __syncthreads();
  }
}

__global__ __launch_bounds__(128) void attn_x_kernel(const u16* __restrict__ Q, const u16* __restrict__ Kx, const u16* __restrict__ Vx,
                                                     const u16* __restrict__ KM, const u16* __restrict__ VM, u16* __restrict__ O) {
  __shared__ float qs[32][129];
  __shared__ float ks[40][129];
  __shared__ float vs[40][128];
  __shared__ float ps[32][41];
  const int s = blockIdx.x >> 5, h = blockIdx.x & 31, t = threadIdx.x;
  for (int e = t; e < 32 * 128; e += 128) {
    const int r = e >> 7, c = e & 127;
    qs[r][c] = bf2f(Q[(size_t)(s * SEG + r) * HD + h * DH + c]);
    ks[8 + r][c] = bf2f(Kx[(size_t)(s * SEG + r) * HD + h * DH + c]);
    vs[8 + r][c] = bf2f(Vx[(size_t)(s * SEG + r) * HD + h * DH + c]);
  }
  for (int e = t; e < 8 * 128; e += 128) {
    const int r = e >> 7, c = e & 127;
    ks[r][c] = bf2f(KM[(size_t)(s * MM + r) * HD + h * DH + c]);
    vs[r][c] = bf2f(VM[(size_t)(s * MM + r) * HD + h * DH + c]);
  }
  __syncthreads();
  const float scale = 0.08838834764831845f;
  for (int e = t; e < 32 * 40; e += 128) {
    const int qi = e / 40, ki = e % 40;
    float d = 0.f;
    if (ki < 8 + qi + 1) {
#pragma unroll 8
      for (int c = 0; c < 128; ++c) d += qs[qi][c] * ks[ki][c];
      d = rbf(rbf(d) * scale);
    } else d = -1e9f;
    ps[qi][ki] = d;
  }
  __syncthreads();
  if (t < 32) {
    float mx = -3e38f;
    for (int k = 0; k < 40; ++k) mx = fmaxf(mx, ps[t][k]);
    float sm = 0.f;
    for (int k = 0; k < 40; ++k) { const float e = __expf(ps[t][k] - mx); ps[t][k] = e; sm += e; }
    const float inv = 1.0f / sm;
    for (int k = 0; k < 40; ++k) ps[t][k] = rbf(ps[t][k] * inv);
  }
  __syncthreads();
  for (int qi = 0; qi < 32; ++qi) {
    float a = 0.f;
#pragma unroll 8
    for (int k = 0; k < 40; ++k) a += ps[qi][k] * vs[k][t];
    O[(size_t)(s * SEG + qi) * HD + h * DH + t] = f2bf(a);
  }
}

extern "C" void kernel_launch(void* const* d_in, const int* in_sizes, int n_in,
                              void* d_out, int out_size, void* d_ws, size_t ws_size,
                              hipStream_t stream) {
  const u16* x = (const u16*)d_in[0];
  const u16* mem0 = (const u16*)d_in[1];
  const u16* Wq = (const u16*)d_in[2];
  const u16* Wk = (const u16*)d_in[3];
  const u16* Wv = (const u16*)d_in[4];
  const u16* Wo = (const u16*)d_in[5];
  const u16* w1 = (const u16*)d_in[8];
  const u16* b1 = (const u16*)d_in[9];
  const u16* w2 = (const u16*)d_in[10];
  const u16* b2 = (const u16*)d_in[11];
  u16* out = (u16*)d_out;

  char* ws = (char*)d_ws;
  size_t off = 0;
  auto carve = [&](size_t bytes) { char* q = ws + off; off += (bytes + 255) & ~(size_t)255; return q; };
  u64* memg = (u64*)carve((size_t)2 * NREP * NG_MEM * 8);
  u64* qkvg = (u64*)carve((size_t)2 * NH * 8 * NG_QKV * 8);
  u64* partg = (u64*)carve((size_t)2 * 256 * NH * 32 * 8);
  u32* tmo = (u32*)carve(256);
  const size_t sync_bytes = off;
  u16* xn = (u16*)carve((size_t)S_ * DM * 2);
  u16* Qx = (u16*)carve((size_t)S_ * HD * 2);
  u16* Kx = (u16*)carve((size_t)S_ * HD * 2);
  u16* Vx = (u16*)carve((size_t)S_ * HD * 2);
  u16* Hb = (u16*)carve((size_t)S_ * FF * 2);
  u16* Ox = (u16*)carve((size_t)S_ * HD * 2);
  u16* KM = (u16*)carve((size_t)NSEG * MM * HD * 2);
  u16* VM = (u16*)carve((size_t)NSEG * MM * HD * 2);
  u16* Bm = (u16*)carve((size_t)S_ * DM * 2);
  u16* Am = (u16*)carve((size_t)S_ * DM * 2);

  hipMemsetAsync(ws, 0, sync_bytes, stream);
  ln_kernel<<<S_, 256, 0, stream>>>(x, xn);
  {
    GemmP g{xn, nullptr, Wq, Wk, Wv, w1, Qx, Kx, Vx, Hb, b1};
    constexpr int LDS1 = gemm_lds(256, 256, 2, 4);
    hipFuncSetAttribute((const void*)gemm_kernel<256, 256, 2, 4, 0>, hipFuncAttributeMaxDynamicSharedMemorySize, LDS1);
    gemm_kernel<256, 256, 2, 4, 0><<<dim3(62, 8), 512, LDS1, stream>>>(g);
  }
  {
    ChainP c{mem0, Wq, Wk, Wv, Wo, Kx, Vx, KM, VM, memg, qkvg, partg, tmo};
    chain_kernel<<<256, 256, 0, stream>>>(c);
  }
  attn_x_kernel<<<NSEG * NH, 128, 0, stream>>>(Qx, Kx, Vx, KM, VM, Ox);
  {
    GemmP g{Ox, Hb, Wo, w2, nullptr, nullptr, Am, Bm, nullptr, nullptr, b2};
    constexpr int LDS2 = gemm_lds(128, 128, 2, 2);
    hipFuncSetAttribute((const void*)gemm_kernel<128, 128, 2, 2, 1>, hipFuncAttributeMaxDynamicSharedMemorySize, LDS2);
    gemm_kernel<128, 128, 2, 2, 1><<<dim3(8, 16, 2), 256, LDS2, stream>>>(g);
  }
  combine_kernel<<<S_ * DM / 8 / 256, 256, 0, stream>>>(x, Am, Bm, out);
}
```

```cpp
#include <hip/hip_runtime.h>
#include <hip/hip_bf16.h>
#include <stdint.h>

typedef unsigned short u16;
typedef unsigned int u32;
typedef unsigned long long u64;
#define GAS __attribute__((address_space(1)))
typedef GAS u64 gu64;
typedef GAS u32 gu32;
typedef __attribute__((ext_vector_type(8))) short bf16x8;
typedef __attribute__((ext_vector_type(4))) float f32x4;
typedef __attribute__((ext_vector_type(4))) unsigned int u32x4;
#define RLX_AGENT __ATOMIC_RELAXED, __HIP_MEMORY_SCOPE_AGENT

constexpr int S_ = 2048, DM = 1024, NH = 32, DH = 128, HD = 4096, MM = 8, SEG = 32, NSEG = 64, FF = 3584;

__device__ __forceinline__ float bf2f(u16 v) { return __uint_as_float(((u32)v) << 16); }
__device__ __forceinline__ u16 f2bf(float f) { u32 u = __float_as_uint(f); u += 0x7fffu + ((u >> 16) & 1u); return (u16)(u >> 16); }
__device__ __forceinline__ float rbf(float f) { return bf2f(f2bf(f)); }
typedef __bf16 hbf2 __attribute__((ext_vector_type(2)));
typedef float f32x2 __attribute__((ext_vector_type(2)));
__device__ __forceinline__ u32 cvtpk(float a, float b) { f32x2 v = {a, b}; return __builtin_bit_cast(u32, __builtin_convertvector(v, hbf2)); }
__device__ __forceinline__ float lo2f(u32 v) { return __uint_as_float(v << 16); }
__device__ __forceinline__ float hi2f(u32 v) { return __uint_as_float(v & 0xffff0000u); }
typedef __attribute__((ext_vector_type(4))) unsigned int u32x4b;

__global__ __launch_bounds__(256) void ln_kernel(const u16* __restrict__ x, u16* __restrict__ xn) {
  __shared__ float red[8];
  const int row = blockIdx.x, t = threadIdx.x, lane = t & 63, w = t >> 6;
  const ushort4 v = *(const ushort4*)(x + (size_t)row * DM + t * 4);
  float f0 = bf2f(v.x), f1 = bf2f(v.y), f2 = bf2f(v.z), f3 = bf2f(v.w);
  float s = (f0 + f1) + (f2 + f3);
#pragma unroll
  for (int m = 32; m >= 1; m >>= 1) s += __shfl_xor(s, m);
  if (lane == 0) red[w] = s;
  __syncthreads();
  const float mean = ((red[0] + red[1]) + (red[2] + red[3])) * (1.0f / DM);
  float d0 = f0 - mean, d1 = f1 - mean, d2 = f2 - mean, d3 = f3 - mean;
  float q = (d0 * d0 + d1 * d1) + (d2 * d2 + d3 * d3);
#pragma unroll
  for (int m = 32; m >= 1; m >>= 1) q += __shfl_xor(q, m);
  if (lane == 0) red[4 + w] = q;
  __syncthreads();
  const float var = ((red[4] + red[5]) + (red[6] + red[7])) * (1.0f / DM);
  const float mb = rbf(mean), vb = rbf(var);
  const float den = rbf(sqrtf(rbf(vb + 1e-5f)));
  ushort4 o;
  o.x = f2bf(rbf(f0 - mb) / den); o.y = f2bf(rbf(f1 - mb) / den);
  o.z = f2bf(rbf(f2 - mb) / den); o.w = f2bf(rbf(f3 - mb) / den);
  *(ushort4*)(xn + (size_t)row * DM + t * 4) = o;
}

__device__ __forceinline__ float gelu_tanh(float x) {
  const float k0 = 0.7978845608028654f, k1 = 0.044715f;
  const float u = k0 * (x + k1 * x * x * x);
  return 0.5f * x * (1.0f + tanhf(u));
}

struct GemmP {
  const u16* A0; const u16* A1; const u16* B0; const u16* B1; const u16* B2; const u16* B3;
  u16* C0; u16* C1; u16* C2; u16* C3;
  const u16* bias;
};
#define LAS __attribute__((address_space(3)))
typedef __attribute__((ext_vector_type(4))) short s16x4;

constexpr int gemm_lds(int BM, int BN, int WM, int WN) {
  const int st = 2 * (BM * 64 * 2 + 64 * BN * 2), ep = WM * WN * (BM / WM) * (BN / WN + 8) * 2;
  return st > ep ? st : ep;
}
template <int BM, int BN, int WM, int WN, int MODE>
__global__ __launch_bounds__(WM * WN * 64) void gemm_kernel(GemmP p) {
  constexpr int NW = WM * WN, NT = NW * 64, BK = 64;
  constexpr int TM = BM / WM / 16, TN = BN / WN / 16;
  constexpr int A_BYTES = BM * BK * 2, B_BYTES = BK * BN * 2, STAGE = A_BYTES + B_BYTES;
  constexpr int NSUB = BN / 16;
  extern __shared__ __attribute__((aligned(16))) char smem[];
  const int t = threadIdx.x, lane = t & 63, w = __builtin_amdgcn_readfirstlane(t >> 6);
  const int wr = w / WN, wc = w % WN, fr = lane & 15, fq = lane >> 4;

  const int nbx = gridDim.x, nby = gridDim.y, nwg = nbx * nby;
  int id = blockIdx.y * nbx + blockIdx.x;
  { const int q = nwg / 8, r = nwg % 8, x = id % 8, o = id / 8; id = (x < r ? x * (q + 1) : r * (q + 1) + (x - r) * q) + o; }
  const int mt = id / nbx, nt = id % nbx;

  const u16* A; const u16* B; u16* C; int lda, ldb, ldc, K, ncol0; bool act = false;
  if (MODE == 0) {
    constexpr int PER = HD / BN;
    A = p.A0; lda = DM; K = DM;
    if (nt < PER) { B = p.B0; C = p.C0; ldb = HD; ncol0 = nt * BN; }
    else if (nt < 2 * PER) { B = p.B1; C = p.C1; ldb = HD; ncol0 = (nt - PER) * BN; }
    else if (nt < 3 * PER) { B = p.B2; C = p.C2; ldb = HD; ncol0 = (nt - 2 * PER) * BN; }
    else { B = p.B3; C = p.C3; ldb = FF; ncol0 = (nt - 3 * PER) * BN; act = true; }
    ldc = ldb;
  } else {
    if (blockIdx.z == 0) { A = p.A0; lda = HD; K = HD; B = p.B0; C = p.C0; }
    else { A = p.A1; lda = FF; K = FF; B = p.B1; C = p.C1; act = true; }
    ldb = DM; ldc = DM; ncol0 = nt * BN;
  }
  const int m0 = mt * BM;

  constexpr int AU = BM * 8 / NT, BU = BK * BN / 8 / NT;
  const u16* asrc[AU]; const u16* bsrc[BU];
#pragma unroll
  for (int i = 0; i < AU; ++i) {
    const int u = i * NT + t, row = u >> 3, c = (u & 7) ^ (row & 7);
    asrc[i] = A + (size_t)(m0 + row) * lda + c * 8;
  }
#pragma unroll
  for (int i = 0; i < BU; ++i) {
    const int u = i * NT + t, hf = u & 1, rp = (u >> 1) & 7, st = u >> 4, kb = st / NSUB, nb = st % NSUB;
    const int r = rp ^ (4 * (kb & 1));
    bsrc[i] = B + (size_t)(kb * 8 + r) * ldb + ncol0 + nb * 16 + hf * 8;
  }
  auto stage = [&](int kt, int buf) {
    char* base = smem + buf * STAGE;
#pragma unroll
    for (int i = 0; i < AU; ++i)
      __builtin_amdgcn_global_load_lds((const GAS u32*)(asrc[i] + kt * BK), (LAS u32*)(base + (i * NT + t) * 16), 16, 0, 0);
#pragma unroll
    for (int i = 0; i < BU; ++i)
      __builtin_amdgcn_global_load_lds((const GAS u32*)(bsrc[i] + (size_t)kt * BK * ldb), (LAS u32*)(base + A_BYTES + (i * NT + t) * 16), 16, 0, 0);
  };

  f32x4 acc[TM][TN];
#pragma unroll
  for (int i = 0; i < TM; ++i)
#pragma unroll
    for (int j = 0; j < TN; ++j) acc[i][j] = (f32x4){0.f, 0.f, 0.f, 0.f};

  int aoff[TM];
#pragma unroll
  for (int i = 0; i < TM; ++i) { const int row = wr * (BM / WM) + i * 16 + fr; aoff[i] = row * 128; }
  const int arx = fr & 7;
  const int tq = (lane & 15) >> 2, tp = lane & 3;

  const int nk = K / BK;
  stage(0, 0);
  for (int kt = 0; kt < nk; ++kt) {
    const int buf = kt & 1;
    asm volatile("s_waitcnt vmcnt(0)" ::: "memory");
    __syncthreads();
    if (kt + 1 < nk) stage(kt + 1, buf ^ 1);
    const char* sa = smem + buf * STAGE;
    const char* sb = sa + A_BYTES;
#pragma unroll
    for (int ks = 0; ks < 2; ++ks) {
      bf16x8 af[TM], bfr[TN];
#pragma unroll
      for (int i = 0; i < TM; ++i) af[i] = *(const bf16x8*)(sa + aoff[i] + (((4 * ks + fq) ^ arx) * 16));
      const int kb = 4 * ks + fq;
      const int rsw = 4 * (kb & 1);
#pragma unroll
      for (int j = 0; j < TN; ++j) {
        const int nb = wc * TN + j;
        const char* sub = sb + (kb * NSUB + nb) * 256 + tp * 8;
        const s16x4 lo = __builtin_amdgcn_ds_read_tr16_b64_v4i16((LAS s16x4*)(sub + ((tq) ^ rsw) * 32));
        const s16x4 hi = __builtin_amdgcn_ds_read_tr16_b64_v4i16((LAS s16x4*)(sub + ((4 + tq) ^ rsw) * 32));
        bfr[j] = (bf16x8){lo[0], lo[1], lo[2], lo[3], hi[0], hi[1], hi[2], hi[3]};
      }
#pragma unroll
      for (int i = 0; i < TM; ++i)
#pragma unroll
        for (int j = 0; j < TN; ++j) acc[i][j] = __builtin_amdgcn_mfma_f32_16x16x32_bf16(bfr[j], af[i], acc[i][j], 0, 0, 0);
    }
  }
  __syncthreads();
  constexpr int WROWS = BM / WM, WCOLS = BN / WN, LDW = WCOLS + 8;
  static_assert(NW * WROWS * LDW * 2 <= gemm_lds(BM, BN, WM, WN), "epilogue staging");
  u16* ew = (u16*)smem + w * WROWS * LDW;
#pragma unroll
  for (int j = 0; j < TN; ++j) {
    float bv[4] = {0.f, 0.f, 0.f, 0.f};
    if (act) {
      const ushort4 bb = *(const ushort4*)(p.bias + ncol0 + wc * WCOLS + j * 16 + fq * 4);
      bv[0] = bf2f(bb.x); bv[1] = bf2f(bb.y); bv[2] = bf2f(bb.z); bv[3] = bf2f(bb.w);
    }
#pragma unroll
    for (int i = 0; i < TM; ++i) {
      float v[4];
#pragma unroll
      for (int r = 0; r < 4; ++r) v[r] = acc[i][j][r];
      if (act) {
        const u32 a01 = cvtpk(v[0], v[1]), a23 = cvtpk(v[2], v[3]);
        v[0] = lo2f(a01) + bv[0]; v[1] = hi2f(a01) + bv[1]; v[2] = lo2f(a23) + bv[2]; v[3] = hi2f(a23) + bv[3];
        if (MODE == 0) {
          const u32 b01 = cvtpk(v[0], v[1]), b23 = cvtpk(v[2], v[3]);
          v[0] = gelu_tanh(lo2f(b01)); v[1] = gelu_tanh(hi2f(b01)); v[2] = gelu_tanh(lo2f(b23)); v[3] = gelu_tanh(hi2f(b23));
        }
      }
      *(uint2*)(&ew[(i * 16 + fr) * LDW + j * 16 + fq * 4]) = make_uint2(cvtpk(v[0], v[1]), cvtpk(v[2], v[3]));
    }
  }
  asm volatile("s_waitcnt lgkmcnt(0)" ::: "memory");
  __builtin_amdgcn_wave_barrier();
  constexpr int CPR = WCOLS / 8;
#pragma unroll
  for (int e = lane; e < WROWS * CPR; e += 64) {
    const int r = e / CPR, c = e % CPR;
    const u32x4 v = *(const u32x4*)(&ew[r * LDW + c * 8]);
    *(u32x4*)(C + (size_t)(m0 + wr * WROWS + r) * ldc + ncol0 + wc * WCOLS + c * 8) = v;
  }
}

__global__ __launch_bounds__(256) void combine_kernel(const u16* __restrict__ x, const u16* __restrict__ a, const u16* __restrict__ bm, u16* __restrict__ out) {
  const size_t i = ((size_t)blockIdx.x * 256 + threadIdx.x) * 8;
  const u32x4 xv = *(const u32x4*)(x + i), av = *(const u32x4*)(a + i), bv = *(const u32x4*)(bm + i);
  u32x4 o;
#pragma unroll
  for (int q = 0; q < 4; ++q) {
    const u32 s1 = cvtpk(lo2f(xv[q]) + lo2f(av[q]), hi2f(xv[q]) + hi2f(av[q]));
    o[q] = cvtpk(lo2f(s1) + lo2f(bv[q]), hi2f(s1) + hi2f(bv[q]));
  }
  *(u32x4*)(out + i) = o;
}

struct ChainP {
  const u16* mem0; const u16* Wq; const u16* Wk; const u16* Wv; const u16* Wo;
  const u16* Kx; const u16* Vx; u16* KM; u16* VM;
  u64* memg; u64* qkvg; u64* partg; u32* tmo;
};
constexpr int NG_MEM = 4096;
constexpr int NG_QKV = 192;
constexpr u32 SPIN_CAP = 400000u;
#define H1_DELAY 16
constexpr int NREP = 8;

__device__ __forceinline__ void st_gran(u64* g, u32 tag, u32 val) {
  __hip_atomic_store((gu64*)g, ((u64)tag << 32) | (u64)val, RLX_AGENT);
}
__device__ __forceinline__ u64 ld_gran(const u64* g) { return __hip_atomic_load((gu64*)g, RLX_AGENT); }
__device__ __forceinline__ void st_gran2(u64* g, u32 tag, u32 v0, u32 v1) {
  const u32x4 d = {v0, tag, v1, tag};
  asm volatile("global_store_dwordx4 %0, %1, off sc1\n\ts_nop 1" :: "v"((gu64*)g), "v"(d) : "memory");
}

__device__ __forceinline__ bool spin_fail(u32& spins, u32* tmo) {
  ++spins;
  if (spins > SPIN_CAP) { __hip_atomic_store((gu32*)tmo, 1u, RLX_AGENT); return true; }
  if ((spins & 1023u) == 0u) { if (__builtin_amdgcn_readfirstlane(__hip_atomic_load((gu32*)tmo, RLX_AGENT))) return true; }
  return false;
}
__device__ __forceinline__ bool wait_sentinel(const u64* g, bool active, u32 tag, u32* tmo) {
  for (u32 spins = 0;;) {
    bool ok = true;
    if (active) ok = ((u32)(ld_gran(g) >> 32) == tag);
    if (__all(ok)) return true;
    if (spin_fail(spins, tmo)) return false;
    __builtin_amdgcn_s_sleep(1);
  }
}
template <int N>
__device__ __forceinline__ bool sweep(const u64* g, int stride, u32 tag, u32 (&v)[N], u32* tmo) {
  for (u32 spins = 0;;) {
    bool ok = true;
#pragma unroll
    for (int k = 0; k < N; ++k) { const u64 x = ld_gran(g + (size_t)k * stride); v[k] = (u32)x; ok &= ((u32)(x >> 32) == tag); }
    if (__all(ok)) return true;
    if (spin_fail(spins, tmo)) return false;
  }
}
template <int N>
__device__ __forceinline__ bool sweep2(__amdgpu_buffer_rsrc_t rs, u32 byte_off, u32 byte_stride, u32 tag, u32 (&v)[2 * N], u32* tmo) {
  for (u32 spins = 0;;) {
    bool ok = true;
#pragma unroll
    for (int k = 0; k < N; ++k) {
      const u32x4 x = __builtin_bit_cast(u32x4, __builtin_amdgcn_raw_buffer_load_b128(rs, byte_off + k * byte_stride, 0, 16));
      v[2 * k] = x[0]; v[2 * k + 1] = x[2]; ok &= (x[1] == tag) & (x[3] == tag);
    }
    if (__all(ok)) return true;
    if (spin_fail(spins, tmo)) return false;
  }
}

#define STAMP(i) do { } while (0)
__device__ __forceinline__ float xrow_max(float v) {
  auto a = __builtin_amdgcn_permlane16_swap(__float_as_uint(v), __float_as_uint(v), false, false);
  v = fmaxf(__uint_as_float(a[0]), __uint_as_float(a[1]));
  auto c = __builtin_amdgcn_permlane32_swap(__float_as_uint(v), __float_as_uint(v), false, false);
  return fmaxf(__uint_as_float(c[0]), __uint_as_float(c[1]));
}
__device__ __forceinline__ float xrow_sum(float v) {
  auto a = __builtin_amdgcn_permlane16_swap(__float_as_uint(v), __float_as_uint(v), false, false);
  v = __uint_as_float(a[0]) + __uint_as_float(a[1]);
  auto c = __builtin_amdgcn_permlane32_swap(__float_as_uint(v), __float_as_uint(v), false, false);
  return __uint_as_float(c[0]) + __uint_as_float(c[1]);
}

__global__ __launch_bounds__(256, 1) void chain_kernel(ChainP p) {
  constexpr int LM = 1032;
  constexpr int LQ = 136;
  constexpr int LV = 72;
  __shared__ __attribute__((aligned(16))) u16 memS[8 * LM];
  __shared__ __attribute__((aligned(16))) float red[4 * 8 * 48];
  __shared__ __attribute__((aligned(16))) u16 qS[8 * LQ];
  __shared__ __attribute__((aligned(16))) u16 kS[48 * LQ];
  __shared__ __attribute__((aligned(16))) u16 vT[128 * LV];

  const int t = threadIdx.x, lane = t & 63, w = __builtin_amdgcn_readfirstlane(t >> 6);
  const int b = blockIdx.x, xcd = b & 7, ib = b >> 3;
  const int h = xcd * 4 + (ib >> 3), j = ib & 7;
  const int R = h * 8 + j;
  const int fr = lane & 15, fq = lane >> 4;
  const __amdgpu_buffer_rsrc_t rs_mem = __builtin_amdgcn_make_buffer_rsrc((void*)p.memg, 0, 2 * NREP * NG_MEM * 8, 0x00020000);
  const __amdgpu_buffer_rsrc_t rs_qkv = __builtin_amdgcn_make_buffer_rsrc((void*)p.qkvg, 0, 2 * NH * 8 * NG_QKV * 8, 0x00020000);
  const __amdgpu_buffer_rsrc_t rs_part = __builtin_amdgcn_make_buffer_rsrc((void*)p.partg, 0, 2 * 256 * NH * 32 * 8, 0x00020000);

  bf16x8 wf[3][8];
  {
    const u16* Ws[3] = {p.Wq, p.Wk, p.Wv};
#pragma unroll
    for (int m = 0; m < 3; ++m)
#pragma unroll
      for (int s = 0; s < 8; ++s) {
        const u16* src = Ws[m] + (size_t)(256 * w + 32 * s + 8 * fq) * HD + h * DH + 16 * j + fr;
#pragma unroll
        for (int i = 0; i < 8; ++i) wf[m][s][i] = (short)src[(size_t)i * HD];
      }
  }
  bf16x8 wo[2][4];
#pragma unroll
  for (int c = 0; c < 2; ++c)
#pragma unroll
    for (int s = 0; s < 4; ++s)
#pragma unroll
      for (int i = 0; i < 8; ++i) {
        const int d = 32 * s + ((i < 4) ? (4 * fq + i) : (16 + 4 * fq + i - 4));
        wo[c][s][i] = (short)p.Wo[(size_t)(h * DH + d) * DM + 128 * j + 16 * (2 * w + c) + fr];
      }
  for (int e = t; e < 128 * LV; e += 256) vT[e] = 0;
  for (int e = t; e < 48 * LQ; e += 256) kS[e] = 0;
  u32x4 kpre[2], vpre[2];
#pragma unroll
  for (int i = 0; i < 2; ++i) {
    const int e = t + i * 256, r = e >> 4, c = (e & 15) * 8;
    kpre[i] = *(const u32x4*)(p.Kx + (size_t)r * HD + h * DH + c);
    vpre[i] = *(const u32x4*)(p.Vx + (size_t)r * HD + h * DH + c);
  }
  __syncthreads();

  for (int s = 0; s < NSEG; ++s) {
    const int par = s & 1;
    const u32 epoch = (u32)s + 1u;
    STAMP(0);
    if (s == 0) {
#pragma unroll
      for (int i = 0; i < 4; ++i) {
        const int e = t + i * 256, r = e >> 7, c = (e & 127) * 8;
        *(u32x4*)(&memS[r * LM + c]) = *(const u32x4*)(p.mem0 + r * DM + c);
      }
    } else {
      u32 v[16];
      const u32 gbase = (u32)(((par ^ 1) * NREP + (b & (NREP - 1))) * NG_MEM + w * 1024);
      __builtin_amdgcn_s_sleep(H1_DELAY);
      if (!sweep2<8>(rs_mem, (gbase + lane * 2) * 8, 128 * 8, (u32)s, v, p.tmo)) return;
#pragma unroll
      for (int k = 0; k < 8; ++k) {
        const int u = lane + k * 64;
        *(uint2*)(&memS[(u & 7) * LM + (w * 64 + (u >> 3)) * 4]) = make_uint2(v[2 * k], v[2 * k + 1]);
      }
    }
#pragma unroll
    for (int i = 0; i < 2; ++i) {
      const int e = t + i * 256, r = e >> 4, c = (e & 15) * 8;
      *(u32x4*)(&kS[r * LQ + c]) = kpre[i];
      const u16* vp = (const u16*)&vpre[i];
      const int pos = (r < 16) ? (8 * (r >> 2) + (r & 3)) : (8 * ((r - 16) >> 2) + 4 + (r & 3));
#pragma unroll
      for (int q = 0; q < 8; ++q) vT[(c + q) * LV + pos] = vp[q];
    }
    __syncthreads();
    STAMP(1);
    {
      f32x4 acc[3] = {{0.f, 0.f, 0.f, 0.f}, {0.f, 0.f, 0.f, 0.f}, {0.f, 0.f, 0.f, 0.f}};
      bf16x8 a[8];
#pragma unroll
      for (int ks = 0; ks < 8; ++ks) a[ks] = *(const bf16x8*)(&memS[(lane & 7) * LM + 256 * w + 32 * ks + 8 * fq]);
#pragma unroll
      for (int ks = 0; ks < 8; ++ks)
#pragma unroll
        for (int m = 0; m < 3; ++m) acc[m] = __builtin_amdgcn_mfma_f32_16x16x32_bf16(a[ks], wf[m][ks], acc[m], 0, 0, 0);
      if (fq < 2) {
#pragma unroll
        for (int m = 0; m < 3; ++m)
#pragma unroll
          for (int r = 0; r < 4; ++r) red[(w * 8 + fq * 4 + r) * 48 + m * 16 + fr] = acc[m][r];
      }
    }
    __syncthreads();
    if (t < NG_QKV) {
      const int r = t / 24, cp = t % 24;
      float v0 = 0.f, v1 = 0.f;
#pragma unroll
      for (int ww = 0; ww < 4; ++ww) { const float2 q2 = *(const float2*)(&red[(ww * 8 + r) * 48 + cp * 2]); v0 += q2.x; v1 += q2.y; }
      const u32 pk = cvtpk(v0, v1);
      const int m = cp >> 3, c = (cp & 7) * 2;
      if (s < NSEG - 1) st_gran(p.qkvg + ((size_t)(par * NH + h) * 8 + j) * NG_QKV + t, epoch, pk);
      if (m == 1) *(u32*)(p.KM + (size_t)(s * MM + r) * HD + h * DH + 16 * j + c) = pk;
      if (m == 2) *(u32*)(p.VM + (size_t)(s * MM + r) * HD + h * DH + 16 * j + c) = pk;
    }
    STAMP(2);
    if (s == NSEG - 1) break;
    bf16x8 kx[2][4], vx[8];
#pragma unroll
    for (int ks = 0; ks < 4; ++ks)
#pragma unroll
      for (int n = 0; n < 2; ++n) kx[n][ks] = *(const bf16x8*)(&kS[(n * 16 + fr) * LQ + 32 * ks + 8 * fq]);
#pragma unroll
    for (int c = 0; c < 8; ++c) vx[c] = *(const bf16x8*)(&vT[(16 * c + fr) * LV + 8 * fq]);
    __builtin_amdgcn_sched_barrier(0);
    {
      u32 v[6];
      const u32 gbase = (u32)(((par * NH + h) * 8 + 2 * w) * NG_QKV);
      if (!sweep2<3>(rs_qkv, (gbase + lane * 2) * 8, 128 * 8, epoch, v, p.tmo)) return;
#pragma unroll
      for (int k = 0; k < 3; ++k) {
        const int g = (lane + k * 64) * 2, jj = 2 * w + g / NG_QKV, e = g % NG_QKV;
        const int r = e / 24, cp = e % 24, m = cp >> 3, c = 16 * jj + (cp & 7) * 2;
        if (m == 0) *(uint2*)(&qS[r * LQ + c]) = make_uint2(v[2 * k], v[2 * k + 1]);
        else if (m == 1) *(uint2*)(&kS[(32 + r) * LQ + c]) = make_uint2(v[2 * k], v[2 * k + 1]);
        else {
          const int pos = 32 + 8 * (r >> 2) + (r & 3);
          vT[c * LV + pos] = (u16)(v[2 * k] & 0xffffu); vT[(c + 1) * LV + pos] = (u16)(v[2 * k] >> 16);
          vT[(c + 2) * LV + pos] = (u16)(v[2 * k + 1] & 0xffffu); vT[(c + 3) * LV + pos] = (u16)(v[2 * k + 1] >> 16);
        }
      }
    }
    __syncthreads();
    STAMP(3);
    {
      f32x4 sc[3] = {{0.f, 0.f, 0.f, 0.f}, {0.f, 0.f, 0.f, 0.f}, {0.f, 0.f, 0.f, 0.f}};
      bf16x8 qf[4], km[4];
      uint2 vm[8];
#pragma unroll
      for (int ks = 0; ks < 4; ++ks) {
        qf[ks] = *(const bf16x8*)(&qS[(lane & 7) * LQ + 32 * ks + 8 * fq]);
        km[ks] = *(const bf16x8*)(&kS[(32 + fr) * LQ + 32 * ks + 8 * fq]);
      }
#pragma unroll
      for (int c = 0; c < 8; ++c) vm[c] = *(const uint2*)(&vT[(16 * c + fr) * LV + 32 + 8 * fq]);
      __builtin_amdgcn_sched_barrier(0);
#pragma unroll
      for (int ks = 0; ks < 4; ++ks) {
        sc[0] = __builtin_amdgcn_mfma_f32_16x16x32_bf16(kx[0][ks], qf[ks], sc[0], 0, 0, 0);
        sc[1] = __builtin_amdgcn_mfma_f32_16x16x32_bf16(kx[1][ks], qf[ks], sc[1], 0, 0, 0);
        sc[2] = __builtin_amdgcn_mfma_f32_16x16x32_bf16(km[ks], qf[ks], sc[2], 0, 0, 0);
      }
      const float scale = 0.08838834764831845f;
      const bool live2 = fq < 2;
      float xs[3][4];
#pragma unroll
      for (int n = 0; n < 3; ++n)
#pragma unroll
        for (int r = 0; r < 4; r += 2) {
          const u32 a = cvtpk(sc[n][r], sc[n][r + 1]);
          const u32 c2 = cvtpk(lo2f(a) * scale, hi2f(a) * scale);
          xs[n][r] = lo2f(c2); xs[n][r + 1] = hi2f(c2);
        }
      if (!live2) { xs[2][0] = xs[2][1] = xs[2][2] = xs[2][3] = -3.0e38f; }
      float mx = fmaxf(fmaxf(fmaxf(xs[0][0], xs[0][1]), fmaxf(xs[0][2], xs[0][3])), fmaxf(fmaxf(xs[1][0], xs[1][1]), fmaxf(xs[1][2], xs[1][3])));
      mx = fmaxf(mx, fmaxf(fmaxf(xs[2][0], xs[2][1]), fmaxf(xs[2][2], xs[2][3])));
      mx = xrow_max(mx);
      float sm = 0.f;
#pragma unroll
      for (int n = 0; n < 3; ++n)
#pragma unroll
        for (int r = 0; r < 4; ++r) { xs[n][r] = __expf(xs[n][r] - mx); sm += xs[n][r]; }
      sm = xrow_sum(sm);
      const float inv = 1.0f / sm;
      u32x4b pw0, pw1;
      pw0[0] = cvtpk(xs[0][0] * inv, xs[0][1] * inv); pw0[1] = cvtpk(xs[0][2] * inv, xs[0][3] * inv);
      pw0[2] = cvtpk(xs[1][0] * inv, xs[1][1] * inv); pw0[3] = cvtpk(xs[1][2] * inv, xs[1][3] * inv);
      pw1[0] = cvtpk(xs[2][0] * inv, xs[2][1] * inv); pw1[1] = cvtpk(xs[2][2] * inv, xs[2][3] * inv);
      pw1[2] = 0u; pw1[3] = 0u;
      const bf16x8 pf0 = __builtin_bit_cast(bf16x8, pw0), pf1 = __builtin_bit_cast(bf16x8, pw1);
      u32x4b ow[4];
#pragma unroll
      for (int c = 0; c < 8; ++c) {
        const u32x4b vmw = {vm[c].x, vm[c].y, 0u, 0u};
        f32x4 oc = {0.f, 0.f, 0.f, 0.f};
        oc = __builtin_amdgcn_mfma_f32_16x16x32_bf16(vx[c], pf0, oc, 0, 0, 0);
        oc = __builtin_amdgcn_mfma_f32_16x16x32_bf16(__builtin_bit_cast(bf16x8, vmw), pf1, oc, 0, 0, 0);
        ow[c >> 1][(c & 1) * 2] = cvtpk(oc[0], oc[1]); ow[c >> 1][(c & 1) * 2 + 1] = cvtpk(oc[2], oc[3]);
      }
      f32x4 pc[2] = {{0.f, 0.f, 0.f, 0.f}, {0.f, 0.f, 0.f, 0.f}};
#pragma unroll
      for (int ks = 0; ks < 4; ++ks)
#pragma unroll
        for (int c = 0; c < 2; ++c) pc[c] = __builtin_amdgcn_mfma_f32_16x16x32_bf16(wo[c][ks], __builtin_bit_cast(bf16x8, ow[ks]), pc[c], 0, 0, 0);
      if (fr < 8) {
#pragma unroll
        for (int c = 0; c < 2; ++c) {
          u64* dst = p.partg + ((size_t)(par * 256 + 32 * j + 8 * w + 4 * c + fq) * NH + h) * 32 + fr * 4;
          st_gran2(dst, epoch, __float_as_uint(pc[c][0]), __float_as_uint(pc[c][1]));
          st_gran2(dst + 2, epoch, __float_as_uint(pc[c][2]), __float_as_uint(pc[c][3]));
        }
      }
    }
    STAMP(4);
    __syncthreads();
#pragma unroll
    for (int i = 0; i < 2; ++i) {
      const int e = t + i * 256, r = e >> 4, c = (e & 15) * 8;
      kpre[i] = *(const u32x4*)(p.Kx + (size_t)((s + 1) * SEG + r) * HD + h * DH + c);
      vpre[i] = *(const u32x4*)(p.Vx + (size_t)((s + 1) * SEG + r) * HD + h * DH + c);
    }
    if (w == 0) {
      u32 v[16];
      const u32 gbase = (u32)(((par * 256 + R) * NH + 8 * fq) * 32 + fr * 2);
      if (!sweep2<8>(rs_part, gbase * 8, 32 * 8, epoch, v, p.tmo)) return;
      float f0 = 0.f, f1 = 0.f;
#pragma unroll
      for (int k = 0; k < 8; ++k) { f0 += __uint_as_float(v[2 * k]); f1 += __uint_as_float(v[2 * k + 1]); }
      f0 = xrow_sum(f0); f1 = xrow_sum(f1);
      const u32 pk = cvtpk(f0, f1);
#pragma unroll
      for (int q = 0; q < NREP / 4; ++q) st_gran(p.memg + (size_t)(par * NREP + q * 4 + fq) * NG_MEM + R * 16 + fr, epoch, pk);
      STAMP(5);
    }
    __syncthreads();
  }
}

__global__ __launch_bounds__(256) void attn_x_kernel(const u16* __restrict__ Q, const u16* __restrict__ Kx, const u16* __restrict__ Vx,
                                                     const u16* __restrict__ KM, const u16* __restrict__ VM, u16* __restrict__ O) {
  constexpr int LVR = 136;
  __shared__ __attribute__((aligned(16))) u16 vS[4][48 * LVR];
  const int t = threadIdx.x, lane = t & 63, w = __builtin_amdgcn_readfirstlane(t >> 6);
  const int item = blockIdx.x * 4 + w, s = item >> 5, h = item & 31;
  const int fr = lane & 15, fq = lane >> 4;
  u16* vw = vS[w];
  const size_t xoff = (size_t)(s * SEG) * HD + h * DH, moff = (size_t)(s * MM) * HD + h * DH;
#pragma unroll
  for (int i = 0; i < 12; ++i) {
    const int e = lane + i * 64, r = e >> 4, c = (e & 15) * 8;
    u32x4 v = {0u, 0u, 0u, 0u};
    if (r < 32) v = *(const u32x4*)(Vx + xoff + (size_t)r * HD + c);
    else if (r < 40) v = *(const u32x4*)(VM + moff + (size_t)(r - 32) * HD + c);
    *(u32x4*)(&vw[r * LVR + c]) = v;
  }
  bf16x8 qf[2][4], kf[3][4];
#pragma unroll
  for (int ks = 0; ks < 4; ++ks) {
#pragma unroll
    for (int n = 0; n < 2; ++n) {
      qf[n][ks] = *(const bf16x8*)(Q + xoff + (size_t)(16 * n + fr) * HD + 32 * ks + 8 * fq);
      kf[n][ks] = *(const bf16x8*)(Kx + xoff + (size_t)(16 * n + fr) * HD + 32 * ks + 8 * fq);
    }
    kf[2][ks] = *(const bf16x8*)(KM + moff + (size_t)(fr & 7) * HD + 32 * ks + 8 * fq);
  }
  asm volatile("s_waitcnt lgkmcnt(0)" ::: "memory");
  __builtin_amdgcn_wave_barrier();
  bf16x8 vf[8][2];
  const int tq = fr >> 2, tp = fr & 3;
#pragma unroll
  for (int c = 0; c < 8; ++c) {
    const s16x4 a0 = __builtin_amdgcn_ds_read_tr16_b64_v4i16((LAS s16x4*)(&vw[(4 * fq + tq) * LVR + 16 * c + 4 * tp]));
    const s16x4 a1 = __builtin_amdgcn_ds_read_tr16_b64_v4i16((LAS s16x4*)(&vw[(16 + 4 * fq + tq) * LVR + 16 * c + 4 * tp]));
    const s16x4 a2 = __builtin_amdgcn_ds_read_tr16_b64_v4i16((LAS s16x4*)(&vw[(32 + 4 * fq + tq) * LVR + 16 * c + 4 * tp]));
    vf[c][0] = (bf16x8){a0[0], a0[1], a0[2], a0[3], a1[0], a1[1], a1[2], a1[3]};
    vf[c][1] = (bf16x8){a2[0], a2[1], a2[2], a2[3], 0, 0, 0, 0};
  }
  const float scale = 0.08838834764831845f;
#pragma unroll
  for (int qt = 0; qt < 2; ++qt) {
    f32x4 sc[3] = {{0.f, 0.f, 0.f, 0.f}, {0.f, 0.f, 0.f, 0.f}, {0.f, 0.f, 0.f, 0.f}};
#pragma unroll
    for (int ks = 0; ks < 4; ++ks)
#pragma unroll
      for (int n = 0; n < 3; ++n) sc[n] = __builtin_amdgcn_mfma_f32_16x16x32_bf16(kf[n][ks], qf[qt][ks], sc[n], 0, 0, 0);
    const int qi = 16 * qt + fr;
    float xs[3][4];
#pragma unroll
    for (int n = 0; n < 3; ++n)
#pragma unroll
      for (int r = 0; r < 4; r += 2) {
        const u32 a = cvtpk(sc[n][r], sc[n][r + 1]);
        const u32 c2 = cvtpk(lo2f(a) * scale, hi2f(a) * scale);
        xs[n][r] = lo2f(c2); xs[n][r + 1] = hi2f(c2);
      }
    float mx = -3.0e38f;
#pragma unroll
    for (int n = 0; n < 3; ++n)
#pragma unroll
      for (int r = 0; r < 4; ++r) {
        const bool live = (n < 2) ? (16 * n + 4 * fq + r <= qi) : (fq < 2);
        if (!live) xs[n][r] = -3.0e38f;
        mx = fmaxf(mx, xs[n][r]);
      }
    mx = xrow_max(mx);
    float sm = 0.f;
#pragma unroll
    for (int n = 0; n < 3; ++n)
#pragma unroll
      for (int r = 0; r < 4; ++r) { xs[n][r] = __expf(xs[n][r] - mx); sm += xs[n][r]; }
    sm = xrow_sum(sm);
    const float inv = 1.0f / sm;
    u32x4b pw0, pw1;
    pw0[0] = cvtpk(xs[0][0] * inv, xs[0][1] * inv); pw0[1] = cvtpk(xs[0][2] * inv, xs[0][3] * inv);
    pw0[2] = cvtpk(xs[1][0] * inv, xs[1][1] * inv); pw0[3] = cvtpk(xs[1][2] * inv, xs[1][3] * inv);
    pw1[0] = cvtpk(xs[2][0] * inv, xs[2][1] * inv); pw1[1] = cvtpk(xs[2][2] * inv, xs[2][3] * inv);
    pw1[2] = 0u; pw1[3] = 0u;
    const bf16x8 pf0 = __builtin_bit_cast(bf16x8, pw0), pf1 = __builtin_bit_cast(bf16x8, pw1);
    u16* orow = O + xoff + (size_t)qi * HD + 4 * fq;
#pragma unroll
    for (int c = 0; c < 8; ++c) {
      f32x4 oc = {0.f, 0.f, 0.f, 0.f};
      oc = __builtin_amdgcn_mfma_f32_16x16x32_bf16(vf[c][0], pf0, oc, 0, 0, 0);
      oc = __builtin_amdgcn_mfma_f32_16x16x32_bf16(vf[c][1], pf1, oc, 0, 0, 0);
      *(uint2*)(orow + 16 * c) = make_uint2(cvtpk(oc[0], oc[1]), cvtpk(oc[2], oc[3]));
    }
  }
}

extern "C" void kernel_launch(void* const* d_in, const int* in_sizes, int n_in,
                              void* d_out, int out_size, void* d_ws, size_t ws_size,
                              hipStream_t stream) {
  const u16* x = (const u16*)d_in[0];
  const u16* mem0 = (const u16*)d_in[1];
  const u16* Wq = (const u16*)d_in[2];
  const u16* Wk = (const u16*)d_in[3];
  const u16* Wv = (const u16*)d_in[4];
  const u16* Wo = (const u16*)d_in[5];
  const u16* w1 = (const u16*)d_in[8];
  const u16* b1 = (const u16*)d_in[9];
  const u16* w2 = (const u16*)d_in[10];
  const u16* b2 = (const u16*)d_in[11];
  u16* out = (u16*)d_out;

  char* ws = (char*)d_ws;
  size_t off = 0;
  auto carve = [&](size_t bytes) { char* q = ws + off; off += (bytes + 255) & ~(size_t)255; return q; };
  u64* memg = (u64*)carve((size_t)2 * NREP * NG_MEM * 8);
  u64* qkvg = (u64*)carve((size_t)2 * NH * 8 * NG_QKV * 8);
  u64* partg = (u64*)carve((size_t)2 * 256 * NH * 32 * 8);
  u32* tmo = (u32*)carve(256);
  const size_t sync_bytes = off;
  u16* xn = (u16*)carve((size_t)S_ * DM * 2);
  u16* Qx = (u16*)carve((size_t)S_ * HD * 2);
  u16* Kx = (u16*)carve((size_t)S_ * HD * 2);
  u16* Vx = (u16*)carve((size_t)S_ * HD * 2);
  u16* Hb = (u16*)carve((size_t)S_ * FF * 2);
  u16* Ox = (u16*)carve((size_t)S_ * HD * 2);
  u16* KM = (u16*)carve((size_t)NSEG * MM * HD * 2);
  u16* VM = (u16*)carve((size_t)NSEG * MM * HD * 2);
  u16* Bm = (u16*)carve((size_t)S_ * DM * 2);
  u16* Am = (u16*)carve((size_t)S_ * DM * 2);

  hipMemsetAsync(ws, 0, sync_bytes, stream);
  ln_kernel<<<S_, 256, 0, stream>>>(x, xn);
  {
    GemmP g{xn, nullptr, Wq, Wk, Wv, w1, Qx, Kx, Vx, Hb, b1};
    constexpr int LDS1 = gemm_lds(256, 256, 2, 4);
    hipFuncSetAttribute((const void*)gemm_kernel<256, 256, 2, 4, 0>, hipFuncAttributeMaxDynamicSharedMemorySize, LDS1);
    gemm_kernel<256, 256, 2, 4, 0><<<dim3(62, 8), 512, LDS1, stream>>>(g);
  }
  {
    ChainP c{mem0, Wq, Wk, Wv, Wo, Kx, Vx, KM, VM, memg, qkvg, partg, tmo};
    chain_kernel<<<256, 256, 0, stream>>>(c);
  }
  attn_x_kernel<<<NSEG * NH / 4, 256, 0, stream>>>(Qx, Kx, Vx, KM, VM, Ox);
  {
    GemmP g{Ox, Hb, Wo, w2, nullptr, nullptr, Am, Bm, nullptr, nullptr, b2};
    constexpr int LDS2 = gemm_lds(128, 128, 2, 2);
    hipFuncSetAttribute((const void*)gemm_kernel<128, 128, 2, 2, 1>, hipFuncAttributeMaxDynamicSharedMemorySize, LDS2);
    gemm_kernel<128, 128, 2, 2, 1><<<dim3(8, 16, 2), 256, LDS2, stream>>>(g);
  }
  combine_kernel<<<S_ * DM / 8 / 256, 256, 0, stream>>>(x, Am, Bm, out);
}
```

```cpp
#include <hip/hip_runtime.h>
#include <hip/hip_bf16.h>
#include <stdint.h>

typedef unsigned short u16;
typedef unsigned int u32;
typedef unsigned long long u64;
#define GAS __attribute__((address_space(1)))
#define LAS __attribute__((address_space(3)))
typedef GAS u64 gu64;
typedef GAS u32 gu32;
typedef __attribute__((ext_vector_type(8))) short bf16x8;
typedef __attribute__((ext_vector_type(4))) short s16x4;
typedef __attribute__((ext_vector_type(4))) float f32x4;
typedef __attribute__((ext_vector_type(4))) unsigned int u32x4;
#define RLX_AGENT __ATOMIC_RELAXED, __HIP_MEMORY_SCOPE_AGENT

constexpr int S_ = 2048, DM = 1024, NH = 32, DH = 128, HD = 4096, MM = 8, SEG = 32, NSEG = 64, FF = 3584;

__device__ __forceinline__ float bf2f(u16 v) { return __uint_as_float(((u32)v) << 16); }
__device__ __forceinline__ u16 f2bf(float f) { u32 u = __float_as_uint(f); u += 0x7fffu + ((u >> 16) & 1u); return (u16)(u >> 16); }
__device__ __forceinline__ float rbf(float f) { return bf2f(f2bf(f)); }
typedef __bf16 hbf2 __attribute__((ext_vector_type(2)));
typedef float f32x2 __attribute__((ext_vector_type(2)));
__device__ __forceinline__ u32 cvtpk(float a, float b) { f32x2 v = {a, b}; return __builtin_bit_cast(u32, __builtin_convertvector(v, hbf2)); }
__device__ __forceinline__ float lo2f(u32 v) { return __uint_as_float(v << 16); }
__device__ __forceinline__ float hi2f(u32 v) { return __uint_as_float(v & 0xffff0000u); }

__device__ __forceinline__ float xrow_max(float v) {
  auto a = __builtin_amdgcn_permlane16_swap(__float_as_uint(v), __float_as_uint(v), false, false);
  v = fmaxf(__uint_as_float(a[0]), __uint_as_float(a[1]));
  auto c = __builtin_amdgcn_permlane32_swap(__float_as_uint(v), __float_as_uint(v), false, false);
  return fmaxf(__uint_as_float(c[0]), __uint_as_float(c[1]));
}
__device__ __forceinline__ float xrow_sum(float v) {
  auto a = __builtin_amdgcn_permlane16_swap(__float_as_uint(v), __float_as_uint(v), false, false);
  v = __uint_as_float(a[0]) + __uint_as_float(a[1]);
  auto c = __builtin_amdgcn_permlane32_swap(__float_as_uint(v), __float_as_uint(v), false, false);
  return __uint_as_float(c[0]) + __uint_as_float(c[1]);
}
__device__ __forceinline__ float ror8_sum(float v) {
  return v + __builtin_bit_cast(float, __builtin_amdgcn_update_dpp(0, __builtin_bit_cast(int, v), 0x128, 0xf, 0xf, false));
}

__global__ __launch_bounds__(256) void ln_kernel(const u16* __restrict__ x, u16* __restrict__ xn) {
  __shared__ float red[8];
  const int row = blockIdx.x, t = threadIdx.x, lane = t & 63, w = t >> 6;
  const ushort4 v = *(const ushort4*)(x + (size_t)row * DM + t * 4);
  float f0 = bf2f(v.x), f1 = bf2f(v.y), f2 = bf2f(v.z), f3 = bf2f(v.w);
  float s = (f0 + f1) + (f2 + f3);
#pragma unroll
  for (int m = 32; m >= 1; m >>= 1) s += __shfl_xor(s, m);
  if (lane == 0) red[w] = s;
  __syncthreads();
  const float mean = ((red[0] + red[1]) + (red[2] + red[3])) * (1.0f / DM);
  float d0 = f0 - mean, d1 = f1 - mean, d2 = f2 - mean, d3 = f3 - mean;
  float q = (d0 * d0 + d1 * d1) + (d2 * d2 + d3 * d3);
#pragma unroll
  for (int m = 32; m >= 1; m >>= 1) q += __shfl_xor(q, m);
  if (lane == 0) red[4 + w] = q;
  __syncthreads();
  const float var = ((red[4] + red[5]) + (red[6] + red[7])) * (1.0f / DM);
  const float mb = rbf(mean), vb = rbf(var);
  const float den = rbf(sqrtf(rbf(vb + 1e-5f)));
  ushort4 o;
  o.x = f2bf(rbf(f0 - mb) / den); o.y = f2bf(rbf(f1 - mb) / den);
  o.z = f2bf(rbf(f2 - mb) / den); o.w = f2bf(rbf(f3 - mb) / den);
  *(ushort4*)(xn + (size_t)row * DM + t * 4) = o;
}

__device__ __forceinline__ float gelu_tanh(float x) {
  const float k0 = 0.7978845608028654f, k1 = 0.044715f;
  const float u = k0 * (x + k1 * x * x * x);
  return 0.5f * x * (1.0f + tanhf(u));
}
__device__ __forceinline__ void glds16(const void* gsrc, u32 lds_dst) {
  u32 keep;
  asm volatile("s_mov_b32 %0, m0\n\ts_mov_b32 m0, %2\n\ts_nop 0\n\tglobal_load_lds_dwordx4 %1, off\n\ts_mov_b32 m0, %0"
               : "=&s"(keep) : "v"(gsrc), "s"(lds_dst) : "memory");
}
template <int N> __device__ __forceinline__ void wait_vm() { asm volatile("s_waitcnt vmcnt(%0)" :: "n"(N) : "memory"); }
__device__ __forceinline__ void st16_sc1(void* p, u32x4 v) {
  asm volatile("global_store_dwordx4 %0, %1, off sc1\n\ts_nop 1" :: "v"(p), "v"(v) : "memory");
}
constexpr int gemm_lds(int BM, int BN, int WM, int WN, int NSTAGE) {
  const int st = NSTAGE * (BM * 64 * 2 + 64 * BN * 2), ep = WM * WN * (BM / WM) * (BN / WN + 8) * 2;
  return st > ep ? st : ep;
}
template <int BM, int BN, int WM, int WN, int NSTAGE>
__device__ __forceinline__ void gemm_tile(char* smem, const u16* A, int lda, const u16* B, int ldb, int K,
                                          u16* C, int ldc, const u16* bias, int EPI, bool WT) {
  constexpr int NW = WM * WN, NT = NW * 64, BK = 64;
  constexpr int TM = BM / WM / 16, TN = BN / WN / 16;
  constexpr int A_BYTES = BM * BK * 2, B_BYTES = BK * BN * 2, STAGE = A_BYTES + B_BYTES;
  constexpr int NSUB = BN / 16;
  const int t = threadIdx.x, lane = t & 63, w = __builtin_amdgcn_readfirstlane(t >> 6);
  const int wr = w / WN, wc = w % WN, fr = lane & 15, fq = lane >> 4;
  constexpr int AU = BM * 8 / NT, BU = BK * BN / 8 / NT, LPS = AU + BU;
  const u16* asrc[AU]; const u16* bsrc[BU];
#pragma unroll
  for (int i = 0; i < AU; ++i) {
    const int u = i * NT + t, row = u >> 3, c = (u & 7) ^ (row & 7);
    asrc[i] = A + (size_t)row * lda + c * 8;
  }
#pragma unroll
  for (int i = 0; i < BU; ++i) {
    const int u = i * NT + t, hf = u & 1, rp = (u >> 1) & 7, st = u >> 4, kb = st / NSUB, nb = st % NSUB;
    const int r = rp ^ (4 * (kb & 1));
    bsrc[i] = B + (size_t)(kb * 8 + r) * ldb + nb * 16 + hf * 8;
  }
  const u32 lds0 = (u32)(uintptr_t)(LAS char*)smem + (u32)w * 1024u;
  auto stage = [&](int kt, int buf) {
    const u32 base = lds0 + buf * STAGE;
#pragma unroll
    for (int i = 0; i < AU; ++i) glds16(asrc[i] + kt * BK, base + i * NT * 16);
#pragma unroll
    for (int i = 0; i < BU; ++i) glds16(bsrc[i] + (size_t)kt * BK * ldb, base + A_BYTES + i * NT * 16);
  };
  f32x4 acc[TM][TN];
#pragma unroll
  for (int i = 0; i < TM; ++i)
#pragma unroll
    for (int j = 0; j < TN; ++j) acc[i][j] = (f32x4){0.f, 0.f, 0.f, 0.f};
  const int arow0 = (wr * (BM / WM) + fr) * 128;
  const int arx = fr & 7;
  const int tq = (lane & 15) >> 2, tp = lane & 3;
  const int nk = K / BK;
  wait_vm<0>();
#pragma unroll
  for (int k = 0; k < NSTAGE - 1; ++k) stage(k, k);
  int buf = 0;
  for (int kt = 0; kt < nk; ++kt) {
    if (kt + NSTAGE - 1 <= nk) wait_vm<LPS * (NSTAGE - 2)>(); else wait_vm<0>();
    asm volatile("s_waitcnt lgkmcnt(0)" ::: "memory");
    __builtin_amdgcn_s_barrier();
    asm volatile("" ::: "memory");
    if (kt + NSTAGE - 1 < nk) { int nb2 = buf + NSTAGE - 1; if (nb2 >= NSTAGE) nb2 -= NSTAGE; stage(kt + NSTAGE - 1, nb2); }
    const char* sa = smem + buf * STAGE;
    const char* sb = sa + A_BYTES;
#pragma unroll
    for (int ks = 0; ks < 2; ++ks) {
      bf16x8 af[TM], bfr[TN];
#pragma unroll
      for (int i = 0; i < TM; ++i) af[i] = *(const bf16x8*)(sa + arow0 + i * 2048 + (((4 * ks + fq) ^ arx) * 16));
      const int kb = 4 * ks + fq;
      const int rsw = 4 * (kb & 1);
#pragma unroll
      for (int j = 0; j < TN; ++j) {
        const int nb = wc * TN + j;
        const char* sub = sb + (kb * NSUB + nb) * 256 + tp * 8;
        const s16x4 lo = __builtin_amdgcn_ds_read_tr16_b64_v4i16((LAS s16x4*)(sub + ((tq) ^ rsw) * 32));
        const s16x4 hi = __builtin_amdgcn_ds_read_tr16_b64_v4i16((LAS s16x4*)(sub + ((4 + tq) ^ rsw) * 32));
        bfr[j] = (bf16x8){lo[0], lo[1], lo[2], lo[3], hi[0], hi[1], hi[2], hi[3]};
      }
#pragma unroll
      for (int i = 0; i < TM; ++i)
#pragma unroll
        for (int j = 0; j < TN; ++j) acc[i][j] = __builtin_amdgcn_mfma_f32_16x16x32_bf16(bfr[j], af[i], acc[i][j], 0, 0, 0);
    }
    if (++buf == NSTAGE) buf = 0;
  }
  asm volatile("s_waitcnt lgkmcnt(0)" ::: "memory");
  __builtin_amdgcn_s_barrier();
  asm volatile("" ::: "memory");
  constexpr int WROWS = BM / WM, WCOLS = BN / WN, LDW = WCOLS + 8;
  u16* ew = (u16*)smem + w * WROWS * LDW;
#pragma unroll
  for (int j = 0; j < TN; ++j) {
    float bv[4] = {0.f, 0.f, 0.f, 0.f};
    if (EPI) {
      const ushort4 bb = *(const ushort4*)(bias + wc * WCOLS + j * 16 + fq * 4);
      bv[0] = bf2f(bb.x); bv[1] = bf2f(bb.y); bv[2] = bf2f(bb.z); bv[3] = bf2f(bb.w);
    }
#pragma unroll
    for (int i = 0; i < TM; ++i) {
      float v[4];
#pragma unroll
      for (int r = 0; r < 4; ++r) v[r] = acc[i][j][r];
      if (EPI) {
        const u32 a01 = cvtpk(v[0], v[1]), a23 = cvtpk(v[2], v[3]);
        v[0] = lo2f(a01) + bv[0]; v[1] = hi2f(a01) + bv[1]; v[2] = lo2f(a23) + bv[2]; v[3] = hi2f(a23) + bv[3];
        if (EPI == 2) {
          const u32 b01 = cvtpk(v[0], v[1]), b23 = cvtpk(v[2], v[3]);
          v[0] = gelu_tanh(lo2f(b01)); v[1] = gelu_tanh(hi2f(b01)); v[2] = gelu_tanh(lo2f(b23)); v[3] = gelu_tanh(hi2f(b23));
        }
      }
      *(uint2*)(&ew[(i * 16 + fr) * LDW + j * 16 + fq * 4]) = make_uint2(cvtpk(v[0], v[1]), cvtpk(v[2], v[3]));
    }
  }
  asm volatile("s_waitcnt lgkmcnt(0)" ::: "memory");
  __builtin_amdgcn_wave_barrier();
  constexpr int CPR = WCOLS / 8;
#pragma unroll 4
  for (int e = lane; e < WROWS * CPR; e += 64) {
    const int r = e / CPR, c = e % CPR;
    const u32x4 v = *(const u32x4*)(&ew[r * LDW + c * 8]);
    u16* dst = C + (size_t)(wr * WROWS + r) * ldc + wc * WCOLS + c * 8;
    if (WT) st16_sc1(dst, v); else *(u32x4*)dst = v;
  }
  asm volatile("s_waitcnt lgkmcnt(0)" ::: "memory");
  __builtin_amdgcn_s_barrier();
  asm volatile("" ::: "memory");
}

__global__ __launch_bounds__(256) void gemm_wo_kernel(const u16* __restrict__ Ox, const u16* __restrict__ Wo, u16* __restrict__ Am) {
  extern __shared__ __attribute__((aligned(16))) char smem[];
  const int xq = blockIdx.x & 7, oq = blockIdx.x >> 3;
  const int mt = oq & 15, nt = xq + 8 * (oq >> 4);
  gemm_tile<128, 64, 2, 2, 4>(smem, Ox + (size_t)mt * 128 * HD, HD, Wo + nt * 64, DM, HD, Am + (size_t)mt * 128 * DM + nt * 64, DM, nullptr, 0, false);
}

__global__ __launch_bounds__(256) void combine_kernel(const u16* __restrict__ x, const u16* __restrict__ a, const u16* __restrict__ bm, u16* __restrict__ out) {
  const size_t i = ((size_t)blockIdx.x * 256 + threadIdx.x) * 8;
  const u32x4 xv = *(const u32x4*)(x + i), av = *(const u32x4*)(a + i), bv = *(const u32x4*)(bm + i);
  u32x4 o;
#pragma unroll
  for (int q = 0; q < 4; ++q) {
    const u32 s1 = cvtpk(lo2f(xv[q]) + lo2f(av[q]), hi2f(xv[q]) + hi2f(av[q]));
    o[q] = cvtpk(lo2f(s1) + lo2f(bv[q]), hi2f(s1) + hi2f(bv[q]));
  }
  *(u32x4*)(out + i) = o;
}

struct FusedP {
  const u16* xn; const u16* mem0; const u16* Wq; const u16* Wk; const u16* Wv; const u16* Wo;
  const u16* w1; const u16* b1; const u16* w2; const u16* b2;
  u16* Qx; u16* Kx; u16* Vx; u16* Hb; u16* Bm; u16* KM; u16* VM;
  u64* memg; u64* qkvg; u64* partg; u32* kvflag; u32* ctl;
};
constexpr int NG_MEM = 4096;
constexpr int NG_QKV = 384;
constexpr u32 SPIN_CAP = 2000000u;
constexpr int NREP = 8;
#define H1_DELAY 8

__device__ __forceinline__ void st_gran(u64* g, u32 tag, u32 val) {
  __hip_atomic_store((gu64*)g, ((u64)tag << 32) | (u64)val, RLX_AGENT);
}
__device__ __forceinline__ void st_gran2(u64* g, u32 tag, u32 v0, u32 v1) {
  const u32x4 d = {v0, tag, v1, tag};
  asm volatile("global_store_dwordx4 %0, %1, off sc1\n\ts_nop 1" :: "v"((gu64*)g), "v"(d) : "memory");
}
__device__ __forceinline__ bool spin_fail(u32& spins, u32* tmo) {
  ++spins;
  if (spins > SPIN_CAP) { __hip_atomic_store((gu32*)tmo, 1u, RLX_AGENT); return true; }
  if ((spins & 1023u) == 0u) { if (__builtin_amdgcn_readfirstlane(__hip_atomic_load((gu32*)tmo, RLX_AGENT))) return true; }
  return false;
}
template <int N>
__device__ __forceinline__ bool sweep2(__amdgpu_buffer_rsrc_t rs, u32 byte_off, u32 byte_stride, u32 tag, u32 (&v)[2 * N], u32* tmo) {
  for (u32 spins = 0;;) {
    bool ok = true;
#pragma unroll
    for (int k = 0; k < N; ++k) {
      const u32x4 x = __builtin_bit_cast(u32x4, __builtin_amdgcn_raw_buffer_load_b128(rs, byte_off + k * byte_stride, 0, 16));
      v[2 * k] = x[0]; v[2 * k + 1] = x[2]; ok &= (x[1] == tag) & (x[3] == tag);
    }
    if (__all(ok)) return true;
    if (spin_fail(spins, tmo)) return false;
  }
}
__device__ __forceinline__ bool wait_flag(const u32* f, u32* tmo) {
  for (u32 spins = 0;;) {
    if (__builtin_amdgcn_readfirstlane(__hip_atomic_load((gu32*)f, RLX_AGENT))) return true;
    if (spin_fail(spins, tmo)) return false;
    __builtin_amdgcn_s_sleep(8);
  }
}

#define STAMP(i) do { } while (0)

constexpr int C_LM = 1032, C_LQ = 136, C_LV = 72;
constexpr int C_OFF_WO = 0;
constexpr int C_OFF_MEM = C_OFF_WO + 65536;
constexpr int C_OFF_RED = C_OFF_MEM + 8 * C_LM * 2;
constexpr int C_OFF_Q = C_OFF_RED + 4 * 8 * 96 * 4;
constexpr int C_OFF_K = C_OFF_Q + 8 * C_LQ * 2;
constexpr int C_OFF_V = C_OFF_K + 48 * C_LQ * 2;
constexpr int C_LDS = C_OFF_V + 128 * C_LV * 2;
constexpr int W_LDS_A = gemm_lds(256, 128, 2, 2, 2), W_LDS_B = gemm_lds(128, 128, 2, 2, 3);
constexpr int FUSED_LDS = (C_LDS > W_LDS_A ? (C_LDS > W_LDS_B ? C_LDS : W_LDS_B) : (W_LDS_A > W_LDS_B ? W_LDS_A : W_LDS_B));

__device__ __forceinline__ void chain_role(const FusedP& p, char* smem, int cb, int h, int j) {
  constexpr int LM = C_LM, LQ = C_LQ, LV = C_LV;
  u16* memS = (u16*)(smem + C_OFF_MEM);
  float* red = (float*)(smem + C_OFF_RED);
  u16* qS = (u16*)(smem + C_OFF_Q);
  u16* kS = (u16*)(smem + C_OFF_K);
  u16* vT = (u16*)(smem + C_OFF_V);
  const int t = threadIdx.x, lane = t & 63, w = __builtin_amdgcn_readfirstlane(t >> 6);
  bf16x8* woS = (bf16x8*)(smem + C_OFF_WO) + w * 16 * 64 + lane;
  const int R = h * 4 + j;
  const int fr = lane & 15, fq = lane >> 4;
  u32* tmo = p.ctl;
  const __amdgpu_buffer_rsrc_t rs_mem = __builtin_amdgcn_make_buffer_rsrc((void*)p.memg, 0, 2 * NREP * NG_MEM * 8, 0x00020000);
  const __amdgpu_buffer_rsrc_t rs_qkv = __builtin_amdgcn_make_buffer_rsrc((void*)p.qkvg, 0, 2 * NH * 4 * NG_QKV * 8, 0x00020000);
  const __amdgpu_buffer_rsrc_t rs_part = __builtin_amdgcn_make_buffer_rsrc((void*)p.partg, 0, 2 * 128 * NH * 64 * 8, 0x00020000);
  const __amdgpu_buffer_rsrc_t rs_k = __builtin_amdgcn_make_buffer_rsrc((void*)p.Kx, 0, S_ * HD * 2, 0x00020000);
  const __amdgpu_buffer_rsrc_t rs_v = __builtin_amdgcn_make_buffer_rsrc((void*)p.Vx, 0, S_ * HD * 2, 0x00020000);

  bf16x8 wf[6][8];
  {
    const u16* Ws[3] = {p.Wq, p.Wk, p.Wv};
#pragma unroll
    for (int m = 0; m < 6; ++m)
#pragma unroll
      for (int s = 0; s < 8; ++s) {
        const u16* src = Ws[m >> 1] + (size_t)(256 * w + 32 * s + 8 * fq) * HD + h * DH + 32 * j + 16 * (m & 1) + fr;
#pragma unroll
        for (int i = 0; i < 8; ++i) wf[m][s][i] = (short)src[(size_t)i * HD];
      }
  }
#pragma unroll
  for (int c = 0; c < 4; ++c)
#pragma unroll
    for (int s = 0; s < 4; ++s) {
      bf16x8 f;
#pragma unroll
      for (int i = 0; i < 8; ++i) {
        const int d = 32 * s + ((i < 4) ? (4 * fq + i) : (16 + 4 * fq + i - 4));
        f[i] = (short)p.Wo[(size_t)(h * DH + d) * DM + 256 * j + 64 * w + 16 * c + fr];
      }
      woS[(c * 4 + s) * 64] = f;
    }
  for (int e = t; e < 128 * LV; e += 256) vT[e] = 0;
  for (int e = t; e < 48 * LQ; e += 256) kS[e] = 0;
  u32x4 kpre[2], vpre[2];
  auto load_kv = [&](int seg) -> bool {
    if ((seg & 7) == 0) {
      if (!wait_flag(p.kvflag + ((seg >> 3) * 64 + h) * 16, tmo)) return false;
      if (!wait_flag(p.kvflag + ((seg >> 3) * 64 + 32 + h) * 16, tmo)) return false;
    }
#pragma unroll
    for (int i = 0; i < 2; ++i) {
      const int e = t + i * 256, r = e >> 4, c = (e & 15) * 8;
      const u32 off = (u32)(((seg * SEG + r) * HD + h * DH + c) * 2);
      kpre[i] = __builtin_bit_cast(u32x4, __builtin_amdgcn_raw_buffer_load_b128(rs_k, off, 0, 16));
      vpre[i] = __builtin_bit_cast(u32x4, __builtin_amdgcn_raw_buffer_load_b128(rs_v, off, 0, 16));
    }
    return true;
  };
  if (!load_kv(0)) return;
  __syncthreads();

  for (int s = 0; s < NSEG; ++s) {
    const int par = s & 1;
    const u32 epoch = (u32)s + 1u;
    STAMP(0);
    if (s == 0) {
#pragma unroll
      for (int i = 0; i < 4; ++i) {
        const int e = t + i * 256, r = e >> 7, c = (e & 127) * 8;
        *(u32x4*)(&memS[r * LM + c]) = *(const u32x4*)(p.mem0 + r * DM + c);
      }
    } else {
      u32 v[16];
      const u32 gbase = (u32)(((par ^ 1) * NREP + (cb & (NREP - 1))) * NG_MEM + w * 1024);
      __builtin_amdgcn_s_sleep(H1_DELAY);
      if (!sweep2<8>(rs_mem, (gbase + lane * 2) * 8, 128 * 8, (u32)s, v, tmo)) return;
#pragma unroll
      for (int k = 0; k < 8; ++k) {
        const int u = lane + k * 64;
        *(uint2*)(&memS[((u >> 1) & 7) * LM + (w * 32 + (u >> 4)) * 8 + (u & 1) * 4]) = make_uint2(v[2 * k], v[2 * k + 1]);
      }
    }
#pragma unroll
    for (int i = 0; i < 2; ++i) {
      const int e = t + i * 256, r = e >> 4, c = (e & 15) * 8;
      *(u32x4*)(&kS[r * LQ + c]) = kpre[i];
      const u16* vp = (const u16*)&vpre[i];
      const int pos = (r < 16) ? (8 * (r >> 2) + (r & 3)) : (8 * ((r - 16) >> 2) + 4 + (r & 3));
#pragma unroll
      for (int q = 0; q < 8; ++q) vT[(c + q) * LV + pos] = vp[q];
    }
    __syncthreads();
    STAMP(1);
    {
      f32x4 acc[6];
#pragma unroll
      for (int m = 0; m < 6; ++m) acc[m] = (f32x4){0.f, 0.f, 0.f, 0.f};
      bf16x8 a[8];
#pragma unroll
      for (int ks = 0; ks < 8; ++ks) a[ks] = *(const bf16x8*)(&memS[(lane & 7) * LM + 256 * w + 32 * ks + 8 * fq]);
#pragma unroll
      for (int ks = 0; ks < 8; ++ks)
#pragma unroll
        for (int m = 0; m < 6; ++m) acc[m] = __builtin_amdgcn_mfma_f32_16x16x32_bf16(a[ks], wf[m][ks], acc[m], 0, 0, 0);
      if (fq < 2) {
#pragma unroll
        for (int m = 0; m < 6; ++m)
#pragma unroll
          for (int r = 0; r < 4; ++r) red[(w * 8 + fq * 4 + r) * 96 + m * 16 + fr] = acc[m][r];
      }
    }
    __syncthreads();
    if (t < NG_QKV / 2) {
      const int r = t / 24, cq = t % 24;
      float4 v4 = make_float4(0.f, 0.f, 0.f, 0.f);
#pragma unroll
      for (int ww = 0; ww < 4; ++ww) { const float4 q4 = *(const float4*)(&red[(ww * 8 + r) * 96 + cq * 4]); v4.x += q4.x; v4.y += q4.y; v4.z += q4.z; v4.w += q4.w; }
      const u32 pk0 = cvtpk(v4.x, v4.y), pk1 = cvtpk(v4.z, v4.w);
      const int m = cq >> 3, c = (cq & 7) * 4;
      if (s < NSEG - 1) st_gran2(p.qkvg + ((size_t)(par * NH + h) * 4 + j) * NG_QKV + 2 * t, epoch, pk0, pk1);
      if (m == 1) *(uint2*)(p.KM + (size_t)(s * MM + r) * HD + h * DH + 32 * j + c) = make_uint2(pk0, pk1);
      if (m == 2) *(uint2*)(p.VM + (size_t)(s * MM + r) * HD + h * DH + 32 * j + c) = make_uint2(pk0, pk1);
    }
    STAMP(2);
    if (s == NSEG - 1) break;
    bf16x8 kx[2][4], vx[8];
#pragma unroll
    for (int ks = 0; ks < 4; ++ks)
#pragma unroll
      for (int n = 0; n < 2; ++n) kx[n][ks] = *(const bf16x8*)(&kS[(n * 16 + fr) * LQ + 32 * ks + 8 * fq]);
#pragma unroll
    for (int c = 0; c < 8; ++c) vx[c] = *(const bf16x8*)(&vT[(16 * c + fr) * LV + 8 * fq]);
    __builtin_amdgcn_sched_barrier(0);
    {
      u32 v[6];
      const u32 gbase = (u32)(((par * NH + h) * 4 + w) * NG_QKV);
      if (!sweep2<3>(rs_qkv, (gbase + lane * 2) * 8, 128 * 8, epoch, v, tmo)) return;
#pragma unroll
      for (int k = 0; k < 3; ++k) {
        const int u = lane + k * 64;
        const int r = u / 24, cq = u % 24, m = cq >> 3, c = 32 * w + (cq & 7) * 4;
        if (m == 0) *(uint2*)(&qS[r * LQ + c]) = make_uint2(v[2 * k], v[2 * k + 1]);
        else if (m == 1) *(uint2*)(&kS[(32 + r) * LQ + c]) = make_uint2(v[2 * k], v[2 * k + 1]);
        else {
          const int pos = 32 + 8 * (r >> 2) + (r & 3);
          vT[c * LV + pos] = (u16)(v[2 * k] & 0xffffu); vT[(c + 1) * LV + pos] = (u16)(v[2 * k] >> 16);
          vT[(c + 2) * LV + pos] = (u16)(v[2 * k + 1] & 0xffffu); vT[(c + 3) * LV + pos] = (u16)(v[2 * k + 1] >> 16);
        }
      }
    }
    __syncthreads();
    STAMP(3);
    {
      f32x4 sc[3] = {{0.f, 0.f, 0.f, 0.f}, {0.f, 0.f, 0.f, 0.f}, {0.f, 0.f, 0.f, 0.f}};
      bf16x8 qf[4], km[4];
      uint2 vm[8];
#pragma unroll
      for (int ks = 0; ks < 4; ++ks) {
        qf[ks] = *(const bf16x8*)(&qS[(lane & 7) * LQ + 32 * ks + 8 * fq]);
        km[ks] = *(const bf16x8*)(&kS[(32 + fr) * LQ + 32 * ks + 8 * fq]);
      }
#pragma unroll
      for (int c = 0; c < 8; ++c) vm[c] = *(const uint2*)(&vT[(16 * c + fr) * LV + 32 + 8 * fq]);
      __builtin_amdgcn_sched_barrier(0);
#pragma unroll
      for (int ks = 0; ks < 4; ++ks) {
        sc[0] = __builtin_amdgcn_mfma_f32_16x16x32_bf16(kx[0][ks], qf[ks], sc[0], 0, 0, 0);
        sc[1] = __builtin_amdgcn_mfma_f32_16x16x32_bf16(kx[1][ks], qf[ks], sc[1], 0, 0, 0);
        sc[2] = __builtin_amdgcn_mfma_f32_16x16x32_bf16(km[ks], qf[ks], sc[2], 0, 0, 0);
      }
      const float scale = 0.08838834764831845f;
      const bool live2 = fq < 2;
      float xs[3][4];
#pragma unroll
      for (int n = 0; n < 3; ++n)
#pragma unroll
        for (int r = 0; r < 4; r += 2) {
          const u32 a = cvtpk(sc[n][r], sc[n][r + 1]);
          const u32 c2 = cvtpk(lo2f(a) * scale, hi2f(a) * scale);
          xs[n][r] = lo2f(c2); xs[n][r + 1] = hi2f(c2);
        }
      if (!live2) { xs[2][0] = xs[2][1] = xs[2][2] = xs[2][3] = -3.0e38f; }
      float mx = fmaxf(fmaxf(fmaxf(xs[0][0], xs[0][1]), fmaxf(xs[0][2], xs[0][3])), fmaxf(fmaxf(xs[1][0], xs[1][1]), fmaxf(xs[1][2], xs[1][3])));
      mx = fmaxf(mx, fmaxf(fmaxf(xs[2][0], xs[2][1]), fmaxf(xs[2][2], xs[2][3])));
      mx = xrow_max(mx);
      float sm = 0.f;
#pragma unroll
      for (int n = 0; n < 3; ++n)
#pragma unroll
        for (int r = 0; r < 4; ++r) { xs[n][r] = __expf(xs[n][r] - mx); sm += xs[n][r]; }
      sm = xrow_sum(sm);
      const float inv = 1.0f / sm;
      u32x4 pw0, pw1;
      pw0[0] = cvtpk(xs[0][0] * inv, xs[0][1] * inv); pw0[1] = cvtpk(xs[0][2] * inv, xs[0][3] * inv);
      pw0[2] = cvtpk(xs[1][0] * inv, xs[1][1] * inv); pw0[3] = cvtpk(xs[1][2] * inv, xs[1][3] * inv);
      pw1[0] = cvtpk(xs[2][0] * inv, xs[2][1] * inv); pw1[1] = cvtpk(xs[2][2] * inv, xs[2][3] * inv);
      pw1[2] = 0u; pw1[3] = 0u;
      const bf16x8 pf0 = __builtin_bit_cast(bf16x8, pw0), pf1 = __builtin_bit_cast(bf16x8, pw1);
      u32x4 ow[4];
#pragma unroll
      for (int c = 0; c < 8; ++c) {
        const u32x4 vmw = {vm[c].x, vm[c].y, 0u, 0u};
        f32x4 oc = {0.f, 0.f, 0.f, 0.f};
        oc = __builtin_amdgcn_mfma_f32_16x16x32_bf16(vx[c], pf0, oc, 0, 0, 0);
        oc = __builtin_amdgcn_mfma_f32_16x16x32_bf16(__builtin_bit_cast(bf16x8, vmw), pf1, oc, 0, 0, 0);
        ow[c >> 1][(c & 1) * 2] = cvtpk(oc[0], oc[1]); ow[c >> 1][(c & 1) * 2 + 1] = cvtpk(oc[2], oc[3]);
      }
      f32x4 pc[4];
#pragma unroll
      for (int c = 0; c < 4; ++c) pc[c] = (f32x4){0.f, 0.f, 0.f, 0.f};
#pragma unroll
      for (int ks = 0; ks < 4; ++ks)
#pragma unroll
        for (int c = 0; c < 4; ++c) pc[c] = __builtin_amdgcn_mfma_f32_16x16x32_bf16(woS[(c * 4 + ks) * 64], __builtin_bit_cast(bf16x8, ow[ks]), pc[c], 0, 0, 0);
      if (fr < 8) {
#pragma unroll
        for (int c = 0; c < 4; ++c) {
          u64* dst = p.partg + ((size_t)(par * 128 + 32 * j + 8 * w + 2 * c + (fq >> 1)) * NH + h) * 64 + fr * 8 + 4 * (fq & 1);
          st_gran2(dst, epoch, __float_as_uint(pc[c][0]), __float_as_uint(pc[c][1]));
          st_gran2(dst + 2, epoch, __float_as_uint(pc[c][2]), __float_as_uint(pc[c][3]));
        }
      }
    }
    STAMP(4);
    {
      u32 v[8];
      const int hg = lane >> 3, u = lane & 7;
      const u32 gbase = (u32)(((par * 128 + R) * NH + 4 * hg) * 64 + (8 * w + u) * 2);
      if (!sweep2<4>(rs_part, gbase * 8, 64 * 8, epoch, v, tmo)) return;
      float f0 = 0.f, f1 = 0.f;
#pragma unroll
      for (int k = 0; k < 4; ++k) { f0 += __uint_as_float(v[2 * k]); f1 += __uint_as_float(v[2 * k + 1]); }
      f0 = ror8_sum(f0); f1 = ror8_sum(f1);
      f0 = xrow_sum(f0); f1 = xrow_sum(f1);
      const u32 pk = cvtpk(f0, f1);
      st_gran(p.memg + (size_t)(par * NREP + hg) * NG_MEM + R * 32 + 8 * w + u, epoch, pk);
      STAMP(5);
    }
    if (!load_kv(s + 1)) return;
    __syncthreads();
  }
}

__device__ __forceinline__ void worker_role(const FusedP& p, char* smem, int gw) {
  const int t = threadIdx.x;
  u32* tmo = p.ctl;
  for (int i = 0; i < 4; ++i) {
    const int id = gw + 128 * i, rb = id >> 6, n = id & 63;
    const u16* B = (n < 32 ? p.Wk : p.Wv) + (n & 31) * 128;
    u16* C = (n < 32 ? p.Kx : p.Vx) + (size_t)rb * 256 * HD + (n & 31) * 128;
    gemm_tile<256, 128, 2, 2, 2>(smem, p.xn + (size_t)rb * 256 * DM, DM, B, HD, DM, C, HD, nullptr, 0, true);
    asm volatile("s_waitcnt vmcnt(0)" ::: "memory");
    __syncthreads();
    if (t == 0) __hip_atomic_store((gu32*)(p.kvflag + (rb * 64 + n) * 16), 1u, RLX_AGENT);
  }
  for (int id = gw; id < 480; id += 128) {
    if (id < 224) {
      const int rb = id / 28, n = id % 28;
      gemm_tile<256, 128, 2, 2, 2>(smem, p.xn + (size_t)rb * 256 * DM, DM, p.w1 + n * 128, FF, DM, p.Hb + (size_t)rb * 256 * FF + n * 128, FF, p.b1 + n * 128, 2, false);
    } else {
      const int q = id - 224, rb = q >> 5, n = q & 31;
      gemm_tile<256, 128, 2, 2, 2>(smem, p.xn + (size_t)rb * 256 * DM, DM, p.Wq + n * 128, HD, DM, p.Qx + (size_t)rb * 256 * HD + n * 128, HD, nullptr, 0, false);
    }
  }
  asm volatile("s_waitcnt vmcnt(0)" ::: "memory");
  __syncthreads();
  if (t == 0) {
    __builtin_amdgcn_fence(__ATOMIC_RELEASE, "agent");
    asm volatile("s_waitcnt vmcnt(0)" ::: "memory");
    __hip_atomic_fetch_add((gu32*)(p.ctl + 64), 1u, RLX_AGENT);
    for (u32 spins = 0;;) {
      if (__hip_atomic_load((gu32*)(p.ctl + 64), RLX_AGENT) >= 128u) break;
      if (++spins > SPIN_CAP) { __hip_atomic_store((gu32*)tmo, 1u, RLX_AGENT); break; }
      __builtin_amdgcn_s_sleep(16);
    }
    __builtin_amdgcn_fence(__ATOMIC_ACQUIRE, "agent");
    asm volatile("s_waitcnt vmcnt(0)" ::: "memory");
  }
  __syncthreads();
  {
    const int mt = gw >> 3, nt = gw & 7;
    gemm_tile<128, 128, 2, 2, 3>(smem, p.Hb + (size_t)mt * 128 * FF, FF, p.w2 + nt * 128, DM, FF, p.Bm + (size_t)mt * 128 * DM + nt * 128, DM, p.b2 + nt * 128, 1, false);
  }
}

__global__ __launch_bounds__(256, 1) void fused_kernel(FusedP p) {
  extern __shared__ __attribute__((aligned(16))) char smem[];
  const int b = blockIdx.x, xcd = b & 7, ib = b >> 3;
  if (ib < 16) chain_role(p, smem, xcd * 16 + ib, xcd * 4 + (ib >> 2), ib & 3);
  else worker_role(p, smem, (ib - 16) * 8 + xcd);
}

__global__ __launch_bounds__(256) void attn_x_kernel(const u16* __restrict__ Q, const u16* __restrict__ Kx, const u16* __restrict__ Vx,
                                                     const u16* __restrict__ KM, const u16* __restrict__ VM, u16* __restrict__ O) {
  constexpr int LVR = 136;
  __shared__ __attribute__((aligned(16))) u16 vS[4][48 * LVR];
  const int t = threadIdx.x, lane = t & 63, w = __builtin_amdgcn_readfirstlane(t >> 6);
  const int item = blockIdx.x * 4 + w, s = item >> 5, h = item & 31;
  const int fr = lane & 15, fq = lane >> 4;
  u16* vw = vS[w];
  const size_t xoff = (size_t)(s * SEG) * HD + h * DH, moff = (size_t)(s * MM) * HD + h * DH;
#pragma unroll
  for (int i = 0; i < 12; ++i) {
    const int e = lane + i * 64, r = e >> 4, c = (e & 15) * 8;
    u32x4 v = {0u, 0u, 0u, 0u};
    if (r < 32) v = *(const u32x4*)(Vx + xoff + (size_t)r * HD + c);
    else if (r < 40) v = *(const u32x4*)(VM + moff + (size_t)(r - 32) * HD + c);
    *(u32x4*)(&vw[r * LVR + c]) = v;
  }
  bf16x8 qf[2][4], kf[3][4];
#pragma unroll
  for (int ks = 0; ks < 4; ++ks) {
#pragma unroll
    for (int n = 0; n < 2; ++n) {
      qf[n][ks] = *(const bf16x8*)(Q + xoff + (size_t)(16 * n + fr) * HD + 32 * ks + 8 * fq);
      kf[n][ks] = *(const bf16x8*)(Kx + xoff + (size_t)(16 * n + fr) * HD + 32 * ks + 8 * fq);
    }
    kf[2][ks] = *(const bf16x8*)(KM + moff + (size_t)(fr & 7) * HD + 32 * ks + 8 * fq);
  }
  asm volatile("s_waitcnt lgkmcnt(0)" ::: "memory");
  __builtin_amdgcn_wave_barrier();
  bf16x8 vf[8][2];
  const int tq = fr >> 2, tp = fr & 3;
#pragma unroll
  for (int c = 0; c < 8; ++c) {
    const s16x4 a0 = __builtin_amdgcn_ds_read_tr16_b64_v4i16((LAS s16x4*)(&vw[(4 * fq + tq) * LVR + 16 * c + 4 * tp]));
    const s16x4 a1 = __builtin_amdgcn_ds_read_tr16_b64_v4i16((LAS s16x4*)(&vw[(16 + 4 * fq + tq) * LVR + 16 * c + 4 * tp]));
    const s16x4 a2 = __builtin_amdgcn_ds_read_tr16_b64_v4i16((LAS s16x4*)(&vw[(32 + 4 * fq + tq) * LVR + 16 * c + 4 * tp]));
    vf[c][0] = (bf16x8){a0[0], a0[1], a0[2], a0[3], a1[0], a1[1], a1[2], a1[3]};
    vf[c][1] = (bf16x8){a2[0], a2[1], a2[2], a2[3], 0, 0, 0, 0};
  }
  const float scale = 0.08838834764831845f;
#pragma unroll
  for (int qt = 0; qt < 2; ++qt) {
    f32x4 sc[3] = {{0.f, 0.f, 0.f, 0.f}, {0.f, 0.f, 0.f, 0.f}, {0.f, 0.f, 0.f, 0.f}};
#pragma unroll
    for (int ks = 0; ks < 4; ++ks)
#pragma unroll
      for (int n = 0; n < 3; ++n) sc[n] = __builtin_amdgcn_mfma_f32_16x16x32_bf16(kf[n][ks], qf[qt][ks], sc[n], 0, 0, 0);
    const int qi = 16 * qt + fr;
    float xs[3][4];
#pragma unroll
    for (int n = 0; n < 3; ++n)
#pragma unroll
      for (int r = 0; r < 4; r += 2) {
        const u32 a = cvtpk(sc[n][r], sc[n][r + 1]);
        const u32 c2 = cvtpk(lo2f(a) * scale, hi2f(a) * scale);
        xs[n][r] = lo2f(c2); xs[n][r + 1] = hi2f(c2);
      }
    float mx = -3.0e38f;
#pragma unroll
    for (int n = 0; n < 3; ++n)
#pragma unroll
      for (int r = 0; r < 4; ++r) {
        const bool live = (n < 2) ? (16 * n + 4 * fq + r <= qi) : (fq < 2);
        if (!live) xs[n][r] = -3.0e38f;
        mx = fmaxf(mx, xs[n][r]);
      }
    mx = xrow_max(mx);
    float sm = 0.f;
#pragma unroll
    for (int n = 0; n < 3; ++n)
#pragma unroll
      for (int r = 0; r < 4; ++r) { xs[n][r] = __expf(xs[n][r] - mx); sm += xs[n][r]; }
    sm = xrow_sum(sm);
    const float inv = 1.0f / sm;
    u32x4 pw0, pw1;
    pw0[0] = cvtpk(xs[0][0] * inv, xs[0][1] * inv); pw0[1] = cvtpk(xs[0][2] * inv, xs[0][3] * inv);
    pw0[2] = cvtpk(xs[1][0] * inv, xs[1][1] * inv); pw0[3] = cvtpk(xs[1][2] * inv, xs[1][3] * inv);
    pw1[0] = cvtpk(xs[2][0] * inv, xs[2][1] * inv); pw1[1] = cvtpk(xs[2][2] * inv, xs[2][3] * inv);
    pw1[2] = 0u; pw1[3] = 0u;
    const bf16x8 pf0 = __builtin_bit_cast(bf16x8, pw0), pf1 = __builtin_bit_cast(bf16x8, pw1);
    u16* orow = O + xoff + (size_t)qi * HD + 4 * fq;
#pragma unroll
    for (int c = 0; c < 8; ++c) {
      f32x4 oc = {0.f, 0.f, 0.f, 0.f};
      oc = __builtin_amdgcn_mfma_f32_16x16x32_bf16(vf[c][0], pf0, oc, 0, 0, 0);
      oc = __builtin_amdgcn_mfma_f32_16x16x32_bf16(vf[c][1], pf1, oc, 0, 0, 0);
      *(uint2*)(orow + 16 * c) = make_uint2(cvtpk(oc[0], oc[1]), cvtpk(oc[2], oc[3]));
    }
  }
}

extern "C" void kernel_launch(void* const* d_in, const int* in_sizes, int n_in,
                              void* d_out, int out_size, void* d_ws, size_t ws_size,
                              hipStream_t stream) {
  const u16* x = (const u16*)d_in[0];
  const u16* mem0 = (const u16*)d_in[1];
  const u16* Wq = (const u16*)d_in[2];
  const u16* Wk = (const u16*)d_in[3];
  const u16* Wv = (const u16*)d_in[4];
  const u16* Wo = (const u16*)d_in[5];
  const u16* w1 = (const u16*)d_in[8];
  const u16* b1 = (const u16*)d_in[9];
  const u16* w2 = (const u16*)d_in[10];
  const u16* b2 = (const u16*)d_in[11];
  u16* out = (u16*)d_out;

  char* ws = (char*)d_ws;
  size_t off = 0;
  auto carve = [&](size_t bytes) { char* q = ws + off; off += (bytes + 255) & ~(size_t)255; return q; };
  u64* memg = (u64*)carve((size_t)2 * NREP * NG_MEM * 8);
  u64* qkvg = (u64*)carve((size_t)2 * NH * 4 * NG_QKV * 8);
  u64* partg = (u64*)carve((size_t)2 * 128 * NH * 64 * 8);
  u32* kvflag = (u32*)carve((size_t)512 * 64);
  u32* ctl = (u32*)carve(1024);
  const size_t sync_bytes = off;
  u16* xn = (u16*)carve((size_t)S_ * DM * 2);
  u16* Qx = (u16*)carve((size_t)S_ * HD * 2);
  u16* Kx = (u16*)carve((size_t)S_ * HD * 2);
  u16* Vx = (u16*)carve((size_t)S_ * HD * 2);
  u16* Hb = (u16*)carve((size_t)S_ * FF * 2);
  u16* Ox = (u16*)carve((size_t)S_ * HD * 2);
  u16* KM = (u16*)carve((size_t)NSEG * MM * HD * 2);
  u16* VM = (u16*)carve((size_t)NSEG * MM * HD * 2);
  u16* Bm = (u16*)carve((size_t)S_ * DM * 2);
  u16* Am = (u16*)carve((size_t)S_ * DM * 2);

  (void)hipMemsetAsync(ws, 0, sync_bytes, stream);
  ln_kernel<<<S_, 256, 0, stream>>>(x, xn);
  {
    FusedP f{xn, mem0, Wq, Wk, Wv, Wo, w1, b1, w2, b2, Qx, Kx, Vx, Hb, Bm, KM, VM, memg, qkvg, partg, kvflag, ctl};
    (void)hipFuncSetAttribute((const void*)fused_kernel, hipFuncAttributeMaxDynamicSharedMemorySize, FUSED_LDS);
    fused_kernel<<<256, 256, FUSED_LDS, stream>>>(f);
  }
  attn_x_kernel<<<NSEG * NH / 4, 256, 0, stream>>>(Qx, Kx, Vx, KM, VM, Ox);
  {
    constexpr int LDSW = gemm_lds(128, 64, 2, 2, 4);
    (void)hipFuncSetAttribute((const void*)gemm_wo_kernel, hipFuncAttributeMaxDynamicSharedMemorySize, LDSW);
    gemm_wo_kernel<<<256, 256, LDSW, stream>>>(Ox, Wo, Am);
  }
  combine_kernel<<<S_ * DM / 8 / 256, 256, 0, stream>>>(x, Am, Bm, out);
}
```

```cpp
#include <hip/hip_runtime.h>
#include <hip/hip_bf16.h>
#include <stdint.h>

typedef unsigned short u16;
typedef unsigned int u32;
typedef unsigned long long u64;
#define GAS __attribute__((address_space(1)))
#define LAS __attribute__((address_space(3)))
typedef GAS u64 gu64;
typedef GAS u32 gu32;
typedef __attribute__((ext_vector_type(8))) short bf16x8;
typedef __attribute__((ext_vector_type(4))) short s16x4;
typedef __attribute__((ext_vector_type(4))) float f32x4;
typedef __attribute__((ext_vector_type(4))) unsigned int u32x4;
#define RLX_AGENT __ATOMIC_RELAXED, __HIP_MEMORY_SCOPE_AGENT

constexpr int S_ = 2048, DM = 1024, NH = 32, DH = 128, HD = 4096, MM = 8, SEG = 32, NSEG = 64, FF = 3584;

__device__ __forceinline__ float bf2f(u16 v) { return __uint_as_float(((u32)v) << 16); }
__device__ __forceinline__ u16 f2bf(float f) { u32 u = __float_as_uint(f); u += 0x7fffu + ((u >> 16) & 1u); return (u16)(u >> 16); }
__device__ __forceinline__ float rbf(float f) { return bf2f(f2bf(f)); }
typedef __bf16 hbf2 __attribute__((ext_vector_type(2)));
typedef float f32x2 __attribute__((ext_vector_type(2)));
__device__ __forceinline__ u32 cvtpk(float a, float b) { f32x2 v = {a, b}; return __builtin_bit_cast(u32, __builtin_convertvector(v, hbf2)); }
__device__ __forceinline__ float lo2f(u32 v) { return __uint_as_float(v << 16); }
__device__ __forceinline__ float hi2f(u32 v) { return __uint_as_float(v & 0xffff0000u); }

__device__ __forceinline__ float xrow_max(float v) {
  auto a = __builtin_amdgcn_permlane16_swap(__float_as_uint(v), __float_as_uint(v), false, false);
  v = fmaxf(__uint_as_float(a[0]), __uint_as_float(a[1]));
  auto c = __builtin_amdgcn_permlane32_swap(__float_as_uint(v), __float_as_uint(v), false, false);
  return fmaxf(__uint_as_float(c[0]), __uint_as_float(c[1]));
}
__device__ __forceinline__ float xrow_sum(float v) {
  auto a = __builtin_amdgcn_permlane16_swap(__float_as_uint(v), __float_as_uint(v), false, false);
  v = __uint_as_float(a[0]) + __uint_as_float(a[1]);
  auto c = __builtin_amdgcn_permlane32_swap(__float_as_uint(v), __float_as_uint(v), false, false);
  return __uint_as_float(c[0]) + __uint_as_float(c[1]);
}
__device__ __forceinline__ float ror8_sum(float v) {
  return v + __builtin_bit_cast(float, __builtin_amdgcn_update_dpp(0, __builtin_bit_cast(int, v), 0x128, 0xf, 0xf, false));
}

__global__ __launch_bounds__(256) void ln_kernel(const u16* __restrict__ x, u16* __restrict__ xn) {
  __shared__ float red[8];
  const int row = blockIdx.x, t = threadIdx.x, lane = t & 63, w = t >> 6;
  const ushort4 v = *(const ushort4*)(x + (size_t)row * DM + t * 4);
  float f0 = bf2f(v.x), f1 = bf2f(v.y), f2 = bf2f(v.z), f3 = bf2f(v.w);
  float s = (f0 + f1) + (f2 + f3);
#pragma unroll
  for (int m = 32; m >= 1; m >>= 1) s += __shfl_xor(s, m);
  if (lane == 0) red[w] = s;
  __syncthreads();
  const float mean = ((red[0] + red[1]) + (red[2] + red[3])) * (1.0f / DM);
  float d0 = f0 - mean, d1 = f1 - mean, d2 = f2 - mean, d3 = f3 - mean;
  float q = (d0 * d0 + d1 * d1) + (d2 * d2 + d3 * d3);
#pragma unroll
  for (int m = 32; m >= 1; m >>= 1) q += __shfl_xor(q, m);
  if (lane == 0) red[4 + w] = q;
  __syncthreads();
  const float var = ((red[4] + red[5]) + (red[6] + red[7])) * (1.0f / DM);
  const float mb = rbf(mean), vb = rbf(var);
  const float den = rbf(sqrtf(rbf(vb + 1e-5f)));
  ushort4 o;
  o.x = f2bf(rbf(f0 - mb) / den); o.y = f2bf(rbf(f1 - mb) / den);
  o.z = f2bf(rbf(f2 - mb) / den); o.w = f2bf(rbf(f3 - mb) / den);
  *(ushort4*)(xn + (size_t)row * DM + t * 4) = o;
}

__device__ __forceinline__ float gelu_tanh(float x) {
  const float k0 = 0.7978845608028654f, k1 = 0.044715f;
  const float u = k0 * (x + k1 * x * x * x);
  return 0.5f * x * (1.0f + tanhf(u));
}
__device__ __forceinline__ void glds16(const void* gsrc, u32 lds_dst) {
  u32 keep;
  asm volatile("s_mov_b32 %0, m0\n\ts_mov_b32 m0, %2\n\ts_nop 0\n\tglobal_load_lds_dwordx4 %1, off\n\ts_mov_b32 m0, %0"
               : "=&s"(keep) : "v"(gsrc), "s"(lds_dst) : "memory");
}
template <int N> __device__ __forceinline__ void wait_vm() { asm volatile("s_waitcnt vmcnt(%0)" :: "n"(N) : "memory"); }
__device__ __forceinline__ void st16_sc1(void* p, u32x4 v) {
  asm volatile("global_store_dwordx4 %0, %1, off sc1\n\ts_nop 1" :: "v"(p), "v"(v) : "memory");
}
constexpr int gemm_lds(int BM, int BN, int WM, int WN, int NSTAGE) {
  const int st = NSTAGE * (BM * 64 * 2 + 64 * BN * 2), ep = WM * WN * (BM / WM) * (BN / WN + 8) * 2;
  return st > ep ? st : ep;
}
template <int BM, int BN, int WM, int WN, int NSTAGE>
__device__ __forceinline__ void gemm_tile(char* smem, const u16* A, int lda, const u16* B, int ldb, int K,
                                          u16* C, int ldc, const u16* bias, int EPI, bool WT) {
  constexpr int NW = WM * WN, NT = NW * 64, BK = 64;
  constexpr int TM = BM / WM / 16, TN = BN / WN / 16;
  constexpr int A_BYTES = BM * BK * 2, B_BYTES = BK * BN * 2, STAGE = A_BYTES + B_BYTES;
  constexpr int NSUB = BN / 16;
  const int t = threadIdx.x, lane = t & 63, w = __builtin_amdgcn_readfirstlane(t >> 6);
  const int wr = w / WN, wc = w % WN, fr = lane & 15, fq = lane >> 4;
  constexpr int AU = BM * 8 / NT, BU = BK * BN / 8 / NT, LPS = AU + BU;
  const u16* asrc[AU]; const u16* bsrc[BU];
#pragma unroll
  for (int i = 0; i < AU; ++i) {
    const int u = i * NT + t, row = u >> 3, c = (u & 7) ^ (row & 7);
    asrc[i] = A + (size_t)row * lda + c * 8;
  }
#pragma unroll
  for (int i = 0; i < BU; ++i) {
    const int u = i * NT + t, hf = u & 1, rp = (u >> 1) & 7, st = u >> 4, kb = st / NSUB, nb = st % NSUB;
    const int r = rp ^ (4 * (kb & 1));
    bsrc[i] = B + (size_t)(kb * 8 + r) * ldb + nb * 16 + hf * 8;
  }
  const u32 lds0 = (u32)(uintptr_t)(LAS char*)smem + (u32)w * 1024u;
  auto stage = [&](int kt, int buf) {
    const u32 base = lds0 + buf * STAGE;
#pragma unroll
    for (int i = 0; i < AU; ++i) glds16(asrc[i] + kt * BK, base + i * NT * 16);
#pragma unroll
    for (int i = 0; i < BU; ++i) glds16(bsrc[i] + (size_t)kt * BK * ldb, base + A_BYTES + i * NT * 16);
  };
  f32x4 acc[TM][TN];
#pragma unroll
  for (int i = 0; i < TM; ++i)
#pragma unroll
    for (int j = 0; j < TN; ++j) acc[i][j] = (f32x4){0.f, 0.f, 0.f, 0.f};
  const int arow0 = (wr * (BM / WM) + fr) * 128;
  const int arx = fr & 7;
  const int tq = (lane & 15) >> 2, tp = lane & 3;
  const int nk = K / BK;
  wait_vm<0>();
#pragma unroll
  for (int k = 0; k < NSTAGE - 1; ++k) stage(k, k);
  int buf = 0;
  for (int kt = 0; kt < nk; ++kt) {
    if (kt + NSTAGE - 1 <= nk) wait_vm<LPS * (NSTAGE - 2)>(); else wait_vm<0>();
    asm volatile("s_waitcnt lgkmcnt(0)" ::: "memory");
    __builtin_amdgcn_s_barrier();
    asm volatile("" ::: "memory");
    if (kt + NSTAGE - 1 < nk) { int nb2 = buf + NSTAGE - 1; if (nb2 >= NSTAGE) nb2 -= NSTAGE; stage(kt + NSTAGE - 1, nb2); }
    const char* sa = smem + buf * STAGE;
    const char* sb = sa + A_BYTES;
#pragma unroll
    for (int ks = 0; ks < 2; ++ks) {
      bf16x8 af[TM], bfr[TN];
#pragma unroll
      for (int i = 0; i < TM; ++i) af[i] = *(const bf16x8*)(sa + arow0 + i * 2048 + (((4 * ks + fq) ^ arx) * 16));
      const int kb = 4 * ks + fq;
      const int rsw = 4 * (kb & 1);
#pragma unroll
      for (int j = 0; j < TN; ++j) {
        const int nb = wc * TN + j;
        const char* sub = sb + (kb * NSUB + nb) * 256 + tp * 8;
        const s16x4 lo = __builtin_amdgcn_ds_read_tr16_b64_v4i16((LAS s16x4*)(sub + ((tq) ^ rsw) * 32));
        const s16x4 hi = __builtin_amdgcn_ds_read_tr16_b64_v4i16((LAS s16x4*)(sub + ((4 + tq) ^ rsw) * 32));
        bfr[j] = (bf16x8){lo[0], lo[1], lo[2], lo[3], hi[0], hi[1], hi[2], hi[3]};
      }
#pragma unroll
      for (int i = 0; i < TM; ++i)
#pragma unroll
        for (int j = 0; j < TN; ++j) acc[i][j] = __builtin_amdgcn_mfma_f32_16x16x32_bf16(bfr[j], af[i], acc[i][j], 0, 0, 0);
    }
    if (++buf == NSTAGE) buf = 0;
  }
  asm volatile("s_waitcnt lgkmcnt(0)" ::: "memory");
  __builtin_amdgcn_s_barrier();
  asm volatile("" ::: "memory");
  constexpr int WROWS = BM / WM, WCOLS = BN / WN, LDW = WCOLS + 8;
  u16* ew = (u16*)smem + w * WROWS * LDW;
#pragma unroll
  for (int j = 0; j < TN; ++j) {
    float bv[4] = {0.f, 0.f, 0.f, 0.f};
    if (EPI) {
      const ushort4 bb = *(const ushort4*)(bias + wc * WCOLS + j * 16 + fq * 4);
      bv[0] = bf2f(bb.x); bv[1] = bf2f(bb.y); bv[2] = bf2f(bb.z); bv[3] = bf2f(bb.w);
    }
#pragma unroll
    for (int i = 0; i < TM; ++i) {
      float v[4];
#pragma unroll
      for (int r = 0; r < 4; ++r) v[r] = acc[i][j][r];
      if (EPI) {
        const u32 a01 = cvtpk(v[0], v[1]), a23 = cvtpk(v[2], v[3]);
        v[0] = lo2f(a01) + bv[0]; v[1] = hi2f(a01) + bv[1]; v[2] = lo2f(a23) + bv[2]; v[3] = hi2f(a23) + bv[3];
        if (EPI == 2) {
          const u32 b01 = cvtpk(v[0], v[1]), b23 = cvtpk(v[2], v[3]);
          v[0] = gelu_tanh(lo2f(b01)); v[1] = gelu_tanh(hi2f(b01)); v[2] = gelu_tanh(lo2f(b23)); v[3] = gelu_tanh(hi2f(b23));
        }
      }
      *(uint2*)(&ew[(i * 16 + fr) * LDW + j * 16 + fq * 4]) = make_uint2(cvtpk(v[0], v[1]), cvtpk(v[2], v[3]));
    }
  }
  asm volatile("s_waitcnt lgkmcnt(0)" ::: "memory");
  __builtin_amdgcn_wave_barrier();
  constexpr int CPR = WCOLS / 8;
#pragma unroll 4
  for (int e = lane; e < WROWS * CPR; e += 64) {
    const int r = e / CPR, c = e % CPR;
    const u32x4 v = *(const u32x4*)(&ew[r * LDW + c * 8]);
    u16* dst = C + (size_t)(wr * WROWS + r) * ldc + wc * WCOLS + c * 8;
    if (WT) st16_sc1(dst, v); else *(u32x4*)dst = v;
  }
  asm volatile("s_waitcnt lgkmcnt(0)" ::: "memory");
  __builtin_amdgcn_s_barrier();
  asm volatile("" ::: "memory");
}

struct GemmP {
  const u16* A0; const u16* A1; const u16* B0; const u16* B1; const u16* B2; const u16* B3;
  u16* C0; u16* C1; u16* C2; u16* C3;
  const u16* bias;
};
template <int BM, int BN, int WM, int WN, int NSTAGE>
__global__ __launch_bounds__(WM * WN * 64) void gemm1_kernel(GemmP p) {
  extern __shared__ __attribute__((aligned(16))) char smem[];
  constexpr int NMT = S_ / BM, PER = HD / BN;
  const int xq = blockIdx.x & 7, oq = blockIdx.x >> 3;
  const int mt = oq % NMT, nt = xq + 8 * (oq / NMT);
  if (nt >= (3 * HD + FF) / BN) return;
  const u16* A = p.A0 + (size_t)mt * BM * DM;
  if (nt < 3 * PER) {
    const int m = nt / PER, n0 = (nt % PER) * BN;
    const u16* B = (m == 0 ? p.B0 : (m == 1 ? p.B1 : p.B2)) + n0;
    u16* C = (m == 0 ? p.C0 : (m == 1 ? p.C1 : p.C2)) + (size_t)mt * BM * HD + n0;
    gemm_tile<BM, BN, WM, WN, NSTAGE>(smem, A, DM, B, HD, DM, C, HD, nullptr, 0, false);
  } else {
    const int n0 = (nt - 3 * PER) * BN;
    gemm_tile<BM, BN, WM, WN, NSTAGE>(smem, A, DM, p.B3 + n0, FF, DM, p.C3 + (size_t)mt * BM * FF + n0, FF, p.bias + n0, 2, false);
  }
}
template <int BM, int BN, int WM, int WN, int NSTAGE>
__global__ __launch_bounds__(WM * WN * 64) void gemm2_kernel(GemmP p) {
  extern __shared__ __attribute__((aligned(16))) char smem[];
  constexpr int NMT = S_ / BM;
  const int xq = blockIdx.x & 7, oq = blockIdx.x >> 3;
  const int mt = oq % NMT, nt = xq + 8 * (oq / NMT);
  if (nt >= DM / BN) return;
  const int n0 = nt * BN;
  if (blockIdx.y == 0) gemm_tile<BM, BN, WM, WN, NSTAGE>(smem, p.A0 + (size_t)mt * BM * HD, HD, p.B0 + n0, DM, HD, p.C0 + (size_t)mt * BM * DM + n0, DM, nullptr, 0, false);
  else gemm_tile<BM, BN, WM, WN, NSTAGE>(smem, p.A1 + (size_t)mt * BM * FF, FF, p.B1 + n0, DM, FF, p.C1 + (size_t)mt * BM * DM + n0, DM, p.bias + n0, 1, false);
}

__global__ __launch_bounds__(256) void combine_kernel(const u16* __restrict__ x, const u16* __restrict__ a, const u16* __restrict__ bm, u16* __restrict__ out) {
  const size_t i = ((size_t)blockIdx.x * 256 + threadIdx.x) * 8;
  const u32x4 xv = *(const u32x4*)(x + i), av = *(const u32x4*)(a + i), bv = *(const u32x4*)(bm + i);
  u32x4 o;
#pragma unroll
  for (int q = 0; q < 4; ++q) {
    const u32 s1 = cvtpk(lo2f(xv[q]) + lo2f(av[q]), hi2f(xv[q]) + hi2f(av[q]));
    o[q] = cvtpk(lo2f(s1) + lo2f(bv[q]), hi2f(s1) + hi2f(bv[q]));
  }
  *(u32x4*)(out + i) = o;
}

struct ChainP {
  const u16* mem0; const u16* Wq; const u16* Wk; const u16* Wv; const u16* Wo;
  const u16* Kx; const u16* Vx; u16* KM; u16* VM;
  u64* memg; u64* qkvg; u64* partg; u32* tmo;
};
constexpr int NG_MEM = 4096;
constexpr int NG_QKV = 192;
constexpr u32 SPIN_CAP = 400000u;
#define PIPE_POLL 0
#define RETRY_SLEEP 0
#define H1_DELAY 16
constexpr int NREP = 8;

__device__ __forceinline__ void st_gran(u64* g, u32 tag, u32 val) {
  __hip_atomic_store((gu64*)g, ((u64)tag << 32) | (u64)val, RLX_AGENT);
}
__device__ __forceinline__ u64 ld_gran(const u64* g) { return __hip_atomic_load((gu64*)g, RLX_AGENT); }
__device__ __forceinline__ void st_gran2(u64* g, u32 tag, u32 v0, u32 v1) {
  const u32x4 d = {v0, tag, v1, tag};
  asm volatile("global_store_dwordx4 %0, %1, off sc1\n\ts_nop 1" :: "v"((gu64*)g), "v"(d) : "memory");
}

__device__ __forceinline__ bool spin_fail(u32& spins, u32* tmo) {
  ++spins;
  if (spins > SPIN_CAP) { __hip_atomic_store((gu32*)tmo, 1u, RLX_AGENT); return true; }
  if ((spins & 1023u) == 0u) { if (__builtin_amdgcn_readfirstlane(__hip_atomic_load((gu32*)tmo, RLX_AGENT))) return true; }
  return false;
}
__device__ __forceinline__ bool wait_sentinel(const u64* g, bool active, u32 tag, u32* tmo) {
  for (u32 spins = 0;;) {
    bool ok = true;
    if (active) ok = ((u32)(ld_gran(g) >> 32) == tag);
    if (__all(ok)) return true;
    if (spin_fail(spins, tmo)) return false;
    __builtin_amdgcn_s_sleep(1);
  }
}
template <int N>
__device__ __forceinline__ bool sweep(const u64* g, int stride, u32 tag, u32 (&v)[N], u32* tmo) {
  for (u32 spins = 0;;) {
    bool ok = true;
#pragma unroll
    for (int k = 0; k < N; ++k) { const u64 x = ld_gran(g + (size_t)k * stride); v[k] = (u32)x; ok &= ((u32)(x >> 32) == tag); }
    if (__all(ok)) return true;
    if (spin_fail(spins, tmo)) return false;
  }
}
template <int N>
__device__ __forceinline__ bool sweep2(__amdgpu_buffer_rsrc_t rs, u32 byte_off, u32 byte_stride, u32 tag, u32 (&v)[2 * N], u32* tmo) {
  for (u32 spins = 0;;) {
    bool ok = true;
#pragma unroll
    for (int k = 0; k < N; ++k) {
      const u32x4 x = __builtin_bit_cast(u32x4, __builtin_amdgcn_raw_buffer_load_b128(rs, byte_off + k * byte_stride, 0, 16));
      v[2 * k] = x[0]; v[2 * k + 1] = x[2]; ok &= (x[1] == tag) & (x[3] == tag);
    }
    if (__all(ok)) return true;
    if (spin_fail(spins, tmo)) return false;
    __builtin_amdgcn_s_sleep(RETRY_SLEEP);
  }
}

#define STAMP(i) do { } while (0)
__global__ __launch_bounds__(256, 1) void chain_kernel(ChainP p) {
  constexpr int LM = 1032;
  constexpr int LQ = 136;
  constexpr int LV = 72;
  __shared__ __attribute__((aligned(16))) u16 memS[8 * LM];
  __shared__ __attribute__((aligned(16))) float red[4 * 8 * 48];
  __shared__ __attribute__((aligned(16))) u16 qS[8 * LQ];
  __shared__ __attribute__((aligned(16))) u16 kS[48 * LQ];
  __shared__ __attribute__((aligned(16))) u16 vT[128 * LV];

  const int t = threadIdx.x, lane = t & 63, w = __builtin_amdgcn_readfirstlane(t >> 6);
  const int b = blockIdx.x, xcd = b & 7, ib = b >> 3;
  const int h = xcd * 4 + (ib >> 3), j = ib & 7;
  const int R = h * 8 + j;
  const int fr = lane & 15, fq = lane >> 4;
  const __amdgpu_buffer_rsrc_t rs_mem = __builtin_amdgcn_make_buffer_rsrc((void*)p.memg, 0, 2 * NREP * NG_MEM * 8, 0x00020000);
  const __amdgpu_buffer_rsrc_t rs_qkv = __builtin_amdgcn_make_buffer_rsrc((void*)p.qkvg, 0, 2 * NH * 8 * NG_QKV * 8, 0x00020000);
  const __amdgpu_buffer_rsrc_t rs_part = __builtin_amdgcn_make_buffer_rsrc((void*)p.partg, 0, 2 * 256 * NH * 32 * 8, 0x00020000);

  bf16x8 wf[3][8];
  {
    const u16* Ws[3] = {p.Wq, p.Wk, p.Wv};
#pragma unroll
    for (int m = 0; m < 3; ++m)
#pragma unroll
      for (int s = 0; s < 8; ++s) {
        const u16* src = Ws[m] + (size_t)(256 * w + 32 * s + 8 * fq) * HD + h * DH + 16 * j + fr;
#pragma unroll
        for (int i = 0; i < 8; ++i) wf[m][s][i] = (short)src[(size_t)i * HD];
      }
  }
  bf16x8 wo[2][4];
#pragma unroll
  for (int c = 0; c < 2; ++c)
#pragma unroll
    for (int s = 0; s < 4; ++s)
#pragma unroll
      for (int i = 0; i < 8; ++i) {
        const int d = 32 * s + ((i < 4) ? (4 * fq + i) : (16 + 4 * fq + i - 4));
        wo[c][s][i] = (short)p.Wo[(size_t)(h * DH + d) * DM + 128 * j + 16 * (2 * w + c) + fr];
      }
  for (int e = t; e < 128 * LV; e += 256) vT[e] = 0;
  for (int e = t; e < 48 * LQ; e += 256) kS[e] = 0;
  u32x4 kpre[2], vpre[2];
#pragma unroll
  for (int i = 0; i < 2; ++i) {
    const int e = t + i * 256, r = e >> 4, c = (e & 15) * 8;
    kpre[i] = *(const u32x4*)(p.Kx + (size_t)r * HD + h * DH + c);
    vpre[i] = *(const u32x4*)(p.Vx + (size_t)r * HD + h * DH + c);
  }
  __syncthreads();

  for (int s = 0; s < NSEG; ++s) {
    const int par = s & 1;
    const u32 epoch = (u32)s + 1u;
    STAMP(0);
    if (s == 0) {
#pragma unroll
      for (int i = 0; i < 4; ++i) {
        const int e = t + i * 256, r = e >> 7, c = (e & 127) * 8;
        *(u32x4*)(&memS[r * LM + c]) = *(const u32x4*)(p.mem0 + r * DM + c);
      }
    } else {
      u32 v[16];
      const u32 gbase = (u32)(((par ^ 1) * NREP + (b & (NREP - 1))) * NG_MEM + w * 1024);
      __builtin_amdgcn_s_sleep(H1_DELAY);
      if (!sweep2<8>(rs_mem, (gbase + lane * 2) * 8, 128 * 8, (u32)s, v, p.tmo)) return;
#pragma unroll
      for (int k = 0; k < 8; ++k) {
        const int u = lane + k * 64;
        *(uint2*)(&memS[(u & 7) * LM + (w * 64 + (u >> 3)) * 4]) = make_uint2(v[2 * k], v[2 * k + 1]);
      }
    }
#pragma unroll
    for (int i = 0; i < 2; ++i) {
      const int e = t + i * 256, r = e >> 4, c = (e & 15) * 8;
      *(u32x4*)(&kS[r * LQ + c]) = kpre[i];
      const u16* vp = (const u16*)&vpre[i];
      const int pos = (r < 16) ? (8 * (r >> 2) + (r & 3)) : (8 * ((r - 16) >> 2) + 4 + (r & 3));
#pragma unroll
      for (int q = 0; q < 8; ++q) vT[(c + q) * LV + pos] = vp[q];
    }
    __syncthreads();
    STAMP(1);
    {
      f32x4 acc[3] = {{0.f, 0.f, 0.f, 0.f}, {0.f, 0.f, 0.f, 0.f}, {0.f, 0.f, 0.f, 0.f}};
      bf16x8 a[8];
#pragma unroll
      for (int ks = 0; ks < 8; ++ks) a[ks] = *(const bf16x8*)(&memS[(lane & 7) * LM + 256 * w + 32 * ks + 8 * fq]);
#pragma unroll
      for (int ks = 0; ks < 8; ++ks)
#pragma unroll
        for (int m = 0; m < 3; ++m) acc[m] = __builtin_amdgcn_mfma_f32_16x16x32_bf16(a[ks], wf[m][ks], acc[m], 0, 0, 0);
      if (fq < 2) {
#pragma unroll
        for (int m = 0; m < 3; ++m)
#pragma unroll
          for (int r = 0; r < 4; ++r) red[(w * 8 + fq * 4 + r) * 48 + m * 16 + fr] = acc[m][r];
      }
    }
    __syncthreads();
    if (t < NG_QKV) {
      const int r = t / 24, cp = t % 24;
      float v0 = 0.f, v1 = 0.f;
#pragma unroll
      for (int ww = 0; ww < 4; ++ww) { const float2 q2 = *(const float2*)(&red[(ww * 8 + r) * 48 + cp * 2]); v0 += q2.x; v1 += q2.y; }
      const u32 pk = cvtpk(v0, v1);
      const int m = cp >> 3, c = (cp & 7) * 2;
      if (s < NSEG - 1) st_gran(p.qkvg + ((size_t)(par * NH + h) * 8 + j) * NG_QKV + t, epoch, pk);
      if (m == 1) *(u32*)(p.KM + (size_t)(s * MM + r) * HD + h * DH + 16 * j + c) = pk;
      if (m == 2) *(u32*)(p.VM + (size_t)(s * MM + r) * HD + h * DH + 16 * j + c) = pk;
    }
    STAMP(2);
    if (s == NSEG - 1) break;
    bf16x8 kx[2][4], vx[8];
#pragma unroll
    for (int ks = 0; ks < 4; ++ks)
#pragma unroll
      for (int n = 0; n < 2; ++n) kx[n][ks] = *(const bf16x8*)(&kS[(n * 16 + fr) * LQ + 32 * ks + 8 * fq]);
#pragma unroll
    for (int c = 0; c < 8; ++c) vx[c] = *(const bf16x8*)(&vT[(16 * c + fr) * LV + 8 * fq]);
    __builtin_amdgcn_sched_barrier(0);
    {
      u32 v[6];
      const u32 gbase = (u32)(((par * NH + h) * 8 + 2 * w) * NG_QKV);
      if (!sweep2<3>(rs_qkv, (gbase + lane * 2) * 8, 128 * 8, epoch, v, p.tmo)) return;
#pragma unroll
      for (int k = 0; k < 3; ++k) {
        const int g = (lane + k * 64) * 2, jj = 2 * w + g / NG_QKV, e = g % NG_QKV;
        const int r = e / 24, cp = e % 24, m = cp >> 3, c = 16 * jj + (cp & 7) * 2;
        if (m == 0) *(uint2*)(&qS[r * LQ + c]) = make_uint2(v[2 * k], v[2 * k + 1]);
        else if (m == 1) *(uint2*)(&kS[(32 + r) * LQ + c]) = make_uint2(v[2 * k], v[2 * k + 1]);
        else {
          const int pos = 32 + 8 * (r >> 2) + (r & 3);
          vT[c * LV + pos] = (u16)(v[2 * k] & 0xffffu); vT[(c + 1) * LV + pos] = (u16)(v[2 * k] >> 16);
          vT[(c + 2) * LV + pos] = (u16)(v[2 * k + 1] & 0xffffu); vT[(c + 3) * LV + pos] = (u16)(v[2 * k + 1] >> 16);
        }
      }
    }
    __syncthreads();
    STAMP(3);
    {
      f32x4 sc[3] = {{0.f, 0.f, 0.f, 0.f}, {0.f, 0.f, 0.f, 0.f}, {0.f, 0.f, 0.f, 0.f}};
      bf16x8 qf[4], km[4];
      uint2 vm[8];
#pragma unroll
      for (int ks = 0; ks < 4; ++ks) {
        qf[ks] = *(const bf16x8*)(&qS[(lane & 7) * LQ + 32 * ks + 8 * fq]);
        km[ks] = *(const bf16x8*)(&kS[(32 + fr) * LQ + 32 * ks + 8 * fq]);
      }
#pragma unroll
      for (int c = 0; c < 8; ++c) vm[c] = *(const uint2*)(&vT[(16 * c + fr) * LV + 32 + 8 * fq]);
      __builtin_amdgcn_sched_barrier(0);
#pragma unroll
      for (int ks = 0; ks < 4; ++ks) {
        sc[0] = __builtin_amdgcn_mfma_f32_16x16x32_bf16(kx[0][ks], qf[ks], sc[0], 0, 0, 0);
        sc[1] = __builtin_amdgcn_mfma_f32_16x16x32_bf16(kx[1][ks], qf[ks], sc[1], 0, 0, 0);
        sc[2] = __builtin_amdgcn_mfma_f32_16x16x32_bf16(km[ks], qf[ks], sc[2], 0, 0, 0);
      }
      const float scale = 0.08838834764831845f;
      const bool live2 = fq < 2;
      float xs[3][4];
#pragma unroll
      for (int n = 0; n < 3; ++n)
#pragma unroll
        for (int r = 0; r < 4; r += 2) {
          const u32 a = cvtpk(sc[n][r], sc[n][r + 1]);
          const u32 c2 = cvtpk(lo2f(a) * scale, hi2f(a) * scale);
          xs[n][r] = lo2f(c2); xs[n][r + 1] = hi2f(c2);
        }
      if (!live2) { xs[2][0] = xs[2][1] = xs[2][2] = xs[2][3] = -3.0e38f; }
      float mx = fmaxf(fmaxf(fmaxf(xs[0][0], xs[0][1]), fmaxf(xs[0][2], xs[0][3])), fmaxf(fmaxf(xs[1][0], xs[1][1]), fmaxf(xs[1][2], xs[1][3])));
      mx = fmaxf(mx, fmaxf(fmaxf(xs[2][0], xs[2][1]), fmaxf(xs[2][2], xs[2][3])));
      mx = xrow_max(mx);
      float sm = 0.f;
#pragma unroll
      for (int n = 0; n < 3; ++n)
#pragma unroll
        for (int r = 0; r < 4; ++r) { xs[n][r] = __expf(xs[n][r] - mx); sm += xs[n][r]; }
      sm = xrow_sum(sm);
      const float inv = 1.0f / sm;
      u32x4 pw0, pw1;
      pw0[0] = cvtpk(xs[0][0] * inv, xs[0][1] * inv); pw0[1] = cvtpk(xs[0][2] * inv, xs[0][3] * inv);
      pw0[2] = cvtpk(xs[1][0] * inv, xs[1][1] * inv); pw0[3] = cvtpk(xs[1][2] * inv, xs[1][3] * inv);
      pw1[0] = cvtpk(xs[2][0] * inv, xs[2][1] * inv); pw1[1] = cvtpk(xs[2][2] * inv, xs[2][3] * inv);
      pw1[2] = 0u; pw1[3] = 0u;
      const bf16x8 pf0 = __builtin_bit_cast(bf16x8, pw0), pf1 = __builtin_bit_cast(bf16x8, pw1);
      u32x4 ow[4];
#pragma unroll
      for (int c = 0; c < 8; ++c) {
        const u32x4 vmw = {vm[c].x, vm[c].y, 0u, 0u};
        f32x4 oc = {0.f, 0.f, 0.f, 0.f};
        oc = __builtin_amdgcn_mfma_f32_16x16x32_bf16(vx[c], pf0, oc, 0, 0, 0);
        oc = __builtin_amdgcn_mfma_f32_16x16x32_bf16(__builtin_bit_cast(bf16x8, vmw), pf1, oc, 0, 0, 0);
        ow[c >> 1][(c & 1) * 2] = cvtpk(oc[0], oc[1]); ow[c >> 1][(c & 1) * 2 + 1] = cvtpk(oc[2], oc[3]);
      }
      f32x4 pc[2] = {{0.f, 0.f, 0.f, 0.f}, {0.f, 0.f, 0.f, 0.f}};
#pragma unroll
      for (int ks = 0; ks < 4; ++ks)
#pragma unroll
        for (int c = 0; c < 2; ++c) pc[c] = __builtin_amdgcn_mfma_f32_16x16x32_bf16(wo[c][ks], __builtin_bit_cast(bf16x8, ow[ks]), pc[c], 0, 0, 0);
      if (fr < 8) {
#pragma unroll
        for (int c = 0; c < 2; ++c) {
          u64* dst = p.partg + ((size_t)(par * 256 + 32 * j + 8 * w + 4 * c + fq) * NH + h) * 32 + fr * 4;
          st_gran2(dst, epoch, __float_as_uint(pc[c][0]), __float_as_uint(pc[c][1]));
          st_gran2(dst + 2, epoch, __float_as_uint(pc[c][2]), __float_as_uint(pc[c][3]));
        }
      }
    }
    STAMP(4);
    __syncthreads();
#pragma unroll
    for (int i = 0; i < 2; ++i) {
      const int e = t + i * 256, r = e >> 4, c = (e & 15) * 8;
      kpre[i] = *(const u32x4*)(p.Kx + (size_t)((s + 1) * SEG + r) * HD + h * DH + c);
      vpre[i] = *(const u32x4*)(p.Vx + (size_t)((s + 1) * SEG + r) * HD + h * DH + c);
    }
    if (w == 0) {
      u32 v[16];
      const u32 gbase = (u32)(((par * 256 + R) * NH + 8 * fq) * 32 + fr * 2);
      if (!sweep2<8>(rs_part, gbase * 8, 32 * 8, epoch, v, p.tmo)) return;
      float f0 = 0.f, f1 = 0.f;
#pragma unroll
      for (int k = 0; k < 8; ++k) { f0 += __uint_as_float(v[2 * k]); f1 += __uint_as_float(v[2 * k + 1]); }
      f0 = xrow_sum(f0); f1 = xrow_sum(f1);
      const u32 pk = cvtpk(f0, f1);
#pragma unroll
      for (int q = 0; q < NREP / 4; ++q) st_gran(p.memg + (size_t)(par * NREP + q * 4 + fq) * NG_MEM + R * 16 + fr, epoch, pk);
      STAMP(5);
    }
    __syncthreads();
  }
}

__global__ __launch_bounds__(256) void attn_x_kernel(const u16* __restrict__ Q, const u16* __restrict__ Kx, const u16* __restrict__ Vx,
                                                     const u16* __restrict__ KM, const u16* __restrict__ VM, u16* __restrict__ O) {
  constexpr int LVR = 136;
  __shared__ __attribute__((aligned(16))) u16 vS[4][48 * LVR];
  const int t = threadIdx.x, lane = t & 63, w = __builtin_amdgcn_readfirstlane(t >> 6);
  const int item = blockIdx.x * 4 + w, s = item >> 5, h = item & 31;
  const int fr = lane & 15, fq = lane >> 4;
  u16* vw = vS[w];
  const size_t xoff = (size_t)(s * SEG) * HD + h * DH, moff = (size_t)(s * MM) * HD + h * DH;
#pragma unroll
  for (int i = 0; i < 12; ++i) {
    const int e = lane + i * 64, r = e >> 4, c = (e & 15) * 8;
    u32x4 v = {0u, 0u, 0u, 0u};
    if (r < 32) v = *(const u32x4*)(Vx + xoff + (size_t)r * HD + c);
    else if (r < 40) v = *(const u32x4*)(VM + moff + (size_t)(r - 32) * HD + c);
    *(u32x4*)(&vw[r * LVR + c]) = v;
  }
  bf16x8 qf[2][4], kf[3][4];
#pragma unroll
  for (int ks = 0; ks < 4; ++ks) {
#pragma unroll
    for (int n = 0; n < 2; ++n) {
      qf[n][ks] = *(const bf16x8*)(Q + xoff + (size_t)(16 * n + fr) * HD + 32 * ks + 8 * fq);
      kf[n][ks] = *(const bf16x8*)(Kx + xoff + (size_t)(16 * n + fr) * HD + 32 * ks + 8 * fq);
    }
    kf[2][ks] = *(const bf16x8*)(KM + moff + (size_t)(fr & 7) * HD + 32 * ks + 8 * fq);
  }
  asm volatile("s_waitcnt lgkmcnt(0)" ::: "memory");
  __builtin_amdgcn_wave_barrier();
  bf16x8 vf[8][2];
  const int tq = fr >> 2, tp = fr & 3;
#pragma unroll
  for (int c = 0; c < 8; ++c) {
    const s16x4 a0 = __builtin_amdgcn_ds_read_tr16_b64_v4i16((LAS s16x4*)(&vw[(4 * fq + tq) * LVR + 16 * c + 4 * tp]));
    const s16x4 a1 = __builtin_amdgcn_ds_read_tr16_b64_v4i16((LAS s16x4*)(&vw[(16 + 4 * fq + tq) * LVR + 16 * c + 4 * tp]));
    const s16x4 a2 = __builtin_amdgcn_ds_read_tr16_b64_v4i16((LAS s16x4*)(&vw[(32 + 4 * fq + tq) * LVR + 16 * c + 4 * tp]));
    vf[c][0] = (bf16x8){a0[0], a0[1], a0[2], a0[3], a1[0], a1[1], a1[2], a1[3]};
    vf[c][1] = (bf16x8){a2[0], a2[1], a2[2], a2[3], 0, 0, 0, 0};
  }
  const float scale = 0.08838834764831845f;
#pragma unroll
  for (int qt = 0; qt < 2; ++qt) {
    f32x4 sc[3] = {{0.f, 0.f, 0.f, 0.f}, {0.f, 0.f, 0.f, 0.f}, {0.f, 0.f, 0.f, 0.f}};
#pragma unroll
    for (int ks = 0; ks < 4; ++ks)
#pragma unroll
      for (int n = 0; n < 3; ++n) sc[n] = __builtin_amdgcn_mfma_f32_16x16x32_bf16(kf[n][ks], qf[qt][ks], sc[n], 0, 0, 0);
    const int qi = 16 * qt + fr;
    float xs[3][4];
#pragma unroll
    for (int n = 0; n < 3; ++n)
#pragma unroll
      for (int r = 0; r < 4; r += 2) {
        const u32 a = cvtpk(sc[n][r], sc[n][r + 1]);
        const u32 c2 = cvtpk(lo2f(a) * scale, hi2f(a) * scale);
        xs[n][r] = lo2f(c2); xs[n][r + 1] = hi2f(c2);
      }
    float mx = -3.0e38f;
#pragma unroll
    for (int n = 0; n < 3; ++n)
#pragma unroll
      for (int r = 0; r < 4; ++r) {
        const bool live = (n < 2) ? (16 * n + 4 * fq + r <= qi) : (fq < 2);
        if (!live) xs[n][r] = -3.0e38f;
        mx = fmaxf(mx, xs[n][r]);
      }
    mx = xrow_max(mx);
    float sm = 0.f;
#pragma unroll
    for (int n = 0; n < 3; ++n)
#pragma unroll
      for (int r = 0; r < 4; ++r) { xs[n][r] = __expf(xs[n][r] - mx); sm += xs[n][r]; }
    sm = xrow_sum(sm);
    const float inv = 1.0f / sm;
    u32x4 pw0, pw1;
    pw0[0] = cvtpk(xs[0][0] * inv, xs[0][1] * inv); pw0[1] = cvtpk(xs[0][2] * inv, xs[0][3] * inv);
    pw0[2] = cvtpk(xs[1][0] * inv, xs[1][1] * inv); pw0[3] = cvtpk(xs[1][2] * inv, xs[1][3] * inv);
    pw1[0] = cvtpk(xs[2][0] * inv, xs[2][1] * inv); pw1[1] = cvtpk(xs[2][2] * inv, xs[2][3] * inv);
    pw1[2] = 0u; pw1[3] = 0u;
    const bf16x8 pf0 = __builtin_bit_cast(bf16x8, pw0), pf1 = __builtin_bit_cast(bf16x8, pw1);
    u16* orow = O + xoff + (size_t)qi * HD + 4 * fq;
#pragma unroll
    for (int c = 0; c < 8; ++c) {
      f32x4 oc = {0.f, 0.f, 0.f, 0.f};
      oc = __builtin_amdgcn_mfma_f32_16x16x32_bf16(vf[c][0], pf0, oc, 0, 0, 0);
      oc = __builtin_amdgcn_mfma_f32_16x16x32_bf16(vf[c][1], pf1, oc, 0, 0, 0);
      *(uint2*)(orow + 16 * c) = make_uint2(cvtpk(oc[0], oc[1]), cvtpk(oc[2], oc[3]));
    }
  }
}

#define G1_BM 256
#define G1_BN 256
#define G1_WM 2
#define G1_WN 4
#define G1_NS 2
#define G2_BM 128
#define G2_BN 128
#define G2_WM 4
#define G2_WN 2
#define G2_NS 4
#define G1_CFG G1_BM, G1_BN, G1_WM, G1_WN, G1_NS
#define G2_CFG G2_BM, G2_BN, G2_WM, G2_WN, G2_NS
#define G1_THREADS (G1_WM * G1_WN * 64)
#define G2_THREADS (G2_WM * G2_WN * 64)
extern "C" void kernel_launch(void* const* d_in, const int* in_sizes, int n_in,
                              void* d_out, int out_size, void* d_ws, size_t ws_size,
                              hipStream_t stream) {
  const u16* x = (const u16*)d_in[0];
  const u16* mem0 = (const u16*)d_in[1];
  const u16* Wq = (const u16*)d_in[2];
  const u16* Wk = (const u16*)d_in[3];
  const u16* Wv = (const u16*)d_in[4];
  const u16* Wo = (const u16*)d_in[5];
  const u16* w1 = (const u16*)d_in[8];
  const u16* b1 = (const u16*)d_in[9];
  const u16* w2 = (const u16*)d_in[10];
  const u16* b2 = (const u16*)d_in[11];
  u16* out = (u16*)d_out;

  char* ws = (char*)d_ws;
  size_t off = 0;
  auto carve = [&](size_t bytes) { char* q = ws + off; off += (bytes + 255) & ~(size_t)255; return q; };
  u64* memg = (u64*)carve((size_t)2 * NREP * NG_MEM * 8);
  u64* qkvg = (u64*)carve((size_t)2 * NH * 8 * NG_QKV * 8);
  u64* partg = (u64*)carve((size_t)2 * 256 * NH * 32 * 8);
  u32* tmo = (u32*)carve(256);
  const size_t sync_bytes = off;
  u16* xn = (u16*)carve((size_t)S_ * DM * 2);
  u16* Qx = (u16*)carve((size_t)S_ * HD * 2);
  u16* Kx = (u16*)carve((size_t)S_ * HD * 2);
  u16* Vx = (u16*)carve((size_t)S_ * HD * 2);
  u16* Hb = (u16*)carve((size_t)S_ * FF * 2);
  u16* Ox = (u16*)carve((size_t)S_ * HD * 2);
  u16* KM = (u16*)carve((size_t)NSEG * MM * HD * 2);
  u16* VM = (u16*)carve((size_t)NSEG * MM * HD * 2);
  u16* Bm = (u16*)carve((size_t)S_ * DM * 2);
  u16* Am = (u16*)carve((size_t)S_ * DM * 2);

  hipMemsetAsync(ws, 0, sync_bytes, stream);
  ln_kernel<<<S_, 256, 0, stream>>>(x, xn);
  {
    GemmP g{xn, nullptr, Wq, Wk, Wv, w1, Qx, Kx, Vx, Hb, b1};
    constexpr int LDS1 = gemm_lds(G1_CFG);
    constexpr int NT1 = (15872 / G1_BN + 7) / 8 * 8 * (S_ / G1_BM);
    (void)hipFuncSetAttribute((const void*)gemm1_kernel<G1_CFG>, hipFuncAttributeMaxDynamicSharedMemorySize, LDS1);
    gemm1_kernel<G1_CFG><<<dim3(NT1), G1_THREADS, LDS1, stream>>>(g);
  }
  {
    ChainP c{mem0, Wq, Wk, Wv, Wo, Kx, Vx, KM, VM, memg, qkvg, partg, tmo};
    chain_kernel<<<256, 256, 0, stream>>>(c);
  }
  attn_x_kernel<<<NSEG * NH / 4, 256, 0, stream>>>(Qx, Kx, Vx, KM, VM, Ox);
  {
    GemmP g{Ox, Hb, Wo, w2, nullptr, nullptr, Am, Bm, nullptr, nullptr, b2};
    constexpr int LDS2 = gemm_lds(G2_CFG);
    constexpr int NT2 = (DM / G2_BN + 7) / 8 * 8 * (S_ / G2_BM);
    (void)hipFuncSetAttribute((const void*)gemm2_kernel<G2_CFG>, hipFuncAttributeMaxDynamicSharedMemorySize, LDS2);
    gemm2_kernel<G2_CFG><<<dim3(NT2, 2), G2_THREADS, LDS2, stream>>>(g);
  }
  combine_kernel<<<S_ * DM / 8 / 256, 256, 0, stream>>>(x, Am, Bm, out);
}
```

```cpp
#include <hip/hip_runtime.h>
#include <hip/hip_bf16.h>
#include <stdint.h>

typedef unsigned short u16;
typedef unsigned int u32;
typedef unsigned long long u64;
#define GAS __attribute__((address_space(1)))
#define LAS __attribute__((address_space(3)))
typedef GAS u64 gu64;
typedef GAS u32 gu32;
typedef __attribute__((ext_vector_type(8))) short bf16x8;
typedef __attribute__((ext_vector_type(4))) short s16x4;
typedef __attribute__((ext_vector_type(4))) float f32x4;
typedef __attribute__((ext_vector_type(4))) unsigned int u32x4;
#define RLX_AGENT __ATOMIC_RELAXED, __HIP_MEMORY_SCOPE_AGENT

constexpr int S_ = 2048, DM = 1024, NH = 32, DH = 128, HD = 4096, MM = 8, SEG = 32, NSEG = 64, FF = 3584;

__device__ __forceinline__ float bf2f(u16 v) { return __uint_as_float(((u32)v) << 16); }
__device__ __forceinline__ u16 f2bf(float f) { u32 u = __float_as_uint(f); u += 0x7fffu + ((u >> 16) & 1u); return (u16)(u >> 16); }
__device__ __forceinline__ float rbf(float f) { return bf2f(f2bf(f)); }
typedef __bf16 hbf2 __attribute__((ext_vector_type(2)));
typedef float f32x2 __attribute__((ext_vector_type(2)));
__device__ __forceinline__ u32 cvtpk(float a, float b) { f32x2 v = {a, b}; return __builtin_bit_cast(u32, __builtin_convertvector(v, hbf2)); }
__device__ __forceinline__ float lo2f(u32 v) { return __uint_as_float(v << 16); }
__device__ __forceinline__ float hi2f(u32 v) { return __uint_as_float(v & 0xffff0000u); }

__device__ __forceinline__ float xrow_max(float v) {
  auto a = __builtin_amdgcn_permlane16_swap(__float_as_uint(v), __float_as_uint(v), false, false);
  v = fmaxf(__uint_as_float(a[0]), __uint_as_float(a[1]));
  auto c = __builtin_amdgcn_permlane32_swap(__float_as_uint(v), __float_as_uint(v), false, false);
  return fmaxf(__uint_as_float(c[0]), __uint_as_float(c[1]));
}
__device__ __forceinline__ float xrow_sum(float v) {
  auto a = __builtin_amdgcn_permlane16_swap(__float_as_uint(v), __float_as_uint(v), false, false);
  v = __uint_as_float(a[0]) + __uint_as_float(a[1]);
  auto c = __builtin_amdgcn_permlane32_swap(__float_as_uint(v), __float_as_uint(v), false, false);
  return __uint_as_float(c[0]) + __uint_as_float(c[1]);
}
__device__ __forceinline__ float ror8_sum(float v) {
  return v + __builtin_bit_cast(float, __builtin_amdgcn_update_dpp(0, __builtin_bit_cast(int, v), 0x128, 0xf, 0xf, false));
}

__global__ __launch_bounds__(256) void ln_kernel(const u16* __restrict__ x, u16* __restrict__ xn) {
  __shared__ float red[8];
  const int row = blockIdx.x, t = threadIdx.x, lane = t & 63, w = t >> 6;
  const ushort4 v = *(const ushort4*)(x + (size_t)row * DM + t * 4);
  float f0 = bf2f(v.x), f1 = bf2f(v.y), f2 = bf2f(v.z), f3 = bf2f(v.w);
  float s = (f0 + f1) + (f2 + f3);
#pragma unroll
  for (int m = 32; m >= 1; m >>= 1) s += __shfl_xor(s, m);
  if (lane == 0) red[w] = s;
  __syncthreads();
  const float mean = ((red[0] + red[1]) + (red[2] + red[3])) * (1.0f / DM);
  float d0 = f0 - mean, d1 = f1 - mean, d2 = f2 - mean, d3 = f3 - mean;
  float q = (d0 * d0 + d1 * d1) + (d2 * d2 + d3 * d3);
#pragma unroll
  for (int m = 32; m >= 1; m >>= 1) q += __shfl_xor(q, m);
  if (lane == 0) red[4 + w] = q;
  __syncthreads();
  const float var = ((red[4] + red[5]) + (red[6] + red[7])) * (1.0f / DM);
  const float mb = rbf(mean), vb = rbf(var);
  const float den = rbf(sqrtf(rbf(vb + 1e-5f)));
  ushort4 o;
  o.x = f2bf(rbf(f0 - mb) / den); o.y = f2bf(rbf(f1 - mb) / den);
  o.z = f2bf(rbf(f2 - mb) / den); o.w = f2bf(rbf(f3 - mb) / den);
  *(ushort4*)(xn + (size_t)row * DM + t * 4) = o;
}

__device__ __forceinline__ float gelu_tanh(float x) {
  const float k0 = 0.7978845608028654f, k1 = 0.044715f;
  const float u = k0 * (x + k1 * x * x * x);
  return 0.5f * x * (1.0f + tanhf(u));
}
__device__ __forceinline__ void glds16(const void* gsrc, u32 lds_dst) {
  u32 keep;
  asm volatile("s_mov_b32 %0, m0\n\ts_mov_b32 m0, %2\n\ts_nop 0\n\tglobal_load_lds_dwordx4 %1, off\n\ts_mov_b32 m0, %0"
               : "=&s"(keep) : "v"(gsrc), "s"(lds_dst) : "memory");
}
template <int N> __device__ __forceinline__ void wait_vm() { asm volatile("s_waitcnt vmcnt(%0)" :: "n"(N) : "memory"); }
__device__ __forceinline__ void st16_sc1(void* p, u32x4 v) {
  asm volatile("global_store_dwordx4 %0, %1, off sc1\n\ts_nop 1" :: "v"(p), "v"(v) : "memory");
}
constexpr int gemm_lds(int BM, int BN, int WM, int WN, int NSTAGE) {
  const int st = NSTAGE * (BM * 64 * 2 + 64 * BN * 2), ep = WM * WN * (BM / WM) * (BN / WN + 8) * 2;
  return st > ep ? st : ep;
}
template <int BM, int BN, int WM, int WN, int NSTAGE>
__device__ __forceinline__ void gemm_tile(char* smem, const u16* A, int lda, const u16* B, int ldb, int K,
                                          u16* C, int ldc, const u16* bias, int EPI, bool WT) {
  constexpr int NW = WM * WN, NT = NW * 64, BK = 64;
  constexpr int TM = BM / WM / 16, TN = BN / WN / 16;
  constexpr int A_BYTES = BM * BK * 2, B_BYTES = BK * BN * 2, STAGE = A_BYTES + B_BYTES;
  constexpr int NSUB = BN / 16;
  const int t = threadIdx.x, lane = t & 63, w = __builtin_amdgcn_readfirstlane(t >> 6);
  const int wr = w / WN, wc = w % WN, fr = lane & 15, fq = lane >> 4;
  constexpr int AU = BM * 8 / NT, BU = BK * BN / 8 / NT, LPS = AU + BU;
  const u16* asrc[AU]; const u16* bsrc[BU];
#pragma unroll
  for (int i = 0; i < AU; ++i) {
    const int u = i * NT + t, row = u >> 3, c = (u & 7) ^ (row & 7);
    asrc[i] = A + (size_t)row * lda + c * 8;
  }
#pragma unroll
  for (int i = 0; i < BU; ++i) {
    const int u = i * NT + t, hf = u & 1, rp = (u >> 1) & 7, st = u >> 4, kb = st / NSUB, nb = st % NSUB;
    const int r = rp ^ (4 * (kb & 1));
    bsrc[i] = B + (size_t)(kb * 8 + r) * ldb + nb * 16 + hf * 8;
  }
  const u32 lds0 = (u32)(uintptr_t)(LAS char*)smem + (u32)w * 1024u;
  auto stage = [&](int kt, int buf) {
    const u32 base = lds0 + buf * STAGE;
#pragma unroll
    for (int i = 0; i < AU; ++i) glds16(asrc[i] + kt * BK, base + i * NT * 16);
#pragma unroll
    for (int i = 0; i < BU; ++i) glds16(bsrc[i] + (size_t)kt * BK * ldb, base + A_BYTES + i * NT * 16);
  };
  f32x4 acc[TM][TN];
#pragma unroll
  for (int i = 0; i < TM; ++i)
#pragma unroll
    for (int j = 0; j < TN; ++j) acc[i][j] = (f32x4){0.f, 0.f, 0.f, 0.f};
  const int arow0 = (wr * (BM / WM) + fr) * 128;
  const int arx = fr & 7;
  const int tq = (lane & 15) >> 2, tp = lane & 3;
  const int nk = K / BK;
  wait_vm<0>();
#pragma unroll
  for (int k = 0; k < NSTAGE - 1; ++k) stage(k, k);
  int buf = 0;
  for (int kt = 0; kt < nk; ++kt) {
    if (kt + NSTAGE - 1 <= nk) wait_vm<LPS * (NSTAGE - 2)>(); else wait_vm<0>();
    asm volatile("s_waitcnt lgkmcnt(0)" ::: "memory");
    __builtin_amdgcn_s_barrier();
    asm volatile("" ::: "memory");
    if (kt + NSTAGE - 1 < nk) { int nb2 = buf + NSTAGE - 1; if (nb2 >= NSTAGE) nb2 -= NSTAGE; stage(kt + NSTAGE - 1, nb2); }
    const char* sa = smem + buf * STAGE;
    const char* sb = sa + A_BYTES;
#pragma unroll
    for (int ks = 0; ks < 2; ++ks) {
      bf16x8 af[TM], bfr[TN];
#pragma unroll
      for (int i = 0; i < TM; ++i) af[i] = *(const bf16x8*)(sa + arow0 + i * 2048 + (((4 * ks + fq) ^ arx) * 16));
      const int kb = 4 * ks + fq;
      const int rsw = 4 * (kb & 1);
#pragma unroll
      for (int j = 0; j < TN; ++j) {
        const int nb = wc * TN + j;
        const char* sub = sb + (kb * NSUB + nb) * 256 + tp * 8;
        const s16x4 lo = __builtin_amdgcn_ds_read_tr16_b64_v4i16((LAS s16x4*)(sub + ((tq) ^ rsw) * 32));
        const s16x4 hi = __builtin_amdgcn_ds_read_tr16_b64_v4i16((LAS s16x4*)(sub + ((4 + tq) ^ rsw) * 32));
        bfr[j] = (bf16x8){lo[0], lo[1], lo[2], lo[3], hi[0], hi[1], hi[2], hi[3]};
      }
#pragma unroll
      for (int i = 0; i < TM; ++i)
#pragma unroll
        for (int j = 0; j < TN; ++j) acc[i][j] = __builtin_amdgcn_mfma_f32_16x16x32_bf16(bfr[j], af[i], acc[i][j], 0, 0, 0);
    }
    if (++buf == NSTAGE) buf = 0;
  }
  asm volatile("s_waitcnt lgkmcnt(0)" ::: "memory");
  __builtin_amdgcn_s_barrier();
  asm volatile("" ::: "memory");
  constexpr int WROWS = BM / WM, WCOLS = BN / WN, LDW = WCOLS + 8;
  u16* ew = (u16*)smem + w * WROWS * LDW;
#pragma unroll
  for (int j = 0; j < TN; ++j) {
    float bv[4] = {0.f, 0.f, 0.f, 0.f};
    if (EPI) {
      const ushort4 bb = *(const ushort4*)(bias + wc * WCOLS + j * 16 + fq * 4);
      bv[0] = bf2f(bb.x); bv[1] = bf2f(bb.y); bv[2] = bf2f(bb.z); bv[3] = bf2f(bb.w);
    }
#pragma unroll
    for (int i = 0; i < TM; ++i) {
      float v[4];
#pragma unroll
      for (int r = 0; r < 4; ++r) v[r] = acc[i][j][r];
      if (EPI) {
        const u32 a01 = cvtpk(v[0], v[1]), a23 = cvtpk(v[2], v[3]);
        v[0] = lo2f(a01) + bv[0]; v[1] = hi2f(a01) + bv[1]; v[2] = lo2f(a23) + bv[2]; v[3] = hi2f(a23) + bv[3];
        if (EPI == 2) {
          const u32 b01 = cvtpk(v[0], v[1]), b23 = cvtpk(v[2], v[3]);
          v[0] = gelu_tanh(lo2f(b01)); v[1] = gelu_tanh(hi2f(b01)); v[2] = gelu_tanh(lo2f(b23)); v[3] = gelu_tanh(hi2f(b23));
        }
      }
      *(uint2*)(&ew[(i * 16 + fr) * LDW + j * 16 + fq * 4]) = make_uint2(cvtpk(v[0], v[1]), cvtpk(v[2], v[3]));
    }
  }
  asm volatile("s_waitcnt lgkmcnt(0)" ::: "memory");
  __builtin_amdgcn_wave_barrier();
  constexpr int CPR = WCOLS / 8;
#pragma unroll 4
  for (int e = lane; e < WROWS * CPR; e += 64) {
    const int r = e / CPR, c = e % CPR;
    const u32x4 v = *(const u32x4*)(&ew[r * LDW + c * 8]);
    u16* dst = C + (size_t)(wr * WROWS + r) * ldc + wc * WCOLS + c * 8;
    if (WT) st16_sc1(dst, v); else *(u32x4*)dst = v;
  }
  asm volatile("s_waitcnt lgkmcnt(0)" ::: "memory");
  __builtin_amdgcn_s_barrier();
  asm volatile("" ::: "memory");
}

__global__ __launch_bounds__(256) void gemm_wo_kernel(const u16* __restrict__ Ox, const u16* __restrict__ Wo, u16* __restrict__ Am) {
  extern __shared__ __attribute__((aligned(16))) char smem[];
  const int xq = blockIdx.x & 7, oq = blockIdx.x >> 3;
  const int mt = oq & 15, nt = xq + 8 * (oq >> 4);
  gemm_tile<128, 64, 2, 2, 4>(smem, Ox + (size_t)mt * 128 * HD, HD, Wo + nt * 64, DM, HD, Am + (size_t)mt * 128 * DM + nt * 64, DM, nullptr, 0, false);
}

__global__ __launch_bounds__(256) void combine_kernel(const u16* __restrict__ x, const u16* __restrict__ a, const u16* __restrict__ bm, u16* __restrict__ out) {
  const size_t i = ((size_t)blockIdx.x * 256 + threadIdx.x) * 8;
  const u32x4 xv = *(const u32x4*)(x + i), av = *(const u32x4*)(a + i), bv = *(const u32x4*)(bm + i);
  u32x4 o;
#pragma unroll
  for (int q = 0; q < 4; ++q) {
    const u32 s1 = cvtpk(lo2f(xv[q]) + lo2f(av[q]), hi2f(xv[q]) + hi2f(av[q]));
    o[q] = cvtpk(lo2f(s1) + lo2f(bv[q]), hi2f(s1) + hi2f(bv[q]));
  }
  *(u32x4*)(out + i) = o;
}

struct FusedP {
  const u16* xn; const u16* mem0; const u16* Wq; const u16* Wk; const u16* Wv; const u16* Wo;
  const u16* w1; const u16* b1; const u16* w2; const u16* b2;
  u16* Qx; u16* Kx; u16* Vx; u16* Hb; u16* Bm; u16* KM; u16* VM;
  u64* memg; u64* qkvg; u64* partg; u32* kvflag; u32* ctl;
};
constexpr int NG_MEM = 4096;
constexpr int NG_QKV = 384;
constexpr u32 SPIN_CAP = 2000000u;
constexpr int NREP = 8;
#define H3W 4
#define H1_DELAY 8

__device__ __forceinline__ void st_gran(u64* g, u32 tag, u32 val) {
  __hip_atomic_store((gu64*)g, ((u64)tag << 32) | (u64)val, RLX_AGENT);
}
__device__ __forceinline__ void st_gran2(u64* g, u32 tag, u32 v0, u32 v1) {
  const u32x4 d = {v0, tag, v1, tag};
  asm volatile("global_store_dwordx4 %0, %1, off sc1\n\ts_nop 1" :: "v"((gu64*)g), "v"(d) : "memory");
}
__device__ __forceinline__ bool spin_fail(u32& spins, u32* tmo) {
  ++spins;
  if (spins > SPIN_CAP) { __hip_atomic_store((gu32*)tmo, 1u, RLX_AGENT); return true; }
  if ((spins & 1023u) == 0u) { if (__builtin_amdgcn_readfirstlane(__hip_atomic_load((gu32*)tmo, RLX_AGENT))) return true; }
  return false;
}
template <int N>
__device__ __forceinline__ bool sweep2(__amdgpu_buffer_rsrc_t rs, u32 byte_off, u32 byte_stride, u32 tag, u32 (&v)[2 * N], u32* tmo) {
  for (u32 spins = 0;;) {
    bool ok = true;
#pragma unroll
    for (int k = 0; k < N; ++k) {
      const u32x4 x = __builtin_bit_cast(u32x4, __builtin_amdgcn_raw_buffer_load_b128(rs, byte_off + k * byte_stride, 0, 16));
      v[2 * k] = x[0]; v[2 * k + 1] = x[2]; ok &= (x[1] == tag) & (x[3] == tag);
    }
    if (__all(ok)) return true;
    if (spin_fail(spins, tmo)) return false;
  }
}
__device__ __forceinline__ bool wait_flag(const u32* f, u32* tmo) {
  for (u32 spins = 0;;) {
    if (__builtin_amdgcn_readfirstlane(__hip_atomic_load((gu32*)f, RLX_AGENT))) return true;
    if (spin_fail(spins, tmo)) return false;
    __builtin_amdgcn_s_sleep(8);
  }
}

#define STAMP(i) do { } while (0)
#define STAMP5 do { } while (0)

constexpr int C_LM = 1032, C_LQ = 136, C_LV = 72;
constexpr int C_OFF_WO = 0;
constexpr int C_OFF_MEM = C_OFF_WO + 65536;
constexpr int C_OFF_RED = C_OFF_MEM + 8 * C_LM * 2;
constexpr int C_OFF_Q = C_OFF_RED + 8 * 8 * 48 * 4;
constexpr int C_OFF_K = C_OFF_Q + 8 * C_LQ * 2;
constexpr int C_OFF_V = C_OFF_K + 48 * C_LQ * 2;
constexpr int C_LDS = C_OFF_V + 128 * C_LV * 2;
constexpr int W_LDS_A = gemm_lds(256, 128, 4, 2, 3), W_LDS_B = gemm_lds(128, 128, 4, 2, 4);
constexpr int FUSED_LDS = (C_LDS > W_LDS_A ? (C_LDS > W_LDS_B ? C_LDS : W_LDS_B) : (W_LDS_A > W_LDS_B ? W_LDS_A : W_LDS_B));
__device__ __forceinline__ float ror4_sum(float v) {
  return v + __builtin_bit_cast(float, __builtin_amdgcn_update_dpp(0, __builtin_bit_cast(int, v), 0x124, 0xf, 0xf, false));
}

__device__ __forceinline__ void chain_role(const FusedP& p, char* smem, int cb, int h, int j) {
  constexpr int LM = C_LM, LQ = C_LQ, LV = C_LV;
  u16* memS = (u16*)(smem + C_OFF_MEM);
  float* red = (float*)(smem + C_OFF_RED);
  u16* qS = (u16*)(smem + C_OFF_Q);
  u16* kS = (u16*)(smem + C_OFF_K);
  u16* vT = (u16*)(smem + C_OFF_V);
  const int t = threadIdx.x, lane = t & 63, w = __builtin_amdgcn_readfirstlane(t >> 6);
  const int wk = w & 3, wc = w >> 2;
  bf16x8* woS = (bf16x8*)(smem + C_OFF_WO) + (w & 3) * 16 * 64 + lane;
  const int R = h * 4 + j;
  const int fr = lane & 15, fq = lane >> 4;
  u32* tmo = p.ctl;
  const __amdgpu_buffer_rsrc_t rs_mem = __builtin_amdgcn_make_buffer_rsrc((void*)p.memg, 0, 2 * NREP * NG_MEM * 8, 0x00020000);
  const __amdgpu_buffer_rsrc_t rs_qkv = __builtin_amdgcn_make_buffer_rsrc((void*)p.qkvg, 0, 2 * NH * 4 * NG_QKV * 8, 0x00020000);
  const __amdgpu_buffer_rsrc_t rs_part = __builtin_amdgcn_make_buffer_rsrc((void*)p.partg, 0, 2 * 128 * NH * 64 * 8, 0x00020000);
  const __amdgpu_buffer_rsrc_t rs_k = __builtin_amdgcn_make_buffer_rsrc((void*)p.Kx, 0, S_ * HD * 2, 0x00020000);
  const __amdgpu_buffer_rsrc_t rs_v = __builtin_amdgcn_make_buffer_rsrc((void*)p.Vx, 0, S_ * HD * 2, 0x00020000);

  bf16x8 wf[3][8];
  {
    const u16* Ws[3] = {p.Wq, p.Wk, p.Wv};
#pragma unroll
    for (int s = 0; s < 8; ++s) {
#pragma unroll
      for (int m = 0; m < 3; ++m) {
        const u16* src = Ws[m] + (size_t)(256 * wk + 32 * s + 8 * fq) * HD + h * DH + 32 * j + 16 * wc + fr;
#pragma unroll
        for (int i = 0; i < 8; ++i) wf[m][s][i] = (short)src[(size_t)i * HD];
      }
      asm volatile("" : "+v"(wf[0][s]), "+v"(wf[1][s]), "+v"(wf[2][s]) :: "memory");
    }
  }
#pragma unroll
  for (int c2 = 0; c2 < 2; ++c2)
#pragma unroll
    for (int s = 0; s < 4; ++s) {
      const int c = 2 * (w >> 2) + c2;
      bf16x8 f;
#pragma unroll
      for (int i = 0; i < 8; ++i) {
        const int d = 32 * s + ((i < 4) ? (4 * fq + i) : (16 + 4 * fq + i - 4));
        f[i] = (short)p.Wo[(size_t)(h * DH + d) * DM + 256 * j + 64 * (w & 3) + 16 * c + fr];
      }
      woS[(c * 4 + s) * 64] = f;
      asm volatile("" ::: "memory");
    }
  for (int e = t; e < 128 * LV; e += 512) vT[e] = 0;
  for (int e = t; e < 48 * LQ; e += 512) kS[e] = 0;
  u32x4 kpre, vpre;
  auto load_kv = [&](int seg) -> bool {
    if ((seg & 7) == 0) {
      if (!wait_flag(p.kvflag + ((seg >> 3) * 64 + h) * 16, tmo)) return false;
      if (!wait_flag(p.kvflag + ((seg >> 3) * 64 + 32 + h) * 16, tmo)) return false;
    }
    const int r = t >> 4, c = (t & 15) * 8;
    const u32 off = (u32)(((seg * SEG + r) * HD + h * DH + c) * 2);
    kpre = __builtin_bit_cast(u32x4, __builtin_amdgcn_raw_buffer_load_b128(rs_k, off, 0, 16));
    vpre = __builtin_bit_cast(u32x4, __builtin_amdgcn_raw_buffer_load_b128(rs_v, off, 0, 16));
    return true;
  };
  if (!load_kv(0)) return;
  __syncthreads();

  for (int s = 0; s < NSEG; ++s) {
    const int par = s & 1;
    const u32 epoch = (u32)s + 1u;
    STAMP(0);
    if (s == 0) {
#pragma unroll
      for (int i = 0; i < 2; ++i) {
        const int e = t + i * 512, r = e >> 7, c = (e & 127) * 8;
        *(u32x4*)(&memS[r * LM + c]) = *(const u32x4*)(p.mem0 + r * DM + c);
      }
    } else {
      u32 v[8];
      const u32 gbase = (u32)(((par ^ 1) * NREP + (cb & (NREP - 1))) * NG_MEM + w * 512);
      __builtin_amdgcn_s_sleep(H1_DELAY);
      if (!sweep2<4>(rs_mem, (gbase + lane * 2) * 8, 128 * 8, (u32)s, v, tmo)) return;
#pragma unroll
      for (int k = 0; k < 4; ++k) {
        const int u = lane + k * 64;
        *(uint2*)(&memS[((u >> 1) & 7) * LM + (w * 16 + (u >> 4)) * 8 + (u & 1) * 4]) = make_uint2(v[2 * k], v[2 * k + 1]);
      }
    }
    {
      const int r = t >> 4, c = (t & 15) * 8;
      *(u32x4*)(&kS[r * LQ + c]) = kpre;
      const u16* vp = (const u16*)&vpre;
      const int pos = (r < 16) ? (8 * (r >> 2) + (r & 3)) : (8 * ((r - 16) >> 2) + 4 + (r & 3));
#pragma unroll
      for (int q = 0; q < 8; ++q) vT[(c + q) * LV + pos] = vp[q];
    }
    __syncthreads();
    STAMP(1);
    {
      f32x4 acc[3] = {{0.f, 0.f, 0.f, 0.f}, {0.f, 0.f, 0.f, 0.f}, {0.f, 0.f, 0.f, 0.f}};
      bf16x8 a[8];
#pragma unroll
      for (int ks = 0; ks < 8; ++ks) a[ks] = *(const bf16x8*)(&memS[(lane & 7) * LM + 256 * wk + 32 * ks + 8 * fq]);
      __builtin_amdgcn_sched_barrier(0);
#pragma unroll
      for (int ks = 0; ks < 8; ++ks)
#pragma unroll
        for (int m = 0; m < 3; ++m) acc[m] = __builtin_amdgcn_mfma_f32_16x16x32_bf16(a[ks], wf[m][ks], acc[m], 0, 0, 0);
      if (fq < 2) {
#pragma unroll
        for (int m = 0; m < 3; ++m)
#pragma unroll
          for (int r = 0; r < 4; ++r) red[(w * 8 + fq * 4 + r) * 48 + m * 16 + fr] = acc[m][r];
      }
    }
    __syncthreads();
    if (t < NG_QKV / 2) {
      const int r = t / 24, cq = t % 24;
      const int m = cq >> 3, c = (cq & 7) * 4, g = c >> 4;
      float4 v4 = make_float4(0.f, 0.f, 0.f, 0.f);
#pragma unroll
      for (int ww = 0; ww < 4; ++ww) { const float4 q4 = *(const float4*)(&red[((g * 4 + ww) * 8 + r) * 48 + m * 16 + (c & 15)]); v4.x += q4.x; v4.y += q4.y; v4.z += q4.z; v4.w += q4.w; }
      const u32 pk0 = cvtpk(v4.x, v4.y), pk1 = cvtpk(v4.z, v4.w);
      if (s < NSEG - 1) st_gran2(p.qkvg + ((size_t)(par * NH + h) * 4 + j) * NG_QKV + 2 * t, epoch, pk0, pk1);
      if (m == 1) *(uint2*)(p.KM + (size_t)(s * MM + r) * HD + h * DH + 32 * j + c) = make_uint2(pk0, pk1);
      if (m == 2) *(uint2*)(p.VM + (size_t)(s * MM + r) * HD + h * DH + 32 * j + c) = make_uint2(pk0, pk1);
    }
    STAMP(2);
    if (s == NSEG - 1) break;
    bf16x8 kx[2][4], vx[8];
#pragma unroll
    for (int ks = 0; ks < 4; ++ks)
#pragma unroll
      for (int n = 0; n < 2; ++n) kx[n][ks] = *(const bf16x8*)(&kS[(n * 16 + fr) * LQ + 32 * ks + 8 * fq]);
    __builtin_amdgcn_sched_barrier(0);
    {
      u32 v[4];
      const int pj = w >> 1;
      const int u0 = (w & 1) * 96 + lane;
      const int u1 = (w & 1) * 96 + 64 + (lane & 31);
      const u32 gbase = (u32)(((par * NH + h) * 4 + pj) * NG_QKV);
      if (!sweep2<2>(rs_qkv, (gbase + u0 * 2) * 8, (u32)((u1 - u0) * 16), epoch, v, tmo)) return;
#pragma unroll
      for (int k = 0; k < 2; ++k) {
        const int u = k ? u1 : u0;
        const int r = u / 24, cq = u % 24, m = cq >> 3, c = 32 * pj + (cq & 7) * 4;
        if (m == 0) *(uint2*)(&qS[r * LQ + c]) = make_uint2(v[2 * k], v[2 * k + 1]);
        else if (m == 1) *(uint2*)(&kS[(32 + r) * LQ + c]) = make_uint2(v[2 * k], v[2 * k + 1]);
        else {
          const int pos = 32 + 8 * (r >> 2) + (r & 3);
          vT[c * LV + pos] = (u16)(v[2 * k] & 0xffffu); vT[(c + 1) * LV + pos] = (u16)(v[2 * k] >> 16);
          vT[(c + 2) * LV + pos] = (u16)(v[2 * k + 1] & 0xffffu); vT[(c + 3) * LV + pos] = (u16)(v[2 * k + 1] >> 16);
        }
      }
    }
    __syncthreads();
    STAMP(3);
    if (w < 4) {
      f32x4 sc[3] = {{0.f, 0.f, 0.f, 0.f}, {0.f, 0.f, 0.f, 0.f}, {0.f, 0.f, 0.f, 0.f}};
      bf16x8 qf[4], km[4];
      uint2 vm[8];
#pragma unroll
      for (int ks = 0; ks < 4; ++ks) {
        qf[ks] = *(const bf16x8*)(&qS[(lane & 7) * LQ + 32 * ks + 8 * fq]);
        km[ks] = *(const bf16x8*)(&kS[(32 + fr) * LQ + 32 * ks + 8 * fq]);
      }
#pragma unroll
      for (int c = 0; c < 8; ++c) vm[c] = *(const uint2*)(&vT[(16 * c + fr) * LV + 32 + 8 * fq]);
      __builtin_amdgcn_sched_barrier(0);
#pragma unroll
      for (int ks = 0; ks < 4; ++ks) {
        sc[0] = __builtin_amdgcn_mfma_f32_16x16x32_bf16(kx[0][ks], qf[ks], sc[0], 0, 0, 0);
        sc[1] = __builtin_amdgcn_mfma_f32_16x16x32_bf16(kx[1][ks], qf[ks], sc[1], 0, 0, 0);
        sc[2] = __builtin_amdgcn_mfma_f32_16x16x32_bf16(km[ks], qf[ks], sc[2], 0, 0, 0);
      }
      __builtin_amdgcn_sched_barrier(0);
#pragma unroll
      for (int c = 0; c < 8; ++c) vx[c] = *(const bf16x8*)(&vT[(16 * c + fr) * LV + 8 * fq]);

      __builtin_amdgcn_sched_barrier(0);
      const float scale = 0.08838834764831845f;
      const bool live2 = fq < 2;
      float xs[3][4];
#pragma unroll
      for (int n = 0; n < 3; ++n)
#pragma unroll
        for (int r = 0; r < 4; r += 2) {
          const u32 a = cvtpk(sc[n][r], sc[n][r + 1]);
          const u32 c2 = cvtpk(lo2f(a) * scale, hi2f(a) * scale);
          xs[n][r] = lo2f(c2); xs[n][r + 1] = hi2f(c2);
        }
      if (!live2) { xs[2][0] = xs[2][1] = xs[2][2] = xs[2][3] = -3.0e38f; }
      float mx = fmaxf(fmaxf(fmaxf(xs[0][0], xs[0][1]), fmaxf(xs[0][2], xs[0][3])), fmaxf(fmaxf(xs[1][0], xs[1][1]), fmaxf(xs[1][2], xs[1][3])));
      mx = fmaxf(mx, fmaxf(fmaxf(xs[2][0], xs[2][1]), fmaxf(xs[2][2], xs[2][3])));
      mx = xrow_max(mx);
      float sm = 0.f;
#pragma unroll
      for (int n = 0; n < 3; ++n)
#pragma unroll
        for (int r = 0; r < 4; ++r) { xs[n][r] = __expf(xs[n][r] - mx); sm += xs[n][r]; }
      sm = xrow_sum(sm);
      const float inv = 1.0f / sm;
      u32x4 pw0, pw1;
      pw0[0] = cvtpk(xs[0][0] * inv, xs[0][1] * inv); pw0[1] = cvtpk(xs[0][2] * inv, xs[0][3] * inv);
      pw0[2] = cvtpk(xs[1][0] * inv, xs[1][1] * inv); pw0[3] = cvtpk(xs[1][2] * inv, xs[1][3] * inv);
      pw1[0] = cvtpk(xs[2][0] * inv, xs[2][1] * inv); pw1[1] = cvtpk(xs[2][2] * inv, xs[2][3] * inv);
      pw1[2] = 0u; pw1[3] = 0u;
      const bf16x8 pf0 = __builtin_bit_cast(bf16x8, pw0), pf1 = __builtin_bit_cast(bf16x8, pw1);
      u32x4 ow[4];
#pragma unroll
      for (int c = 0; c < 8; ++c) {
        const u32x4 vmw = {vm[c].x, vm[c].y, 0u, 0u};
        f32x4 oc = {0.f, 0.f, 0.f, 0.f};
        oc = __builtin_amdgcn_mfma_f32_16x16x32_bf16(vx[c], pf0, oc, 0, 0, 0);
        oc = __builtin_amdgcn_mfma_f32_16x16x32_bf16(__builtin_bit_cast(bf16x8, vmw), pf1, oc, 0, 0, 0);
        ow[c >> 1][(c & 1) * 2] = cvtpk(oc[0], oc[1]); ow[c >> 1][(c & 1) * 2 + 1] = cvtpk(oc[2], oc[3]);
      }
      f32x4 pc[4];
#pragma unroll
      for (int c = 0; c < 4; ++c) pc[c] = (f32x4){0.f, 0.f, 0.f, 0.f};
#pragma unroll
      for (int ks = 0; ks < 4; ++ks)
#pragma unroll
        for (int c = 0; c < 4; ++c) pc[c] = __builtin_amdgcn_mfma_f32_16x16x32_bf16(woS[(c * 4 + ks) * 64], __builtin_bit_cast(bf16x8, ow[ks]), pc[c], 0, 0, 0);
      if (fr < 8) {
#pragma unroll
        for (int c = 0; c < 4; ++c) {
          u64* dst = p.partg + ((size_t)(par * 128 + 32 * j + 8 * w + 2 * c + (fq >> 1)) * NH + h) * 64 + fr * 8 + 4 * (fq & 1);
          st_gran2(dst, epoch, __float_as_uint(pc[c][0]), __float_as_uint(pc[c][1]));
          st_gran2(dst + 2, epoch, __float_as_uint(pc[c][2]), __float_as_uint(pc[c][3]));
        }
      }
    }
    STAMP(4);
    __syncthreads();
    if (w >= 4) {
      u32 v[8];
      const int w4 = w - 4;
      const int hg = lane >> 3, u = lane & 7;
      const u32 gbase = (u32)(((par * 128 + R) * NH + 4 * hg) * 64 + (8 * w4 + u) * 2);
      if (!sweep2<4>(rs_part, gbase * 8, 64 * 8, epoch, v, tmo)) return;
      float f0 = 0.f, f1 = 0.f;
#pragma unroll
      for (int k = 0; k < 4; ++k) { f0 += __uint_as_float(v[2 * k]); f1 += __uint_as_float(v[2 * k + 1]); }
      f0 = ror8_sum(f0); f1 = ror8_sum(f1);
      f0 = xrow_sum(f0); f1 = xrow_sum(f1);
      const u32 pk = cvtpk(f0, f1);
      st_gran(p.memg + (size_t)(par * NREP + hg) * NG_MEM + R * 32 + 8 * w4 + u, epoch, pk);
      STAMP5;
    }
    if (!load_kv(s + 1)) return;
    __syncthreads();
  }
}

__device__ __forceinline__ void worker_role(const FusedP& p, char* smem, int gw) {
  const int t = threadIdx.x;
  u32* tmo = p.ctl;
  for (int i = 0; i < 4; ++i) {
    const int id = gw + 128 * i, rb = id >> 6, n = id & 63;
    const u16* B = (n < 32 ? p.Wk : p.Wv) + (n & 31) * 128;
    u16* C = (n < 32 ? p.Kx : p.Vx) + (size_t)rb * 256 * HD + (n & 31) * 128;
    gemm_tile<256, 128, 4, 2, 3>(smem, p.xn + (size_t)rb * 256 * DM, DM, B, HD, DM, C, HD, nullptr, 0, true);
    asm volatile("s_waitcnt vmcnt(0)" ::: "memory");
    __syncthreads();
    if (t == 0) __hip_atomic_store((gu32*)(p.kvflag + (rb * 64 + n) * 16), 1u, RLX_AGENT);
  }
  for (int id = gw; id < 480; id += 128) {
    if (id < 224) {
      const int rb = id / 28, n = id % 28;
      gemm_tile<256, 128, 4, 2, 3>(smem, p.xn + (size_t)rb * 256 * DM, DM, p.w1 + n * 128, FF, DM, p.Hb + (size_t)rb * 256 * FF + n * 128, FF, p.b1 + n * 128, 2, false);
    } else {
      const int q = id - 224, rb = q >> 5, n = q & 31;
      gemm_tile<256, 128, 4, 2, 3>(smem, p.xn + (size_t)rb * 256 * DM, DM, p.Wq + n * 128, HD, DM, p.Qx + (size_t)rb * 256 * HD + n * 128, HD, nullptr, 0, false);
    }
  }
  asm volatile("s_waitcnt vmcnt(0)" ::: "memory");
  __syncthreads();
  if (t == 0) {
    __builtin_amdgcn_fence(__ATOMIC_RELEASE, "agent");
    asm volatile("s_waitcnt vmcnt(0)" ::: "memory");
    __hip_atomic_fetch_add((gu32*)(p.ctl + 64), 1u, RLX_AGENT);
    for (u32 spins = 0;;) {
      if (__hip_atomic_load((gu32*)(p.ctl + 64), RLX_AGENT) >= 128u) break;
      if (++spins > SPIN_CAP) { __hip_atomic_store((gu32*)tmo, 1u, RLX_AGENT); break; }
      __builtin_amdgcn_s_sleep(16);
    }
    __builtin_amdgcn_fence(__ATOMIC_ACQUIRE, "agent");
    asm volatile("s_waitcnt vmcnt(0)" ::: "memory");
  }
  __syncthreads();
  {
    const int mt = gw >> 3, nt = gw & 7;
    gemm_tile<128, 128, 4, 2, 4>(smem, p.Hb + (size_t)mt * 128 * FF, FF, p.w2 + nt * 128, DM, FF, p.Bm + (size_t)mt * 128 * DM + nt * 128, DM, p.b2 + nt * 128, 1, false);
  }
}

__global__ __launch_bounds__(512, 2) void fused_kernel(FusedP p) {
  extern __shared__ __attribute__((aligned(16))) char smem[];
  const int b = blockIdx.x, xcd = b & 7, ib = b >> 3;
  if (ib < 16) chain_role(p, smem, xcd * 16 + ib, xcd * 4 + (ib >> 2), ib & 3);
  else worker_role(p, smem, (ib - 16) * 8 + xcd);
}

__global__ __launch_bounds__(256) void attn_x_kernel(const u16* __restrict__ Q, const u16* __restrict__ Kx, const u16* __restrict__ Vx,
                                                     const u16* __restrict__ KM, const u16* __restrict__ VM, u16* __restrict__ O) {
  constexpr int LVR = 136;
  __shared__ __attribute__((aligned(16))) u16 vS[4][48 * LVR];
  const int t = threadIdx.x, lane = t & 63, w = __builtin_amdgcn_readfirstlane(t >> 6);
  const int item = blockIdx.x * 4 + w, s = item >> 5, h = item & 31;
  const int fr = lane & 15, fq = lane >> 4;
  u16* vw = vS[w];
  const size_t xoff = (size_t)(s * SEG) * HD + h * DH, moff = (size_t)(s * MM) * HD + h * DH;
#pragma unroll
  for (int i = 0; i < 12; ++i) {
    const int e = lane + i * 64, r = e >> 4, c = (e & 15) * 8;
    u32x4 v = {0u, 0u, 0u, 0u};
    if (r < 32) v = *(const u32x4*)(Vx + xoff + (size_t)r * HD + c);
    else if (r < 40) v = *(const u32x4*)(VM + moff + (size_t)(r - 32) * HD + c);
    *(u32x4*)(&vw[r * LVR + c]) = v;
  }
  bf16x8 qf[2][4], kf[3][4];
#pragma unroll
  for (int ks = 0; ks < 4; ++ks) {
#pragma unroll
    for (int n = 0; n < 2; ++n) {
      qf[n][ks] = *(const bf16x8*)(Q + xoff + (size_t)(16 * n + fr) * HD + 32 * ks + 8 * fq);
      kf[n][ks] = *(const bf16x8*)(Kx + xoff + (size_t)(16 * n + fr) * HD + 32 * ks + 8 * fq);
    }
    kf[2][ks] = *(const bf16x8*)(KM + moff + (size_t)(fr & 7) * HD + 32 * ks + 8 * fq);
  }
  asm volatile("s_waitcnt lgkmcnt(0)" ::: "memory");
  __builtin_amdgcn_wave_barrier();
  bf16x8 vf[8][2];
  const int tq = fr >> 2, tp = fr & 3;
#pragma unroll
  for (int c = 0; c < 8; ++c) {
    const s16x4 a0 = __builtin_amdgcn_ds_read_tr16_b64_v4i16((LAS s16x4*)(&vw[(4 * fq + tq) * LVR + 16 * c + 4 * tp]));
    const s16x4 a1 = __builtin_amdgcn_ds_read_tr16_b64_v4i16((LAS s16x4*)(&vw[(16 + 4 * fq + tq) * LVR + 16 * c + 4 * tp]));
    const s16x4 a2 = __builtin_amdgcn_ds_read_tr16_b64_v4i16((LAS s16x4*)(&vw[(32 + 4 * fq + tq) * LVR + 16 * c + 4 * tp]));
    vf[c][0] = (bf16x8){a0[0], a0[1], a0[2], a0[3], a1[0], a1[1], a1[2], a1[3]};
    vf[c][1] = (bf16x8){a2[0], a2[1], a2[2], a2[3], 0, 0, 0, 0};
  }
  const float scale = 0.08838834764831845f;
#pragma unroll
  for (int qt = 0; qt < 2; ++qt) {
    f32x4 sc[3] = {{0.f, 0.f, 0.f, 0.f}, {0.f, 0.f, 0.f, 0.f}, {0.f, 0.f, 0.f, 0.f}};
#pragma unroll
    for (int ks = 0; ks < 4; ++ks)
#pragma unroll
      for (int n = 0; n < 3; ++n) sc[n] = __builtin_amdgcn_mfma_f32_16x16x32_bf16(kf[n][ks], qf[qt][ks], sc[n], 0, 0, 0);
    const int qi = 16 * qt + fr;
    float xs[3][4];
#pragma unroll
    for (int n = 0; n < 3; ++n)
#pragma unroll
      for (int r = 0; r < 4; r += 2) {
        const u32 a = cvtpk(sc[n][r], sc[n][r + 1]);
        const u32 c2 = cvtpk(lo2f(a) * scale, hi2f(a) * scale);
        xs[n][r] = lo2f(c2); xs[n][r + 1] = hi2f(c2);
      }
    float mx = -3.0e38f;
#pragma unroll
    for (int n = 0; n < 3; ++n)
#pragma unroll
      for (int r = 0; r < 4; ++r) {
        const bool live = (n < 2) ? (16 * n + 4 * fq + r <= qi) : (fq < 2);
        if (!live) xs[n][r] = -3.0e38f;
        mx = fmaxf(mx, xs[n][r]);
      }
    mx = xrow_max(mx);
    float sm = 0.f;
#pragma unroll
    for (int n = 0; n < 3; ++n)
#pragma unroll
      for (int r = 0; r < 4; ++r) { xs[n][r] = __expf(xs[n][r] - mx); sm += xs[n][r]; }
    sm = xrow_sum(sm);
    const float inv = 1.0f / sm;
    u32x4 pw0, pw1;
    pw0[0] = cvtpk(xs[0][0] * inv, xs[0][1] * inv); pw0[1] = cvtpk(xs[0][2] * inv, xs[0][3] * inv);
    pw0[2] = cvtpk(xs[1][0] * inv, xs[1][1] * inv); pw0[3] = cvtpk(xs[1][2] * inv, xs[1][3] * inv);
    pw1[0] = cvtpk(xs[2][0] * inv, xs[2][1] * inv); pw1[1] = cvtpk(xs[2][2] * inv, xs[2][3] * inv);
    pw1[2] = 0u; pw1[3] = 0u;
    const bf16x8 pf0 = __builtin_bit_cast(bf16x8, pw0), pf1 = __builtin_bit_cast(bf16x8, pw1);
    u16* orow = O + xoff + (size_t)qi * HD + 4 * fq;
#pragma unroll
    for (int c = 0; c < 8; ++c) {
      f32x4 oc = {0.f, 0.f, 0.f, 0.f};
      oc = __builtin_amdgcn_mfma_f32_16x16x32_bf16(vf[c][0], pf0, oc, 0, 0, 0);
      oc = __builtin_amdgcn_mfma_f32_16x16x32_bf16(vf[c][1], pf1, oc, 0, 0, 0);
      *(uint2*)(orow + 16 * c) = make_uint2(cvtpk(oc[0], oc[1]), cvtpk(oc[2], oc[3]));
    }
  }
}

extern "C" void kernel_launch(void* const* d_in, const int* in_sizes, int n_in,
                              void* d_out, int out_size, void* d_ws, size_t ws_size,
                              hipStream_t stream) {
  const u16* x = (const u16*)d_in[0];
  const u16* mem0 = (const u16*)d_in[1];
  const u16* Wq = (const u16*)d_in[2];
  const u16* Wk = (const u16*)d_in[3];
  const u16* Wv = (const u16*)d_in[4];
  const u16* Wo = (const u16*)d_in[5];
  const u16* w1 = (const u16*)d_in[8];
  const u16* b1 = (const u16*)d_in[9];
  const u16* w2 = (const u16*)d_in[10];
  const u16* b2 = (const u16*)d_in[11];
  u16* out = (u16*)d_out;

  char* ws = (char*)d_ws;
  size_t off = 0;
  auto carve = [&](size_t bytes) { char* q = ws + off; off += (bytes + 255) & ~(size_t)255; return q; };
  u64* memg = (u64*)carve((size_t)2 * NREP * NG_MEM * 8);
  u64* qkvg = (u64*)carve((size_t)2 * NH * 4 * NG_QKV * 8);
  u64* partg = (u64*)carve((size_t)2 * 128 * NH * 64 * 8);
  u32* kvflag = (u32*)carve((size_t)512 * 64);
  u32* ctl = (u32*)carve(1024);
  const size_t sync_bytes = off;
  u16* xn = (u16*)carve((size_t)S_ * DM * 2);
  u16* Qx = (u16*)carve((size_t)S_ * HD * 2);
  u16* Kx = (u16*)carve((size_t)S_ * HD * 2);
  u16* Vx = (u16*)carve((size_t)S_ * HD * 2);
  u16* Hb = (u16*)carve((size_t)S_ * FF * 2);
  u16* Ox = (u16*)carve((size_t)S_ * HD * 2);
  u16* KM = (u16*)carve((size_t)NSEG * MM * HD * 2);
  u16* VM = (u16*)carve((size_t)NSEG * MM * HD * 2);
  u16* Bm = (u16*)carve((size_t)S_ * DM * 2);
  u16* Am = (u16*)carve((size_t)S_ * DM * 2);

  (void)hipMemsetAsync(ws, 0, sync_bytes, stream);
  ln_kernel<<<S_, 256, 0, stream>>>(x, xn);
  {
    FusedP f{xn, mem0, Wq, Wk, Wv, Wo, w1, b1, w2, b2, Qx, Kx, Vx, Hb, Bm, KM, VM, memg, qkvg, partg, kvflag, ctl};
    (void)hipFuncSetAttribute((const void*)fused_kernel, hipFuncAttributeMaxDynamicSharedMemorySize, FUSED_LDS);
    fused_kernel<<<256, 512, FUSED_LDS, stream>>>(f);
  }
  attn_x_kernel<<<NSEG * NH / 4, 256, 0, stream>>>(Qx, Kx, Vx, KM, VM, Ox);
  {
    constexpr int LDSW = gemm_lds(128, 64, 2, 2, 4);
    (void)hipFuncSetAttribute((const void*)gemm_wo_kernel, hipFuncAttributeMaxDynamicSharedMemorySize, LDSW);
    gemm_wo_kernel<<<256, 256, LDSW, stream>>>(Ox, Wo, Am);
  }
  combine_kernel<<<S_ * DM / 8 / 256, 256, 0, stream>>>(x, Am, Bm, out);
}
```

```cpp
#include <hip/hip_runtime.h>
#include <hip/hip_bf16.h>
#include <stdint.h>

typedef unsigned short u16;
typedef unsigned int u32;
typedef unsigned long long u64;
#define GAS __attribute__((address_space(1)))
#define LAS __attribute__((address_space(3)))
typedef GAS u64 gu64;
typedef GAS u32 gu32;
typedef __attribute__((ext_vector_type(8))) short bf16x8;
typedef __attribute__((ext_vector_type(4))) short s16x4;
typedef __attribute__((ext_vector_type(4))) float f32x4;
typedef __attribute__((ext_vector_type(4))) unsigned int u32x4;
#define RLX_AGENT __ATOMIC_RELAXED, __HIP_MEMORY_SCOPE_AGENT

constexpr int S_ = 2048, DM = 1024, NH = 32, DH = 128, HD = 4096, MM = 8, SEG = 32, NSEG = 64, FF = 3584;

__device__ __forceinline__ float bf2f(u16 v) { return __uint_as_float(((u32)v) << 16); }
__device__ __forceinline__ u16 f2bf(float f) { u32 u = __float_as_uint(f); u += 0x7fffu + ((u >> 16) & 1u); return (u16)(u >> 16); }
__device__ __forceinline__ float rbf(float f) { return bf2f(f2bf(f)); }
typedef __bf16 hbf2 __attribute__((ext_vector_type(2)));
typedef float f32x2 __attribute__((ext_vector_type(2)));
__device__ __forceinline__ u32 cvtpk(float a, float b) { f32x2 v = {a, b}; return __builtin_bit_cast(u32, __builtin_convertvector(v, hbf2)); }
__device__ __forceinline__ float lo2f(u32 v) { return __uint_as_float(v << 16); }
__device__ __forceinline__ float hi2f(u32 v) { return __uint_as_float(v & 0xffff0000u); }

__device__ __forceinline__ float xrow_max(float v) {
  auto a = __builtin_amdgcn_permlane16_swap(__float_as_uint(v), __float_as_uint(v), false, false);
  v = fmaxf(__uint_as_float(a[0]), __uint_as_float(a[1]));
  auto c = __builtin_amdgcn_permlane32_swap(__float_as_uint(v), __float_as_uint(v), false, false);
  return fmaxf(__uint_as_float(c[0]), __uint_as_float(c[1]));
}
__device__ __forceinline__ float xrow_sum(float v) {
  auto a = __builtin_amdgcn_permlane16_swap(__float_as_uint(v), __float_as_uint(v), false, false);
  v = __uint_as_float(a[0]) + __uint_as_float(a[1]);
  auto c = __builtin_amdgcn_permlane32_swap(__float_as_uint(v), __float_as_uint(v), false, false);
  return __uint_as_float(c[0]) + __uint_as_float(c[1]);
}
__device__ __forceinline__ float ror8_sum(float v) {
  return v + __builtin_bit_cast(float, __builtin_amdgcn_update_dpp(0, __builtin_bit_cast(int, v), 0x128, 0xf, 0xf, false));
}

__global__ __launch_bounds__(256) void ln_kernel(const u16* __restrict__ x, u16* __restrict__ xn) {
  __shared__ float red[8];
  const int row = blockIdx.x, t = threadIdx.x, lane = t & 63, w = t >> 6;
  const ushort4 v = *(const ushort4*)(x + (size_t)row * DM + t * 4);
  float f0 = bf2f(v.x), f1 = bf2f(v.y), f2 = bf2f(v.z), f3 = bf2f(v.w);
  float s = (f0 + f1) + (f2 + f3);
#pragma unroll
  for (int m = 32; m >= 1; m >>= 1) s += __shfl_xor(s, m);
  if (lane == 0) red[w] = s;
  __syncthreads();
  const float mean = ((red[0] + red[1]) + (red[2] + red[3])) * (1.0f / DM);
  float d0 = f0 - mean, d1 = f1 - mean, d2 = f2 - mean, d3 = f3 - mean;
  float q = (d0 * d0 + d1 * d1) + (d2 * d2 + d3 * d3);
#pragma unroll
  for (int m = 32; m >= 1; m >>= 1) q += __shfl_xor(q, m);
  if (lane == 0) red[4 + w] = q;
  __syncthreads();
  const float var = ((red[4] + red[5]) + (red[6] + red[7])) * (1.0f / DM);
  const float mb = rbf(mean), vb = rbf(var);
  const float den = rbf(sqrtf(rbf(vb + 1e-5f)));
  ushort4 o;
  o.x = f2bf(rbf(f0 - mb) / den); o.y = f2bf(rbf(f1 - mb) / den);
  o.z = f2bf(rbf(f2 - mb) / den); o.w = f2bf(rbf(f3 - mb) / den);
  *(ushort4*)(xn + (size_t)row * DM + t * 4) = o;
}

__device__ __forceinline__ float gelu_tanh(float x) {
  const float k0 = 0.7978845608028654f, k1 = 0.044715f;
  const float u = k0 * (x + k1 * x * x * x);
  return 0.5f * x * (1.0f + tanhf(u));
}
__device__ __forceinline__ void glds16(const void* gsrc, u32 lds_dst) {
  u32 keep;
  asm volatile("s_mov_b32 %0, m0\n\ts_mov_b32 m0, %2\n\ts_nop 0\n\tglobal_load_lds_dwordx4 %1, off\n\ts_mov_b32 m0, %0"
               : "=&s"(keep) : "v"(gsrc), "s"(lds_dst) : "memory");
}
template <int N> __device__ __forceinline__ void wait_vm() { asm volatile("s_waitcnt vmcnt(%0)" :: "n"(N) : "memory"); }
__device__ __forceinline__ void st16_sc1(void* p, u32x4 v) {
  asm volatile("global_store_dwordx4 %0, %1, off sc1\n\ts_nop 1" :: "v"(p), "v"(v) : "memory");
}
constexpr int gemm_lds(int BM, int BN, int WM, int WN, int NSTAGE) {
  const int st = NSTAGE * (BM * 64 * 2 + 64 * BN * 2), ep = WM * WN * (BM / WM) * (BN / WN + 8) * 2;
  return st > ep ? st : ep;
}
template <int BM, int BN, int WM, int WN, int NSTAGE>
__device__ __forceinline__ void gemm_tile(char* smem, const u16* A, int lda, const u16* B, int ldb, int K,
                                          u16* C, int ldc, const u16* bias, int EPI, bool WT,
                                          const u16* rx = nullptr, const u16* rb = nullptr) {
  constexpr int NW = WM * WN, NT = NW * 64, BK = 64;
  constexpr int TM = BM / WM / 16, TN = BN / WN / 16;
  constexpr int A_BYTES = BM * BK * 2, B_BYTES = BK * BN * 2, STAGE = A_BYTES + B_BYTES;
  constexpr int NSUB = BN / 16;
  const int t = threadIdx.x, lane = t & 63, w = __builtin_amdgcn_readfirstlane(t >> 6);
  const int wr = w / WN, wc = w % WN, fr = lane & 15, fq = lane >> 4;
  constexpr int AU = BM * 8 / NT, BU = BK * BN / 8 / NT, LPS = AU + BU;
  const u16* asrc[AU]; const u16* bsrc[BU];
#pragma unroll
  for (int i = 0; i < AU; ++i) {
    const int u = i * NT + t, row = u >> 3, c = (u & 7) ^ (row & 7);
    asrc[i] = A + (size_t)row * lda + c * 8;
  }
#pragma unroll
  for (int i = 0; i < BU; ++i) {
    const int u = i * NT + t, hf = u & 1, rp = (u >> 1) & 7, st = u >> 4, kb = st / NSUB, nb = st % NSUB;
    const int r = rp ^ (4 * (kb & 1));
    bsrc[i] = B + (size_t)(kb * 8 + r) * ldb + nb * 16 + hf * 8;
  }
  const u32 lds0 = (u32)(uintptr_t)(LAS char*)smem + (u32)w * 1024u;
  auto stage = [&](int kt, int buf) {
    const u32 base = lds0 + buf * STAGE;
#pragma unroll
    for (int i = 0; i < AU; ++i) glds16(asrc[i] + kt * BK, base + i * NT * 16);
#pragma unroll
    for (int i = 0; i < BU; ++i) glds16(bsrc[i] + (size_t)kt * BK * ldb, base + A_BYTES + i * NT * 16);
  };
  f32x4 acc[TM][TN];
#pragma unroll
  for (int i = 0; i < TM; ++i)
#pragma unroll
    for (int j = 0; j < TN; ++j) acc[i][j] = (f32x4){0.f, 0.f, 0.f, 0.f};
  const int arow0 = (wr * (BM / WM) + fr) * 128;
  const int arx = fr & 7;
  const int tq = (lane & 15) >> 2, tp = lane & 3;
  const int nk = K / BK;
  wait_vm<0>();
#pragma unroll
  for (int k = 0; k < NSTAGE - 1; ++k) stage(k, k);
  int buf = 0;
  for (int kt = 0; kt < nk; ++kt) {
    if (kt + NSTAGE - 1 <= nk) wait_vm<LPS * (NSTAGE - 2)>(); else wait_vm<0>();
    asm volatile("s_waitcnt lgkmcnt(0)" ::: "memory");
    __builtin_amdgcn_s_barrier();
    asm volatile("" ::: "memory");
    if (kt + NSTAGE - 1 < nk) { int nb2 = buf + NSTAGE - 1; if (nb2 >= NSTAGE) nb2 -= NSTAGE; stage(kt + NSTAGE - 1, nb2); }
    const char* sa = smem + buf * STAGE;
    const char* sb = sa + A_BYTES;
#pragma unroll
    for (int ks = 0; ks < 2; ++ks) {
      bf16x8 af[TM], bfr[TN];
#pragma unroll
      for (int i = 0; i < TM; ++i) af[i] = *(const bf16x8*)(sa + arow0 + i * 2048 + (((4 * ks + fq) ^ arx) * 16));
      const int kb = 4 * ks + fq;
      const int rsw = 4 * (kb & 1);
#pragma unroll
      for (int j = 0; j < TN; ++j) {
        const int nb = wc * TN + j;
        const char* sub = sb + (kb * NSUB + nb) * 256 + tp * 8;
        const s16x4 lo = __builtin_amdgcn_ds_read_tr16_b64_v4i16((LAS s16x4*)(sub + ((tq) ^ rsw) * 32));
        const s16x4 hi = __builtin_amdgcn_ds_read_tr16_b64_v4i16((LAS s16x4*)(sub + ((4 + tq) ^ rsw) * 32));
        bfr[j] = (bf16x8){lo[0], lo[1], lo[2], lo[3], hi[0], hi[1], hi[2], hi[3]};
      }
#pragma unroll
      for (int i = 0; i < TM; ++i)
#pragma unroll
        for (int j = 0; j < TN; ++j) acc[i][j] = __builtin_amdgcn_mfma_f32_16x16x32_bf16(bfr[j], af[i], acc[i][j], 0, 0, 0);
    }
    if (++buf == NSTAGE) buf = 0;
  }
  asm volatile("s_waitcnt lgkmcnt(0)" ::: "memory");
  __builtin_amdgcn_s_barrier();
  asm volatile("" ::: "memory");
  constexpr int WROWS = BM / WM, WCOLS = BN / WN, LDW = WCOLS + 8;
  u16* ew = (u16*)smem + w * WROWS * LDW;
#pragma unroll
  for (int j = 0; j < TN; ++j) {
    float bv[4] = {0.f, 0.f, 0.f, 0.f};
    if (EPI) {
      const ushort4 bb = *(const ushort4*)(bias + wc * WCOLS + j * 16 + fq * 4);
      bv[0] = bf2f(bb.x); bv[1] = bf2f(bb.y); bv[2] = bf2f(bb.z); bv[3] = bf2f(bb.w);
    }
#pragma unroll
    for (int i = 0; i < TM; ++i) {
      float v[4];
#pragma unroll
      for (int r = 0; r < 4; ++r) v[r] = acc[i][j][r];
      if (EPI) {
        const u32 a01 = cvtpk(v[0], v[1]), a23 = cvtpk(v[2], v[3]);
        v[0] = lo2f(a01) + bv[0]; v[1] = hi2f(a01) + bv[1]; v[2] = lo2f(a23) + bv[2]; v[3] = hi2f(a23) + bv[3];
        if (EPI == 2) {
          const u32 b01 = cvtpk(v[0], v[1]), b23 = cvtpk(v[2], v[3]);
          v[0] = gelu_tanh(lo2f(b01)); v[1] = gelu_tanh(hi2f(b01)); v[2] = gelu_tanh(lo2f(b23)); v[3] = gelu_tanh(hi2f(b23));
        }
      }
      *(uint2*)(&ew[(i * 16 + fr) * LDW + j * 16 + fq * 4]) = make_uint2(cvtpk(v[0], v[1]), cvtpk(v[2], v[3]));
    }
  }
  asm volatile("s_waitcnt lgkmcnt(0)" ::: "memory");
  __builtin_amdgcn_wave_barrier();
  constexpr int CPR = WCOLS / 8;
#pragma unroll 4
  for (int e = lane; e < WROWS * CPR; e += 64) {
    const int r = e / CPR, c = e % CPR;
    u32x4 v = *(const u32x4*)(&ew[r * LDW + c * 8]);
    const size_t eo = (size_t)(wr * WROWS + r) * ldc + wc * WCOLS + c * 8;
    u16* dst = C + eo;
    if (rx) {
      const u32x4 xv = *(const u32x4*)(rx + eo), bv = *(const u32x4*)(rb + eo);
#pragma unroll
      for (int q = 0; q < 4; ++q) {
        const u32 s1 = cvtpk(lo2f(xv[q]) + lo2f(v[q]), hi2f(xv[q]) + hi2f(v[q]));
        v[q] = cvtpk(lo2f(s1) + lo2f(bv[q]), hi2f(s1) + hi2f(bv[q]));
      }
    }
    if (WT) st16_sc1(dst, v); else *(u32x4*)dst = v;
  }
  asm volatile("s_waitcnt lgkmcnt(0)" ::: "memory");
  __builtin_amdgcn_s_barrier();
  asm volatile("" ::: "memory");
}

#define WO_BM 128
#define WO_BN 64
#define WO_WM 2
#define WO_WN 2
#define WO_NS 4
__global__ __launch_bounds__(WO_WM * WO_WN * 64) void gemm_wo_kernel(const u16* __restrict__ Ox, const u16* __restrict__ Wo, const u16* __restrict__ x,
                                                       const u16* __restrict__ Bm, u16* __restrict__ out) {
  extern __shared__ __attribute__((aligned(16))) char smem[];
  constexpr int NMT = S_ / WO_BM;
  const int xq = blockIdx.x & 7, oq = blockIdx.x >> 3;
  const int mt = oq % NMT, nt = xq + 8 * (oq / NMT);
  const size_t co = (size_t)mt * WO_BM * DM + nt * WO_BN;
  gemm_tile<WO_BM, WO_BN, WO_WM, WO_WN, WO_NS>(smem, Ox + (size_t)mt * WO_BM * HD, HD, Wo + nt * WO_BN, DM, HD, out + co, DM, nullptr, 0, false, x + co, Bm + co);
}

__global__ __launch_bounds__(256) void combine_kernel(const u16* __restrict__ x, const u16* __restrict__ a, const u16* __restrict__ bm, u16* __restrict__ out) {
  const size_t i = ((size_t)blockIdx.x * 256 + threadIdx.x) * 8;
  const u32x4 xv = *(const u32x4*)(x + i), av = *(const u32x4*)(a + i), bv = *(const u32x4*)(bm + i);
  u32x4 o;
#pragma unroll
  for (int q = 0; q < 4; ++q) {
    const u32 s1 = cvtpk(lo2f(xv[q]) + lo2f(av[q]), hi2f(xv[q]) + hi2f(av[q]));
    o[q] = cvtpk(lo2f(s1) + lo2f(bv[q]), hi2f(s1) + hi2f(bv[q]));
  }
  *(u32x4*)(out + i) = o;
}

struct FusedP {
  const u16* xn; const u16* mem0; const u16* Wq; const u16* Wk; const u16* Wv; const u16* Wo;
  const u16* w1; const u16* b1; const u16* w2; const u16* b2;
  u16* Qx; u16* Kx; u16* Vx; u16* Hb; u16* Bm; u16* KM; u16* VM;
  u64* memg; u64* qkvg; u64* partg; u32* kvflag; u32* ctl;
};
constexpr int NG_MEM = 4096;
constexpr int NG_QKV = 384;
constexpr u32 SPIN_CAP = 2000000u;
constexpr int NREP = 8;
#define H3W 4
#define H1_DELAY 8

__device__ __forceinline__ void st_gran(u64* g, u32 tag, u32 val) {
  __hip_atomic_store((gu64*)g, ((u64)tag << 32) | (u64)val, RLX_AGENT);
}
__device__ __forceinline__ void st_gran2(u64* g, u32 tag, u32 v0, u32 v1) {
  const u32x4 d = {v0, tag, v1, tag};
  asm volatile("global_store_dwordx4 %0, %1, off sc1\n\ts_nop 1" :: "v"((gu64*)g), "v"(d) : "memory");
}
__device__ __forceinline__ bool spin_fail(u32& spins, u32* tmo) {
  ++spins;
  if (spins > SPIN_CAP) { __hip_atomic_store((gu32*)tmo, 1u, RLX_AGENT); return true; }
  if ((spins & 1023u) == 0u) { if (__builtin_amdgcn_readfirstlane(__hip_atomic_load((gu32*)tmo, RLX_AGENT))) return true; }
  return false;
}
template <int N>
__device__ __forceinline__ bool sweep2(__amdgpu_buffer_rsrc_t rs, u32 byte_off, u32 byte_stride, u32 tag, u32 (&v)[2 * N], u32* tmo) {
  for (u32 spins = 0;;) {
    bool ok = true;
#pragma unroll
    for (int k = 0; k < N; ++k) {
      const u32x4 x = __builtin_bit_cast(u32x4, __builtin_amdgcn_raw_buffer_load_b128(rs, byte_off + k * byte_stride, 0, 16));
      v[2 * k] = x[0]; v[2 * k + 1] = x[2]; ok &= (x[1] == tag) & (x[3] == tag);
    }
    if (__all(ok)) return true;
    if (spin_fail(spins, tmo)) return false;
  }
}
__device__ __forceinline__ bool wait_flag(const u32* f, u32* tmo) {
  for (u32 spins = 0;;) {
    if (__builtin_amdgcn_readfirstlane(__hip_atomic_load((gu32*)f, RLX_AGENT))) return true;
    if (spin_fail(spins, tmo)) return false;
    __builtin_amdgcn_s_sleep(8);
  }
}

#define STAMP(i) do { } while (0)
#define STAMP5 do { } while (0)

constexpr int C_LM = 1032, C_LQ = 136, C_LV = 72;
constexpr int C_OFF_WO = 0;
constexpr int C_OFF_MEM = C_OFF_WO + 65536;
constexpr int C_OFF_RED = C_OFF_MEM + 8 * C_LM * 2;
constexpr int C_OFF_Q = C_OFF_RED + 8 * 8 * 48 * 4;
constexpr int C_OFF_K = C_OFF_Q + 8 * C_LQ * 2;
constexpr int C_OFF_V = C_OFF_K + 48 * C_LQ * 2;
constexpr int C_LDS = C_OFF_V + 128 * C_LV * 2;
constexpr int W_LDS_A = gemm_lds(256, 128, 4, 2, 3), W_LDS_B = gemm_lds(128, 128, 4, 2, 4);
constexpr int FUSED_LDS = (C_LDS > W_LDS_A ? (C_LDS > W_LDS_B ? C_LDS : W_LDS_B) : (W_LDS_A > W_LDS_B ? W_LDS_A : W_LDS_B));
__device__ __forceinline__ float ror4_sum(float v) {
  return v + __builtin_bit_cast(float, __builtin_amdgcn_update_dpp(0, __builtin_bit_cast(int, v), 0x124, 0xf, 0xf, false));
}

__device__ __forceinline__ void chain_role(const FusedP& p, char* smem, int cb, int h, int j) {
  constexpr int LM = C_LM, LQ = C_LQ, LV = C_LV;
  u16* memS = (u16*)(smem + C_OFF_MEM);
  float* red = (float*)(smem + C_OFF_RED);
  u16* qS = (u16*)(smem + C_OFF_Q);
  u16* kS = (u16*)(smem + C_OFF_K);
  u16* vT = (u16*)(smem + C_OFF_V);
  const int t = threadIdx.x, lane = t & 63, w = __builtin_amdgcn_readfirstlane(t >> 6);
  const int wk = w & 3, wc = w >> 2;
  bf16x8* woS = (bf16x8*)(smem + C_OFF_WO) + (w & 3) * 16 * 64 + lane;
  const int R = h * 4 + j;
  const int fr = lane & 15, fq = lane >> 4;
  u32* tmo = p.ctl;
  const __amdgpu_buffer_rsrc_t rs_mem = __builtin_amdgcn_make_buffer_rsrc((void*)p.memg, 0, 2 * NREP * NG_MEM * 8, 0x00020000);
  const __amdgpu_buffer_rsrc_t rs_qkv = __builtin_amdgcn_make_buffer_rsrc((void*)p.qkvg, 0, 2 * NH * 4 * NG_QKV * 8, 0x00020000);
  const __amdgpu_buffer_rsrc_t rs_part = __builtin_amdgcn_make_buffer_rsrc((void*)p.partg, 0, 2 * 128 * NH * 64 * 8, 0x00020000);
  const __amdgpu_buffer_rsrc_t rs_k = __builtin_amdgcn_make_buffer_rsrc((void*)p.Kx, 0, S_ * HD * 2, 0x00020000);
  const __amdgpu_buffer_rsrc_t rs_v = __builtin_amdgcn_make_buffer_rsrc((void*)p.Vx, 0, S_ * HD * 2, 0x00020000);

  bf16x8 wf[3][8];
  {
    const u16* Ws[3] = {p.Wq, p.Wk, p.Wv};
#pragma unroll
    for (int s = 0; s < 8; ++s) {
#pragma unroll
      for (int m = 0; m < 3; ++m) {
        const u16* src = Ws[m] + (size_t)(256 * wk + 32 * s + 8 * fq) * HD + h * DH + 32 * j + 16 * wc + fr;
#pragma unroll
        for (int i = 0; i < 8; ++i) wf[m][s][i] = (short)src[(size_t)i * HD];
      }
      asm volatile("" : "+v"(wf[0][s]), "+v"(wf[1][s]), "+v"(wf[2][s]) :: "memory");
    }
  }
#pragma unroll
  for (int c2 = 0; c2 < 2; ++c2)
#pragma unroll
    for (int s = 0; s < 4; ++s) {
      const int c = 2 * (w >> 2) + c2;
      bf16x8 f;
#pragma unroll
      for (int i = 0; i < 8; ++i) {
        const int d = 32 * s + ((i < 4) ? (4 * fq + i) : (16 + 4 * fq + i - 4));
        f[i] = (short)p.Wo[(size_t)(h * DH + d) * DM + 256 * j + 64 * (w & 3) + 16 * c + fr];
      }
      woS[(c * 4 + s) * 64] = f;
      asm volatile("" ::: "memory");
    }
  for (int e = t; e < 128 * LV; e += 512) vT[e] = 0;
  for (int e = t; e < 48 * LQ; e += 512) kS[e] = 0;
  u32x4 kpre, vpre;
  auto load_kv = [&](int seg) -> bool {
    if ((seg & 7) == 0) {
      if (!wait_flag(p.kvflag + ((seg >> 3) * 64 + h) * 16, tmo)) return false;
      if (!wait_flag(p.kvflag + ((seg >> 3) * 64 + 32 + h) * 16, tmo)) return false;
    }
    const int r = t >> 4, c = (t & 15) * 8;
    const u32 off = (u32)(((seg * SEG + r) * HD + h * DH + c) * 2);
    kpre = __builtin_bit_cast(u32x4, __builtin_amdgcn_raw_buffer_load_b128(rs_k, off, 0, 16));
    vpre = __builtin_bit_cast(u32x4, __builtin_amdgcn_raw_buffer_load_b128(rs_v, off, 0, 16));
    return true;
  };
  if (!load_kv(0)) return;
  __syncthreads();

  for (int s = 0; s < NSEG; ++s) {
    const int par = s & 1;
    const u32 epoch = (u32)s + 1u;
    STAMP(0);
    if (s == 0) {
#pragma unroll
      for (int i = 0; i < 2; ++i) {
        const int e = t + i * 512, r = e >> 7, c = (e & 127) * 8;
        *(u32x4*)(&memS[r * LM + c]) = *(const u32x4*)(p.mem0 + r * DM + c);
      }
    } else {
      u32 v[8];
      const u32 gbase = (u32)(((par ^ 1) * NREP + (cb & (NREP - 1))) * NG_MEM + w * 512);
      __builtin_amdgcn_s_sleep(H1_DELAY);
      if (!sweep2<4>(rs_mem, (gbase + lane * 2) * 8, 128 * 8, (u32)s, v, tmo)) return;
#pragma unroll
      for (int k = 0; k < 4; ++k) {
        const int u = lane + k * 64;
        *(uint2*)(&memS[((u >> 1) & 7) * LM + (w * 16 + (u >> 4)) * 8 + (u & 1) * 4]) = make_uint2(v[2 * k], v[2 * k + 1]);
      }
    }
    {
      const int r = t >> 4, c = (t & 15) * 8;
      *(u32x4*)(&kS[r * LQ + c]) = kpre;
      const u16* vp = (const u16*)&vpre;
      const int pos = (r < 16) ? (8 * (r >> 2) + (r & 3)) : (8 * ((r - 16) >> 2) + 4 + (r & 3));
#pragma unroll
      for (int q = 0; q < 8; ++q) vT[(c + q) * LV + pos] = vp[q];
    }
    __syncthreads();
    STAMP(1);
    {
      f32x4 acc[3] = {{0.f, 0.f, 0.f, 0.f}, {0.f, 0.f, 0.f, 0.f}, {0.f, 0.f, 0.f, 0.f}};
      bf16x8 a[8];
#pragma unroll
      for (int ks = 0; ks < 8; ++ks) a[ks] = *(const bf16x8*)(&memS[(lane & 7) * LM + 256 * wk + 32 * ks + 8 * fq]);
      __builtin_amdgcn_sched_barrier(0);
#pragma unroll
      for (int ks = 0; ks < 8; ++ks)
#pragma unroll
        for (int m = 0; m < 3; ++m) acc[m] = __builtin_amdgcn_mfma_f32_16x16x32_bf16(a[ks], wf[m][ks], acc[m], 0, 0, 0);
      if (fq < 2) {
#pragma unroll
        for (int m = 0; m < 3; ++m)
#pragma unroll
          for (int r = 0; r < 4; ++r) red[(w * 8 + fq * 4 + r) * 48 + m * 16 + fr] = acc[m][r];
      }
    }
    __syncthreads();
    if (t < NG_QKV / 2) {
      const int r = t / 24, cq = t % 24;
      const int m = cq >> 3, c = (cq & 7) * 4, g = c >> 4;
      float4 v4 = make_float4(0.f, 0.f, 0.f, 0.f);
#pragma unroll
      for (int ww = 0; ww < 4; ++ww) { const float4 q4 = *(const float4*)(&red[((g * 4 + ww) * 8 + r) * 48 + m * 16 + (c & 15)]); v4.x += q4.x; v4.y += q4.y; v4.z += q4.z; v4.w += q4.w; }
      const u32 pk0 = cvtpk(v4.x, v4.y), pk1 = cvtpk(v4.z, v4.w);
      if (s < NSEG - 1) st_gran2(p.qkvg + ((size_t)(par * NH + h) * 4 + j) * NG_QKV + 2 * t, epoch, pk0, pk1);
      if (m == 1) *(uint2*)(p.KM + (size_t)(s * MM + r) * HD + h * DH + 32 * j + c) = make_uint2(pk0, pk1);
      if (m == 2) *(uint2*)(p.VM + (size_t)(s * MM + r) * HD + h * DH + 32 * j + c) = make_uint2(pk0, pk1);
    }
    STAMP(2);
    if (s == NSEG - 1) break;
    bf16x8 kx[2][4], vx[8];
#pragma unroll
    for (int ks = 0; ks < 4; ++ks)
#pragma unroll
      for (int n = 0; n < 2; ++n) kx[n][ks] = *(const bf16x8*)(&kS[(n * 16 + fr) * LQ + 32 * ks + 8 * fq]);
    __builtin_amdgcn_sched_barrier(0);
    {
      u32 v[4];
      const int pj = w >> 1;
      const int u0 = (w & 1) * 96 + lane;
      const int u1 = (w & 1) * 96 + 64 + (lane & 31);
      const u32 gbase = (u32)(((par * NH + h) * 4 + pj) * NG_QKV);
      if (!sweep2<2>(rs_qkv, (gbase + u0 * 2) * 8, (u32)((u1 - u0) * 16), epoch, v, tmo)) return;
#pragma unroll
      for (int k = 0; k < 2; ++k) {
        const int u = k ? u1 : u0;
        const int r = u / 24, cq = u % 24, m = cq >> 3, c = 32 * pj + (cq & 7) * 4;
        if (m == 0) *(uint2*)(&qS[r * LQ + c]) = make_uint2(v[2 * k], v[2 * k + 1]);
        else if (m == 1) *(uint2*)(&kS[(32 + r) * LQ + c]) = make_uint2(v[2 * k], v[2 * k + 1]);
        else {
          const int pos = 32 + 8 * (r >> 2) + (r & 3);
          vT[c * LV + pos] = (u16)(v[2 * k] & 0xffffu); vT[(c + 1) * LV + pos] = (u16)(v[2 * k] >> 16);
          vT[(c + 2) * LV + pos] = (u16)(v[2 * k + 1] & 0xffffu); vT[(c + 3) * LV + pos] = (u16)(v[2 * k + 1] >> 16);
        }
      }
    }
    __syncthreads();
    STAMP(3);
    if (w < 4) {
      f32x4 sc[3] = {{0.f, 0.f, 0.f, 0.f}, {0.f, 0.f, 0.f, 0.f}, {0.f, 0.f, 0.f, 0.f}};
      bf16x8 qf[4], km[4];
      uint2 vm[8];
#pragma unroll
      for (int ks = 0; ks < 4; ++ks) {
        qf[ks] = *(const bf16x8*)(&qS[(lane & 7) * LQ + 32 * ks + 8 * fq]);
        km[ks] = *(const bf16x8*)(&kS[(32 + fr) * LQ + 32 * ks + 8 * fq]);
      }
#pragma unroll
      for (int c = 0; c < 8; ++c) vm[c] = *(const uint2*)(&vT[(16 * c + fr) * LV + 32 + 8 * fq]);
      __builtin_amdgcn_sched_barrier(0);
#pragma unroll
      for (int ks = 0; ks < 4; ++ks) {
        sc[0] = __builtin_amdgcn_mfma_f32_16x16x32_bf16(kx[0][ks], qf[ks], sc[0], 0, 0, 0);
        sc[1] = __builtin_amdgcn_mfma_f32_16x16x32_bf16(kx[1][ks], qf[ks], sc[1], 0, 0, 0);
        sc[2] = __builtin_amdgcn_mfma_f32_16x16x32_bf16(km[ks], qf[ks], sc[2], 0, 0, 0);
      }
      __builtin_amdgcn_sched_barrier(0);
#pragma unroll
      for (int c = 0; c < 8; ++c) vx[c] = *(const bf16x8*)(&vT[(16 * c + fr) * LV + 8 * fq]);

      __builtin_amdgcn_sched_barrier(0);
      const float scale = 0.08838834764831845f;
      const bool live2 = fq < 2;
      float xs[3][4];
#pragma unroll
      for (int n = 0; n < 3; ++n)
#pragma unroll
        for (int r = 0; r < 4; r += 2) {
          const u32 a = cvtpk(sc[n][r], sc[n][r + 1]);
          const u32 c2 = cvtpk(lo2f(a) * scale, hi2f(a) * scale);
          xs[n][r] = lo2f(c2); xs[n][r + 1] = hi2f(c2);
        }
      if (!live2) { xs[2][0] = xs[2][1] = xs[2][2] = xs[2][3] = -3.0e38f; }
      float mx = fmaxf(fmaxf(fmaxf(xs[0][0], xs[0][1]), fmaxf(xs[0][2], xs[0][3])), fmaxf(fmaxf(xs[1][0], xs[1][1]), fmaxf(xs[1][2], xs[1][3])));
      mx = fmaxf(mx, fmaxf(fmaxf(xs[2][0], xs[2][1]), fmaxf(xs[2][2], xs[2][3])));
      mx = xrow_max(mx);
      float sm = 0.f;
#pragma unroll
      for (int n = 0; n < 3; ++n)
#pragma unroll
        for (int r = 0; r < 4; ++r) { xs[n][r] = __expf(xs[n][r] - mx); sm += xs[n][r]; }
      sm = xrow_sum(sm);
      const float inv = 1.0f / sm;
      u32x4 pw0, pw1;
      pw0[0] = cvtpk(xs[0][0] * inv, xs[0][1] * inv); pw0[1] = cvtpk(xs[0][2] * inv, xs[0][3] * inv);
      pw0[2] = cvtpk(xs[1][0] * inv, xs[1][1] * inv); pw0[3] = cvtpk(xs[1][2] * inv, xs[1][3] * inv);
      pw1[0] = cvtpk(xs[2][0] * inv, xs[2][1] * inv); pw1[1] = cvtpk(xs[2][2] * inv, xs[2][3] * inv);
      pw1[2] = 0u; pw1[3] = 0u;
      const bf16x8 pf0 = __builtin_bit_cast(bf16x8, pw0), pf1 = __builtin_bit_cast(bf16x8, pw1);
      u32x4 ow[4];
#pragma unroll
      for (int c = 0; c < 8; ++c) {
        const u32x4 vmw = {vm[c].x, vm[c].y, 0u, 0u};
        f32x4 oc = {0.f, 0.f, 0.f, 0.f};
        oc = __builtin_amdgcn_mfma_f32_16x16x32_bf16(vx[c], pf0, oc, 0, 0, 0);
        oc = __builtin_amdgcn_mfma_f32_16x16x32_bf16(__builtin_bit_cast(bf16x8, vmw), pf1, oc, 0, 0, 0);
        ow[c >> 1][(c & 1) * 2] = cvtpk(oc[0], oc[1]); ow[c >> 1][(c & 1) * 2 + 1] = cvtpk(oc[2], oc[3]);
      }
      f32x4 pc[4];
#pragma unroll
      for (int c = 0; c < 4; ++c) pc[c] = (f32x4){0.f, 0.f, 0.f, 0.f};
#pragma unroll
      for (int ks = 0; ks < 4; ++ks)
#pragma unroll
        for (int c = 0; c < 4; ++c) pc[c] = __builtin_amdgcn_mfma_f32_16x16x32_bf16(woS[(c * 4 + ks) * 64], __builtin_bit_cast(bf16x8, ow[ks]), pc[c], 0, 0, 0);
      if (fr < 8) {
#pragma unroll
        for (int c = 0; c < 4; ++c) {
          u64* dst = p.partg + ((size_t)(par * 128 + 32 * j + 8 * w + 2 * c + (fq >> 1)) * NH + h) * 64 + fr * 8 + 4 * (fq & 1);
          st_gran2(dst, epoch, __float_as_uint(pc[c][0]), __float_as_uint(pc[c][1]));
          st_gran2(dst + 2, epoch, __float_as_uint(pc[c][2]), __float_as_uint(pc[c][3]));
        }
      }
    }
    STAMP(4);
    __syncthreads();
    if (w >= 4) {
      u32 v[8];
      const int w4 = w - 4;
      const int hg = lane >> 3, u = lane & 7;
      const u32 gbase = (u32)(((par * 128 + R) * NH + 4 * hg) * 64 + (8 * w4 + u) * 2);
      if (!sweep2<4>(rs_part, gbase * 8, 64 * 8, epoch, v, tmo)) return;
      float f0 = 0.f, f1 = 0.f;
#pragma unroll
      for (int k = 0; k < 4; ++k) { f0 += __uint_as_float(v[2 * k]); f1 += __uint_as_float(v[2 * k + 1]); }
      f0 = ror8_sum(f0); f1 = ror8_sum(f1);
      f0 = xrow_sum(f0); f1 = xrow_sum(f1);
      const u32 pk = cvtpk(f0, f1);
      st_gran(p.memg + (size_t)(par * NREP + hg) * NG_MEM + R * 32 + 8 * w4 + u, epoch, pk);
      STAMP5;
    }
    if (!load_kv(s + 1)) return;
    __syncthreads();
  }
}

__device__ __forceinline__ void worker_role(const FusedP& p, char* smem, int gw) {
  const int t = threadIdx.x;
  u32* tmo = p.ctl;
  for (int i = 0; i < 4; ++i) {
    const int id = gw + 128 * i, rb = id >> 6, n = id & 63;
    const u16* B = (n < 32 ? p.Wk : p.Wv) + (n & 31) * 128;
    u16* C = (n < 32 ? p.Kx : p.Vx) + (size_t)rb * 256 * HD + (n & 31) * 128;
    gemm_tile<256, 128, 4, 2, 3>(smem, p.xn + (size_t)rb * 256 * DM, DM, B, HD, DM, C, HD, nullptr, 0, true);
    asm volatile("s_waitcnt vmcnt(0)" ::: "memory");
    __syncthreads();
    if (t == 0) __hip_atomic_store((gu32*)(p.kvflag + (rb * 64 + n) * 16), 1u, RLX_AGENT);
  }
  for (int id = gw; id < 480; id += 128) {
    if (id < 224) {
      const int rb = id / 28, n = id % 28;
      gemm_tile<256, 128, 4, 2, 3>(smem, p.xn + (size_t)rb * 256 * DM, DM, p.w1 + n * 128, FF, DM, p.Hb + (size_t)rb * 256 * FF + n * 128, FF, p.b1 + n * 128, 2, false);
    } else {
      const int q = id - 224, rb = q >> 5, n = q & 31;
      gemm_tile<256, 128, 4, 2, 3>(smem, p.xn + (size_t)rb * 256 * DM, DM, p.Wq + n * 128, HD, DM, p.Qx + (size_t)rb * 256 * HD + n * 128, HD, nullptr, 0, false);
    }
  }
  asm volatile("s_waitcnt vmcnt(0)" ::: "memory");
  __syncthreads();
  if (t == 0) {
    __builtin_amdgcn_fence(__ATOMIC_RELEASE, "agent");
    asm volatile("s_waitcnt vmcnt(0)" ::: "memory");
    __hip_atomic_fetch_add((gu32*)(p.ctl + 64), 1u, RLX_AGENT);
    for (u32 spins = 0;;) {
      if (__hip_atomic_load((gu32*)(p.ctl + 64), RLX_AGENT) >= 128u) break;
      if (++spins > SPIN_CAP) { __hip_atomic_store((gu32*)tmo, 1u, RLX_AGENT); break; }
      __builtin_amdgcn_s_sleep(16);
    }
    __builtin_amdgcn_fence(__ATOMIC_ACQUIRE, "agent");
    asm volatile("s_waitcnt vmcnt(0)" ::: "memory");
  }
  __syncthreads();
  {
    const int mt = gw >> 3, nt = gw & 7;
    gemm_tile<128, 128, 4, 2, 4>(smem, p.Hb + (size_t)mt * 128 * FF, FF, p.w2 + nt * 128, DM, FF, p.Bm + (size_t)mt * 128 * DM + nt * 128, DM, p.b2 + nt * 128, 1, false);
  }
}

__global__ __launch_bounds__(512, 2) void fused_kernel(FusedP p) {
  extern __shared__ __attribute__((aligned(16))) char smem[];
  const int b = blockIdx.x, xcd = b & 7, ib = b >> 3;
  if (ib < 16) chain_role(p, smem, xcd * 16 + ib, xcd * 4 + (ib >> 2), ib & 3);
  else worker_role(p, smem, (ib - 16) * 8 + xcd);
}

__global__ __launch_bounds__(256) void attn_x_kernel(const u16* __restrict__ Q, const u16* __restrict__ Kx, const u16* __restrict__ Vx,
                                                     const u16* __restrict__ KM, const u16* __restrict__ VM, u16* __restrict__ O) {
  constexpr int LVR = 136;
  __shared__ __attribute__((aligned(16))) u16 vS[4][48 * LVR];
  const int t = threadIdx.x, lane = t & 63, w = __builtin_amdgcn_readfirstlane(t >> 6);
  const int item = blockIdx.x * 4 + w, s = item >> 5, h = item & 31;
  const int fr = lane & 15, fq = lane >> 4;
  u16* vw = vS[w];
  const size_t xoff = (size_t)(s * SEG) * HD + h * DH, moff = (size_t)(s * MM) * HD + h * DH;
#pragma unroll
  for (int i = 0; i < 12; ++i) {
    const int e = lane + i * 64, r = e >> 4, c = (e & 15) * 8;
    u32x4 v = {0u, 0u, 0u, 0u};
    if (r < 32) v = *(const u32x4*)(Vx + xoff + (size_t)r * HD + c);
    else if (r < 40) v = *(const u32x4*)(VM + moff + (size_t)(r - 32) * HD + c);
    *(u32x4*)(&vw[r * LVR + c]) = v;
  }
  bf16x8 qf[2][4], kf[3][4];
#pragma unroll
  for (int ks = 0; ks < 4; ++ks) {
#pragma unroll
    for (int n = 0; n < 2; ++n) {
      qf[n][ks] = *(const bf16x8*)(Q + xoff + (size_t)(16 * n + fr) * HD + 32 * ks + 8 * fq);
      kf[n][ks] = *(const bf16x8*)(Kx + xoff + (size_t)(16 * n + fr) * HD + 32 * ks + 8 * fq);
    }
    kf[2][ks] = *(const bf16x8*)(KM + moff + (size_t)(fr & 7) * HD + 32 * ks + 8 * fq);
  }
  asm volatile("s_waitcnt lgkmcnt(0)" ::: "memory");
  __builtin_amdgcn_wave_barrier();
  bf16x8 vf[8][2];
  const int tq = fr >> 2, tp = fr & 3;
#pragma unroll
  for (int c = 0; c < 8; ++c) {
    const s16x4 a0 = __builtin_amdgcn_ds_read_tr16_b64_v4i16((LAS s16x4*)(&vw[(4 * fq + tq) * LVR + 16 * c + 4 * tp]));
    const s16x4 a1 = __builtin_amdgcn_ds_read_tr16_b64_v4i16((LAS s16x4*)(&vw[(16 + 4 * fq + tq) * LVR + 16 * c + 4 * tp]));
    const s16x4 a2 = __builtin_amdgcn_ds_read_tr16_b64_v4i16((LAS s16x4*)(&vw[(32 + 4 * fq + tq) * LVR + 16 * c + 4 * tp]));
    vf[c][0] = (bf16x8){a0[0], a0[1], a0[2], a0[3], a1[0], a1[1], a1[2], a1[3]};
    vf[c][1] = (bf16x8){a2[0], a2[1], a2[2], a2[3], 0, 0, 0, 0};
  }
  const float scale = 0.08838834764831845f;
#pragma unroll
  for (int qt = 0; qt < 2; ++qt) {
    f32x4 sc[3] = {{0.f, 0.f, 0.f, 0.f}, {0.f, 0.f, 0.f, 0.f}, {0.f, 0.f, 0.f, 0.f}};
#pragma unroll
    for (int ks = 0; ks < 4; ++ks)
#pragma unroll
      for (int n = 0; n < 3; ++n) sc[n] = __builtin_amdgcn_mfma_f32_16x16x32_bf16(kf[n][ks], qf[qt][ks], sc[n], 0, 0, 0);
    const int qi = 16 * qt + fr;
    float xs[3][4];
#pragma unroll
    for (int n = 0; n < 3; ++n)
#pragma unroll
      for (int r = 0; r < 4; r += 2) {
        const u32 a = cvtpk(sc[n][r], sc[n][r + 1]);
        const u32 c2 = cvtpk(lo2f(a) * scale, hi2f(a) * scale);
        xs[n][r] = lo2f(c2); xs[n][r + 1] = hi2f(c2);
      }
    float mx = -3.0e38f;
#pragma unroll
    for (int n = 0; n < 3; ++n)
#pragma unroll
      for (int r = 0; r < 4; ++r) {
        const bool live = (n < 2) ? (16 * n + 4 * fq + r <= qi) : (fq < 2);
        if (!live) xs[n][r] = -3.0e38f;
        mx = fmaxf(mx, xs[n][r]);
      }
    mx = xrow_max(mx);
    float sm = 0.f;
#pragma unroll
    for (int n = 0; n < 3; ++n)
#pragma unroll
      for (int r = 0; r < 4; ++r) { xs[n][r] = __expf(xs[n][r] - mx); sm += xs[n][r]; }
    sm = xrow_sum(sm);
    const float inv = 1.0f / sm;
    u32x4 pw0, pw1;
    pw0[0] = cvtpk(xs[0][0] * inv, xs[0][1] * inv); pw0[1] = cvtpk(xs[0][2] * inv, xs[0][3] * inv);
    pw0[2] = cvtpk(xs[1][0] * inv, xs[1][1] * inv); pw0[3] = cvtpk(xs[1][2] * inv, xs[1][3] * inv);
    pw1[0] = cvtpk(xs[2][0] * inv, xs[2][1] * inv); pw1[1] = cvtpk(xs[2][2] * inv, xs[2][3] * inv);
    pw1[2] = 0u; pw1[3] = 0u;
    const bf16x8 pf0 = __builtin_bit_cast(bf16x8, pw0), pf1 = __builtin_bit_cast(bf16x8, pw1);
    u16* orow = O + xoff + (size_t)qi * HD + 4 * fq;
#pragma unroll
    for (int c = 0; c < 8; ++c) {
      f32x4 oc = {0.f, 0.f, 0.f, 0.f};
      oc = __builtin_amdgcn_mfma_f32_16x16x32_bf16(vf[c][0], pf0, oc, 0, 0, 0);
      oc = __builtin_amdgcn_mfma_f32_16x16x32_bf16(vf[c][1], pf1, oc, 0, 0, 0);
      *(uint2*)(orow + 16 * c) = make_uint2(cvtpk(oc[0], oc[1]), cvtpk(oc[2], oc[3]));
    }
  }
}

extern "C" void kernel_launch(void* const* d_in, const int* in_sizes, int n_in,
                              void* d_out, int out_size, void* d_ws, size_t ws_size,
                              hipStream_t stream) {
  const u16* x = (const u16*)d_in[0];
  const u16* mem0 = (const u16*)d_in[1];
  const u16* Wq = (const u16*)d_in[2];
  const u16* Wk = (const u16*)d_in[3];
  const u16* Wv = (const u16*)d_in[4];
  const u16* Wo = (const u16*)d_in[5];
  const u16* w1 = (const u16*)d_in[8];
  const u16* b1 = (const u16*)d_in[9];
  const u16* w2 = (const u16*)d_in[10];
  const u16* b2 = (const u16*)d_in[11];
  u16* out = (u16*)d_out;

  char* ws = (char*)d_ws;
  size_t off = 0;
  auto carve = [&](size_t bytes) { char* q = ws + off; off += (bytes + 255) & ~(size_t)255; return q; };
  u64* memg = (u64*)carve((size_t)2 * NREP * NG_MEM * 8);
  u64* qkvg = (u64*)carve((size_t)2 * NH * 4 * NG_QKV * 8);
  u64* partg = (u64*)carve((size_t)2 * 128 * NH * 64 * 8);
  u32* kvflag = (u32*)carve((size_t)512 * 64);
  u32* ctl = (u32*)carve(1024);
  const size_t sync_bytes = off;
  u16* xn = (u16*)carve((size_t)S_ * DM * 2);
  u16* Qx = (u16*)carve((size_t)S_ * HD * 2);
  u16* Kx = (u16*)carve((size_t)S_ * HD * 2);
  u16* Vx = (u16*)carve((size_t)S_ * HD * 2);
  u16* Hb = (u16*)carve((size_t)S_ * FF * 2);
  u16* Ox = (u16*)carve((size_t)S_ * HD * 2);
  u16* KM = (u16*)carve((size_t)NSEG * MM * HD * 2);
  u16* VM = (u16*)carve((size_t)NSEG * MM * HD * 2);
  u16* Bm = (u16*)carve((size_t)S_ * DM * 2);
  u16* Am = (u16*)carve((size_t)S_ * DM * 2);

  (void)hipMemsetAsync(ws, 0, sync_bytes, stream);
  ln_kernel<<<S_, 256, 0, stream>>>(x, xn);
  {
    FusedP f{xn, mem0, Wq, Wk, Wv, Wo, w1, b1, w2, b2, Qx, Kx, Vx, Hb, Bm, KM, VM, memg, qkvg, partg, kvflag, ctl};
    (void)hipFuncSetAttribute((const void*)fused_kernel, hipFuncAttributeMaxDynamicSharedMemorySize, FUSED_LDS);
    fused_kernel<<<256, 512, FUSED_LDS, stream>>>(f);
  }
  attn_x_kernel<<<NSEG * NH / 4, 256, 0, stream>>>(Qx, Kx, Vx, KM, VM, Ox);
  {
    constexpr int LDSW = gemm_lds(WO_BM, WO_BN, WO_WM, WO_WN, WO_NS);
    (void)hipFuncSetAttribute((const void*)gemm_wo_kernel, hipFuncAttributeMaxDynamicSharedMemorySize, LDSW);
    gemm_wo_kernel<<<(S_ / WO_BM) * (DM / WO_BN), WO_WM * WO_WN * 64, LDSW, stream>>>(Ox, Wo, x, Bm, out);
  }
}
```

```cpp
#include <hip/hip_runtime.h>
#include <hip/hip_bf16.h>
#include <stdint.h>

typedef unsigned short u16;
typedef unsigned int u32;
typedef unsigned long long u64;
#define GAS __attribute__((address_space(1)))
#define LAS __attribute__((address_space(3)))
typedef GAS u64 gu64;
typedef GAS u32 gu32;
typedef __attribute__((ext_vector_type(8))) short bf16x8;
typedef __attribute__((ext_vector_type(4))) short s16x4;
typedef __attribute__((ext_vector_type(4))) float f32x4;
typedef __attribute__((ext_vector_type(4))) unsigned int u32x4;
#define RLX_AGENT __ATOMIC_RELAXED, __HIP_MEMORY_SCOPE_AGENT

constexpr int S_ = 2048, DM = 1024, NH = 32, DH = 128, HD = 4096, MM = 8, SEG = 32, NSEG = 64, FF = 3584;

__device__ __forceinline__ float bf2f(u16 v) { return __uint_as_float(((u32)v) << 16); }
__device__ __forceinline__ u16 f2bf(float f) { u32 u = __float_as_uint(f); u += 0x7fffu + ((u >> 16) & 1u); return (u16)(u >> 16); }
__device__ __forceinline__ float rbf(float f) { return bf2f(f2bf(f)); }
typedef __bf16 hbf2 __attribute__((ext_vector_type(2)));
typedef float f32x2 __attribute__((ext_vector_type(2)));
__device__ __forceinline__ u32 cvtpk(float a, float b) { f32x2 v = {a, b}; return __builtin_bit_cast(u32, __builtin_convertvector(v, hbf2)); }
__device__ __forceinline__ float lo2f(u32 v) { return __uint_as_float(v << 16); }
__device__ __forceinline__ float hi2f(u32 v) { return __uint_as_float(v & 0xffff0000u); }

__device__ __forceinline__ float xrow_max(float v) {
  auto a = __builtin_amdgcn_permlane16_swap(__float_as_uint(v), __float_as_uint(v), false, false);
  v = fmaxf(__uint_as_float(a[0]), __uint_as_float(a[1]));
  auto c = __builtin_amdgcn_permlane32_swap(__float_as_uint(v), __float_as_uint(v), false, false);
  return fmaxf(__uint_as_float(c[0]), __uint_as_float(c[1]));
}
__device__ __forceinline__ float xrow_sum(float v) {
  auto a = __builtin_amdgcn_permlane16_swap(__float_as_uint(v), __float_as_uint(v), false, false);
  v = __uint_as_float(a[0]) + __uint_as_float(a[1]);
  auto c = __builtin_amdgcn_permlane32_swap(__float_as_uint(v), __float_as_uint(v), false, false);
  return __uint_as_float(c[0]) + __uint_as_float(c[1]);
}
__device__ __forceinline__ float ror8_sum(float v) {
  return v + __builtin_bit_cast(float, __builtin_amdgcn_update_dpp(0, __builtin_bit_cast(int, v), 0x128, 0xf, 0xf, false));
}

__global__ __launch_bounds__(256) void ln_kernel(const u16* __restrict__ x, u16* __restrict__ xn, u32x4* __restrict__ zero_base, int zero_units) {
  __shared__ float red[8];
  const int row = blockIdx.x, t = threadIdx.x, lane = t & 63, w = t >> 6;
  for (int i = row * 256 + t; i < zero_units; i += S_ * 256) zero_base[i] = (u32x4){0u, 0u, 0u, 0u};
  const ushort4 v = *(const ushort4*)(x + (size_t)row * DM + t * 4);
  float f0 = bf2f(v.x), f1 = bf2f(v.y), f2 = bf2f(v.z), f3 = bf2f(v.w);
  float s = (f0 + f1) + (f2 + f3);
#pragma unroll
  for (int m = 32; m >= 1; m >>= 1) s += __shfl_xor(s, m);
  if (lane == 0) red[w] = s;
  __syncthreads();
  const float mean = ((red[0] + red[1]) + (red[2] + red[3])) * (1.0f / DM);
  float d0 = f0 - mean, d1 = f1 - mean, d2 = f2 - mean, d3 = f3 - mean;
  float q = (d0 * d0 + d1 * d1) + (d2 * d2 + d3 * d3);
#pragma unroll
  for (int m = 32; m >= 1; m >>= 1) q += __shfl_xor(q, m);
  if (lane == 0) red[4 + w] = q;
  __syncthreads();
  const float var = ((red[4] + red[5]) + (red[6] + red[7])) * (1.0f / DM);
  const float mb = rbf(mean), vb = rbf(var);
  const float den = rbf(sqrtf(rbf(vb + 1e-5f)));
  ushort4 o;
  o.x = f2bf(rbf(f0 - mb) / den); o.y = f2bf(rbf(f1 - mb) / den);
  o.z = f2bf(rbf(f2 - mb) / den); o.w = f2bf(rbf(f3 - mb) / den);
  *(ushort4*)(xn + (size_t)row * DM + t * 4) = o;
}

__device__ __forceinline__ float gelu_tanh(float x) {
  const float k0 = 0.7978845608028654f, k1 = 0.044715f;
  const float u = k0 * (x + k1 * x * x * x);
  return 0.5f * x * (1.0f + tanhf(u));
}
__device__ __forceinline__ void glds16(const void* gsrc, u32 lds_dst) {
  u32 keep;
  asm volatile("s_mov_b32 %0, m0\n\ts_mov_b32 m0, %2\n\ts_nop 0\n\tglobal_load_lds_dwordx4 %1, off\n\ts_mov_b32 m0, %0"
               : "=&s"(keep) : "v"(gsrc), "s"(lds_dst) : "memory");
}
template <int N> __device__ __forceinline__ void wait_vm() { asm volatile("s_waitcnt vmcnt(%0)" :: "n"(N) : "memory"); }
__device__ __forceinline__ void st16_sc1(void* p, u32x4 v) {
  asm volatile("global_store_dwordx4 %0, %1, off sc1\n\ts_nop 1" :: "v"(p), "v"(v) : "memory");
}
constexpr int gemm_lds(int BM, int BN, int WM, int WN, int NSTAGE) {
  const int st = NSTAGE * (BM * 64 * 2 + 64 * BN * 2), ep = WM * WN * (BM / WM) * (BN / WN + 8) * 2;
  return st > ep ? st : ep;
}
template <int BM, int BN, int WM, int WN, int NSTAGE>
__device__ __forceinline__ void gemm_tile(char* smem, const u16* A, int lda, const u16* B, int ldb, int K,
                                          u16* C, int ldc, const u16* bias, int EPI, bool WT,
                                          const u16* rx = nullptr, const u16* rb = nullptr) {
  constexpr int NW = WM * WN, NT = NW * 64, BK = 64;
  constexpr int TM = BM / WM / 16, TN = BN / WN / 16;
  constexpr int A_BYTES = BM * BK * 2, B_BYTES = BK * BN * 2, STAGE = A_BYTES + B_BYTES;
  constexpr int NSUB = BN / 16;
  const int t = threadIdx.x, lane = t & 63, w = __builtin_amdgcn_readfirstlane(t >> 6);
  const int wr = w / WN, wc = w % WN, fr = lane & 15, fq = lane >> 4;
  constexpr int AU = BM * 8 / NT, BU = BK * BN / 8 / NT, LPS = AU + BU;
  const u16* asrc[AU]; const u16* bsrc[BU];
#pragma unroll
  for (int i = 0; i < AU; ++i) {
    const int u = i * NT + t, row = u >> 3, c = (u & 7) ^ (row & 7);
    asrc[i] = A + (size_t)row * lda + c * 8;
  }
#pragma unroll
  for (int i = 0; i < BU; ++i) {
    const int u = i * NT + t, hf = u & 1, rp = (u >> 1) & 7, st = u >> 4, kb = st / NSUB, nb = st % NSUB;
    const int r = rp ^ (4 * (kb & 1));
    bsrc[i] = B + (size_t)(kb * 8 + r) * ldb + nb * 16 + hf * 8;
  }
  const u32 lds0 = (u32)(uintptr_t)(LAS char*)smem + (u32)w * 1024u;
  auto stage = [&](int kt, int buf) {
    const u32 base = lds0 + buf * STAGE;
#pragma unroll
    for (int i = 0; i < AU; ++i) glds16(asrc[i] + kt * BK, base + i * NT * 16);
#pragma unroll
    for (int i = 0; i < BU; ++i) glds16(bsrc[i] + (size_t)kt * BK * ldb, base + A_BYTES + i * NT * 16);
  };
  f32x4 acc[TM][TN];
#pragma unroll
  for (int i = 0; i < TM; ++i)
#pragma unroll
    for (int j = 0; j < TN; ++j) acc[i][j] = (f32x4){0.f, 0.f, 0.f, 0.f};
  const int arow0 = (wr * (BM / WM) + fr) * 128;
  const int arx = fr & 7;
  const int tq = (lane & 15) >> 2, tp = lane & 3;
  const int nk = K / BK;
  wait_vm<0>();
#pragma unroll
  for (int k = 0; k < NSTAGE - 1; ++k) stage(k, k);
  int buf = 0;
  for (int kt = 0; kt < nk; ++kt) {
    if (kt + NSTAGE - 1 <= nk) wait_vm<LPS * (NSTAGE - 2)>(); else wait_vm<0>();
    asm volatile("s_waitcnt lgkmcnt(0)" ::: "memory");
    __builtin_amdgcn_s_barrier();
    asm volatile("" ::: "memory");
    if (kt + NSTAGE - 1 < nk) { int nb2 = buf + NSTAGE - 1; if (nb2 >= NSTAGE) nb2 -= NSTAGE; stage(kt + NSTAGE - 1, nb2); }
    const char* sa = smem + buf * STAGE;
    const char* sb = sa + A_BYTES;
#pragma unroll
    for (int ks = 0; ks < 2; ++ks) {
      bf16x8 af[TM], bfr[TN];
#pragma unroll
      for (int i = 0; i < TM; ++i) af[i] = *(const bf16x8*)(sa + arow0 + i * 2048 + (((4 * ks + fq) ^ arx) * 16));
      const int kb = 4 * ks + fq;
      const int rsw = 4 * (kb & 1);
#pragma unroll
      for (int j = 0; j < TN; ++j) {
        const int nb = wc * TN + j;
        const char* sub = sb + (kb * NSUB + nb) * 256 + tp * 8;
        const s16x4 lo = __builtin_amdgcn_ds_read_tr16_b64_v4i16((LAS s16x4*)(sub + ((tq) ^ rsw) * 32));
        const s16x4 hi = __builtin_amdgcn_ds_read_tr16_b64_v4i16((LAS s16x4*)(sub + ((4 + tq) ^ rsw) * 32));
        bfr[j] = (bf16x8){lo[0], lo[1], lo[2], lo[3], hi[0], hi[1], hi[2], hi[3]};
      }
#pragma unroll
      for (int i = 0; i < TM; ++i)
#pragma unroll
        for (int j = 0; j < TN; ++j) acc[i][j] = __builtin_amdgcn_mfma_f32_16x16x32_bf16(bfr[j], af[i], acc[i][j], 0, 0, 0);
    }
    if (++buf == NSTAGE) buf = 0;
  }
  asm volatile("s_waitcnt lgkmcnt(0)" ::: "memory");
  __builtin_amdgcn_s_barrier();
  asm volatile("" ::: "memory");
  constexpr int WROWS = BM / WM, WCOLS = BN / WN, LDW = WCOLS + 8;
  u16* ew = (u16*)smem + w * WROWS * LDW;
#pragma unroll
  for (int j = 0; j < TN; ++j) {
    float bv[4] = {0.f, 0.f, 0.f, 0.f};
    if (EPI) {
      const ushort4 bb = *(const ushort4*)(bias + wc * WCOLS + j * 16 + fq * 4);
      bv[0] = bf2f(bb.x); bv[1] = bf2f(bb.y); bv[2] = bf2f(bb.z); bv[3] = bf2f(bb.w);
    }
#pragma unroll
    for (int i = 0; i < TM; ++i) {
      float v[4];
#pragma unroll
      for (int r = 0; r < 4; ++r) v[r] = acc[i][j][r];
      if (EPI) {
        const u32 a01 = cvtpk(v[0], v[1]), a23 = cvtpk(v[2], v[3]);
        v[0] = lo2f(a01) + bv[0]; v[1] = hi2f(a01) + bv[1]; v[2] = lo2f(a23) + bv[2]; v[3] = hi2f(a23) + bv[3];
        if (EPI == 2) {
          const u32 b01 = cvtpk(v[0], v[1]), b23 = cvtpk(v[2], v[3]);
          v[0] = gelu_tanh(lo2f(b01)); v[1] = gelu_tanh(hi2f(b01)); v[2] = gelu_tanh(lo2f(b23)); v[3] = gelu_tanh(hi2f(b23));
        }
      }
      *(uint2*)(&ew[(i * 16 + fr) * LDW + j * 16 + fq * 4]) = make_uint2(cvtpk(v[0], v[1]), cvtpk(v[2], v[3]));
    }
  }
  asm volatile("s_waitcnt lgkmcnt(0)" ::: "memory");
  __builtin_amdgcn_wave_barrier();
  constexpr int CPR = WCOLS / 8;
#pragma unroll 4
  for (int e = lane; e < WROWS * CPR; e += 64) {
    const int r = e / CPR, c = e % CPR;
    u32x4 v = *(const u32x4*)(&ew[r * LDW + c * 8]);
    const size_t eo = (size_t)(wr * WROWS + r) * ldc + wc * WCOLS + c * 8;
    u16* dst = C + eo;
    if (rx) {
      const u32x4 xv = *(const u32x4*)(rx + eo), bv = *(const u32x4*)(rb + eo);
#pragma unroll
      for (int q = 0; q < 4; ++q) {
        const u32 s1 = cvtpk(lo2f(xv[q]) + lo2f(v[q]), hi2f(xv[q]) + hi2f(v[q]));
        v[q] = cvtpk(lo2f(s1) + lo2f(bv[q]), hi2f(s1) + hi2f(bv[q]));
      }
    }
    if (WT) st16_sc1(dst, v); else *(u32x4*)dst = v;
  }
  asm volatile("s_waitcnt lgkmcnt(0)" ::: "memory");
  __builtin_amdgcn_s_barrier();
  asm volatile("" ::: "memory");
}

#define WO_BM 128
#define WO_BN 64
#define WO_WM 2
#define WO_WN 2
#define WO_NS 4
__global__ __launch_bounds__(WO_WM * WO_WN * 64) void gemm_wo_kernel(const u16* __restrict__ Ox, const u16* __restrict__ Wo, const u16* __restrict__ x,
                                                       const u16* __restrict__ Bm, u16* __restrict__ out) {
  extern __shared__ __attribute__((aligned(16))) char smem[];
  constexpr int NMT = S_ / WO_BM;
  const int xq = blockIdx.x & 7, oq = blockIdx.x >> 3;
  const int mt = oq % NMT, nt = xq + 8 * (oq / NMT);
  const size_t co = (size_t)mt * WO_BM * DM + nt * WO_BN;
  gemm_tile<WO_BM, WO_BN, WO_WM, WO_WN, WO_NS>(smem, Ox + (size_t)mt * WO_BM * HD, HD, Wo + nt * WO_BN, DM, HD, out + co, DM, nullptr, 0, false, x + co, Bm + co);
}

struct FusedP {
  const u16* xn; const u16* mem0; const u16* Wq; const u16* Wk; const u16* Wv; const u16* Wo;
  const u16* w1; const u16* b1; const u16* w2; const u16* b2;
  u16* Qx; u16* Kx; u16* Vx; u16* Hb; u16* Bm; u16* KM; u16* VM;
  u64* memg; u64* qkvg; u64* partg; u32* kvflag; u32* ctl;
};
constexpr int NG_MEM = 4096;
constexpr int NG_QKV = 384;
constexpr u32 SPIN_CAP = 2000000u;
constexpr int NREP = 8;
#define H1_DELAY 8

__device__ __forceinline__ void st_gran(u64* g, u32 tag, u32 val) {
  __hip_atomic_store((gu64*)g, ((u64)tag << 32) | (u64)val, RLX_AGENT);
}
__device__ __forceinline__ void st_gran2(u64* g, u32 tag, u32 v0, u32 v1) {
  const u32x4 d = {v0, tag, v1, tag};
  asm volatile("global_store_dwordx4 %0, %1, off sc1\n\ts_nop 1" :: "v"((gu64*)g), "v"(d) : "memory");
}
__device__ __forceinline__ bool spin_fail(u32& spins, u32* tmo) {
  ++spins;
  if (spins > SPIN_CAP) { __hip_atomic_store((gu32*)tmo, 1u, RLX_AGENT); return true; }
  if ((spins & 1023u) == 0u) { if (__builtin_amdgcn_readfirstlane(__hip_atomic_load((gu32*)tmo, RLX_AGENT))) return true; }
  return false;
}
template <int N>
__device__ __forceinline__ bool sweep2(__amdgpu_buffer_rsrc_t rs, u32 byte_off, u32 byte_stride, u32 tag, u32 (&v)[2 * N], u32* tmo) {
  for (u32 spins = 0;;) {
    bool ok = true;
#pragma unroll
    for (int k = 0; k < N; ++k) {
      const u32x4 x = __builtin_bit_cast(u32x4, __builtin_amdgcn_raw_buffer_load_b128(rs, byte_off + k * byte_stride, 0, 16));
      v[2 * k] = x[0]; v[2 * k + 1] = x[2]; ok &= (x[1] == tag) & (x[3] == tag);
    }
    if (__all(ok)) return true;
    if (spin_fail(spins, tmo)) return false;
  }
}
__device__ __forceinline__ bool wait_flag(const u32* f, u32* tmo) {
  for (u32 spins = 0;;) {
    if (__builtin_amdgcn_readfirstlane(__hip_atomic_load((gu32*)f, RLX_AGENT))) return true;
    if (spin_fail(spins, tmo)) return false;
    __builtin_amdgcn_s_sleep(8);
  }
}

#define STAMP(i) do { } while (0)
#define STAMP5 do { } while (0)

constexpr int C_LM = 1032, C_LQ = 136, C_LV = 72;
constexpr int C_OFF_WO = 0;
constexpr int C_OFF_MEM = C_OFF_WO + 65536;
constexpr int C_OFF_RED = C_OFF_MEM + 8 * C_LM * 2;
constexpr int C_OFF_Q = C_OFF_RED + 8 * 8 * 48 * 4;
constexpr int C_OFF_K = C_OFF_Q + 8 * C_LQ * 2;
constexpr int C_OFF_V = C_OFF_K + 48 * C_LQ * 2;
constexpr int C_LDS = C_OFF_V + 128 * C_LV * 2;
constexpr int W_LDS_A = gemm_lds(256, 128, 4, 2, 3), W_LDS_B = gemm_lds(128, 128, 4, 2, 4);
constexpr int FUSED_LDS = (C_LDS > W_LDS_A ? (C_LDS > W_LDS_B ? C_LDS : W_LDS_B) : (W_LDS_A > W_LDS_B ? W_LDS_A : W_LDS_B));
__device__ __forceinline__ float ror4_sum(float v) {
  return v + __builtin_bit_cast(float, __builtin_amdgcn_update_dpp(0, __builtin_bit_cast(int, v), 0x124, 0xf, 0xf, false));
}

__device__ __forceinline__ void chain_role(const FusedP& p, char* smem, int cb, int h, int j) {
  constexpr int LM = C_LM, LQ = C_LQ, LV = C_LV;
  u16* memS = (u16*)(smem + C_OFF_MEM);
  float* red = (float*)(smem + C_OFF_RED);
  u16* qS = (u16*)(smem + C_OFF_Q);
  u16* kS = (u16*)(smem + C_OFF_K);
  u16* vT = (u16*)(smem + C_OFF_V);
  const int t = threadIdx.x, lane = t & 63, w = __builtin_amdgcn_readfirstlane(t >> 6);
  const int wk = w & 3, wc = w >> 2;
  bf16x8* woS = (bf16x8*)(smem + C_OFF_WO) + (w & 3) * 16 * 64 + lane;
  const int R = h * 4 + j;
  const int fr = lane & 15, fq = lane >> 4;
  u32* tmo = p.ctl;
  const __amdgpu_buffer_rsrc_t rs_mem = __builtin_amdgcn_make_buffer_rsrc((void*)p.memg, 0, 2 * NREP * NG_MEM * 8, 0x00020000);
  const __amdgpu_buffer_rsrc_t rs_qkv = __builtin_amdgcn_make_buffer_rsrc((void*)p.qkvg, 0, 2 * NH * 4 * NG_QKV * 8, 0x00020000);
  const __amdgpu_buffer_rsrc_t rs_part = __builtin_amdgcn_make_buffer_rsrc((void*)p.partg, 0, 2 * 128 * NH * 64 * 8, 0x00020000);
  const __amdgpu_buffer_rsrc_t rs_k = __builtin_amdgcn_make_buffer_rsrc((void*)p.Kx, 0, S_ * HD * 2, 0x00020000);
  const __amdgpu_buffer_rsrc_t rs_v = __builtin_amdgcn_make_buffer_rsrc((void*)p.Vx, 0, S_ * HD * 2, 0x00020000);

  bf16x8 wf[3][8];
  {
    const u16* Ws[3] = {p.Wq, p.Wk, p.Wv};
#pragma unroll
    for (int s = 0; s < 8; ++s) {
#pragma unroll
      for (int m = 0; m < 3; ++m) {
        const u16* src = Ws[m] + (size_t)(256 * wk + 32 * s + 8 * fq) * HD + h * DH + 32 * j + 16 * wc + fr;
#pragma unroll
        for (int i = 0; i < 8; ++i) wf[m][s][i] = (short)src[(size_t)i * HD];
      }
      asm volatile("" : "+v"(wf[0][s]), "+v"(wf[1][s]), "+v"(wf[2][s]) :: "memory");
    }
  }
#pragma unroll
  for (int c2 = 0; c2 < 2; ++c2)
#pragma unroll
    for (int s = 0; s < 4; ++s) {
      const int c = 2 * (w >> 2) + c2;
      bf16x8 f;
#pragma unroll
      for (int i = 0; i < 8; ++i) {
        const int d = 32 * s + ((i < 4) ? (4 * fq + i) : (16 + 4 * fq + i - 4));
        f[i] = (short)p.Wo[(size_t)(h * DH + d) * DM + 256 * j + 64 * (w & 3) + 16 * c + fr];
      }
      woS[(c * 4 + s) * 64] = f;
      asm volatile("" ::: "memory");
    }
  for (int e = t; e < 128 * LV; e += 512) vT[e] = 0;
  for (int e = t; e < 48 * LQ; e += 512) kS[e] = 0;
  u32x4 kpre, vpre;
  auto load_kv = [&](int seg) -> bool {
    if ((seg & 7) == 0) {
      if (!wait_flag(p.kvflag + ((seg >> 3) * 64 + h) * 16, tmo)) return false;
      if (!wait_flag(p.kvflag + ((seg >> 3) * 64 + 32 + h) * 16, tmo)) return false;
    }
    const int r = t >> 4, c = (t & 15) * 8;
    const u32 off = (u32)(((seg * SEG + r) * HD + h * DH + c) * 2);
    kpre = __builtin_bit_cast(u32x4, __builtin_amdgcn_raw_buffer_load_b128(rs_k, off, 0, 16));
    vpre = __builtin_bit_cast(u32x4, __builtin_amdgcn_raw_buffer_load_b128(rs_v, off, 0, 16));
    return true;
  };
  if (!load_kv(0)) return;
  __syncthreads();

  for (int s = 0; s < NSEG; ++s) {
    const int par = s & 1;
    const u32 epoch = (u32)s + 1u;
    STAMP(0);
    if (s == 0) {
#pragma unroll
      for (int i = 0; i < 2; ++i) {
        const int e = t + i * 512, r = e >> 7, c = (e & 127) * 8;
        *(u32x4*)(&memS[r * LM + c]) = *(const u32x4*)(p.mem0 + r * DM + c);
      }
    } else {
      u32 v[8];
      const u32 gbase = (u32)(((par ^ 1) * NREP + (cb & (NREP - 1))) * NG_MEM + w * 512);
      __builtin_amdgcn_s_sleep(H1_DELAY);
      if (!sweep2<4>(rs_mem, (gbase + lane * 2) * 8, 128 * 8, (u32)s, v, tmo)) return;
#pragma unroll
      for (int k = 0; k < 4; ++k) {
        const int u = lane + k * 64;
        *(uint2*)(&memS[((u >> 1) & 7) * LM + (w * 16 + (u >> 4)) * 8 + (u & 1) * 4]) = make_uint2(v[2 * k], v[2 * k + 1]);
      }
    }
    {
      const int r = t >> 4, c = (t & 15) * 8;
      *(u32x4*)(&kS[r * LQ + c]) = kpre;
      const u16* vp = (const u16*)&vpre;
      const int pos = (r < 16) ? (8 * (r >> 2) + (r & 3)) : (8 * ((r - 16) >> 2) + 4 + (r & 3));
#pragma unroll
      for (int q = 0; q < 8; ++q) vT[(c + q) * LV + pos] = vp[q];
    }
    __syncthreads();
    STAMP(1);
    {
      f32x4 acc[3] = {{0.f, 0.f, 0.f, 0.f}, {0.f, 0.f, 0.f, 0.f}, {0.f, 0.f, 0.f, 0.f}};
      bf16x8 a[8];
#pragma unroll
      for (int ks = 0; ks < 8; ++ks) a[ks] = *(const bf16x8*)(&memS[(lane & 7) * LM + 256 * wk + 32 * ks + 8 * fq]);
      __builtin_amdgcn_sched_barrier(0);
#pragma unroll
      for (int ks = 0; ks < 8; ++ks)
#pragma unroll
        for (int m = 0; m < 3; ++m) acc[m] = __builtin_amdgcn_mfma_f32_16x16x32_bf16(a[ks], wf[m][ks], acc[m], 0, 0, 0);
      if (fq < 2) {
#pragma unroll
        for (int m = 0; m < 3; ++m)
#pragma unroll
          for (int r = 0; r < 4; ++r) red[(w * 8 + fq * 4 + r) * 48 + m * 16 + fr] = acc[m][r];
      }
    }
    __syncthreads();
    if (t < NG_QKV / 2) {
      const int r = t / 24, cq = t % 24;
      const int m = cq >> 3, c = (cq & 7) * 4, g = c >> 4;
      float4 v4 = make_float4(0.f, 0.f, 0.f, 0.f);
#pragma unroll
      for (int ww = 0; ww < 4; ++ww) { const float4 q4 = *(const float4*)(&red[((g * 4 + ww) * 8 + r) * 48 + m * 16 + (c & 15)]); v4.x += q4.x; v4.y += q4.y; v4.z += q4.z; v4.w += q4.w; }
      const u32 pk0 = cvtpk(v4.x, v4.y), pk1 = cvtpk(v4.z, v4.w);
      if (s < NSEG - 1) st_gran2(p.qkvg + ((size_t)(par * NH + h) * 4 + j) * NG_QKV + 2 * t, epoch, pk0, pk1);
      if (m == 1) *(uint2*)(p.KM + (size_t)(s * MM + r) * HD + h * DH + 32 * j + c) = make_uint2(pk0, pk1);
      if (m == 2) *(uint2*)(p.VM + (size_t)(s * MM + r) * HD + h * DH + 32 * j + c) = make_uint2(pk0, pk1);
    }
    STAMP(2);
    if (s == NSEG - 1) break;
    bf16x8 kx[2][4], vx[8];
#pragma unroll
    for (int ks = 0; ks < 4; ++ks)
#pragma unroll
      for (int n = 0; n < 2; ++n) kx[n][ks] = *(const bf16x8*)(&kS[(n * 16 + fr) * LQ + 32 * ks + 8 * fq]);
    __builtin_amdgcn_sched_barrier(0);
    {
      u32 v[4];
      const int pj = w >> 1;
      const int u0 = (w & 1) * 96 + lane;
      const int u1 = (w & 1) * 96 + 64 + (lane & 31);
      const u32 gbase = (u32)(((par * NH + h) * 4 + pj) * NG_QKV);
      if (!sweep2<2>(rs_qkv, (gbase + u0 * 2) * 8, (u32)((u1 - u0) * 16), epoch, v, tmo)) return;
#pragma unroll
      for (int k = 0; k < 2; ++k) {
        const int u = k ? u1 : u0;
        const int r = u / 24, cq = u % 24, m = cq >> 3, c = 32 * pj + (cq & 7) * 4;
        if (m == 0) *(uint2*)(&qS[r * LQ + c]) = make_uint2(v[2 * k], v[2 * k + 1]);
        else if (m == 1) *(uint2*)(&kS[(32 + r) * LQ + c]) = make_uint2(v[2 * k], v[2 * k + 1]);
        else {
          const int pos = 32 + 8 * (r >> 2) + (r & 3);
          vT[c * LV + pos] = (u16)(v[2 * k] & 0xffffu); vT[(c + 1) * LV + pos] = (u16)(v[2 * k] >> 16);
          vT[(c + 2) * LV + pos] = (u16)(v[2 * k + 1] & 0xffffu); vT[(c + 3) * LV + pos] = (u16)(v[2 * k + 1] >> 16);
        }
      }
    }
    __syncthreads();
    STAMP(3);
    if (w < 4) {
      f32x4 sc[3] = {{0.f, 0.f, 0.f, 0.f}, {0.f, 0.f, 0.f, 0.f}, {0.f, 0.f, 0.f, 0.f}};
      bf16x8 qf[4], km[4];
      uint2 vm[8];
#pragma unroll
      for (int ks = 0; ks < 4; ++ks) {
        qf[ks] = *(const bf16x8*)(&qS[(lane & 7) * LQ + 32 * ks + 8 * fq]);
        km[ks] = *(const bf16x8*)(&kS[(32 + fr) * LQ + 32 * ks + 8 * fq]);
      }
#pragma unroll
      for (int c = 0; c < 8; ++c) vm[c] = *(const uint2*)(&vT[(16 * c + fr) * LV + 32 + 8 * fq]);
      __builtin_amdgcn_sched_barrier(0);
#pragma unroll
      for (int ks = 0; ks < 4; ++ks) {
        sc[0] = __builtin_amdgcn_mfma_f32_16x16x32_bf16(kx[0][ks], qf[ks], sc[0], 0, 0, 0);
        sc[1] = __builtin_amdgcn_mfma_f32_16x16x32_bf16(kx[1][ks], qf[ks], sc[1], 0, 0, 0);
        sc[2] = __builtin_amdgcn_mfma_f32_16x16x32_bf16(km[ks], qf[ks], sc[2], 0, 0, 0);
      }
      __builtin_amdgcn_sched_barrier(0);
#pragma unroll
      for (int c = 0; c < 8; ++c) vx[c] = *(const bf16x8*)(&vT[(16 * c + fr) * LV + 8 * fq]);

      __builtin_amdgcn_sched_barrier(0);
      const float scale = 0.08838834764831845f;
      const bool live2 = fq < 2;
      float xs[3][4];
#pragma unroll
      for (int n = 0; n < 3; ++n)
#pragma unroll
        for (int r = 0; r < 4; r += 2) {
          const u32 a = cvtpk(sc[n][r], sc[n][r + 1]);
          const u32 c2 = cvtpk(lo2f(a) * scale, hi2f(a) * scale);
          xs[n][r] = lo2f(c2); xs[n][r + 1] = hi2f(c2);
        }
      if (!live2) { xs[2][0] = xs[2][1] = xs[2][2] = xs[2][3] = -3.0e38f; }
      float mx = fmaxf(fmaxf(fmaxf(xs[0][0], xs[0][1]), fmaxf(xs[0][2], xs[0][3])), fmaxf(fmaxf(xs[1][0], xs[1][1]), fmaxf(xs[1][2], xs[1][3])));
      mx = fmaxf(mx, fmaxf(fmaxf(xs[2][0], xs[2][1]), fmaxf(xs[2][2], xs[2][3])));
      mx = xrow_max(mx);
      float sm = 0.f;
#pragma unroll
      for (int n = 0; n < 3; ++n)
#pragma unroll
        for (int r = 0; r < 4; ++r) { xs[n][r] = __expf(xs[n][r] - mx); sm += xs[n][r]; }
      sm = xrow_sum(sm);
      const float inv = 1.0f / sm;
      u32x4 pw0, pw1;
      pw0[0] = cvtpk(xs[0][0] * inv, xs[0][1] * inv); pw0[1] = cvtpk(xs[0][2] * inv, xs[0][3] * inv);
      pw0[2] = cvtpk(xs[1][0] * inv, xs[1][1] * inv); pw0[3] = cvtpk(xs[1][2] * inv, xs[1][3] * inv);
      pw1[0] = cvtpk(xs[2][0] * inv, xs[2][1] * inv); pw1[1] = cvtpk(xs[2][2] * inv, xs[2][3] * inv);
      pw1[2] = 0u; pw1[3] = 0u;
      const bf16x8 pf0 = __builtin_bit_cast(bf16x8, pw0), pf1 = __builtin_bit_cast(bf16x8, pw1);
      u32x4 ow[4];
#pragma unroll
      for (int c = 0; c < 8; ++c) {
        const u32x4 vmw = {vm[c].x, vm[c].y, 0u, 0u};
        f32x4 oc = {0.f, 0.f, 0.f, 0.f};
        oc = __builtin_amdgcn_mfma_f32_16x16x32_bf16(vx[c], pf0, oc, 0, 0, 0);
        oc = __builtin_amdgcn_mfma_f32_16x16x32_bf16(__builtin_bit_cast(bf16x8, vmw), pf1, oc, 0, 0, 0);
        ow[c >> 1][(c & 1) * 2] = cvtpk(oc[0], oc[1]); ow[c >> 1][(c & 1) * 2 + 1] = cvtpk(oc[2], oc[3]);
      }
      f32x4 pc[4];
#pragma unroll
      for (int c = 0; c < 4; ++c) pc[c] = (f32x4){0.f, 0.f, 0.f, 0.f};
#pragma unroll
      for (int ks = 0; ks < 4; ++ks)
#pragma unroll
        for (int c = 0; c < 4; ++c) pc[c] = __builtin_amdgcn_mfma_f32_16x16x32_bf16(woS[(c * 4 + ks) * 64], __builtin_bit_cast(bf16x8, ow[ks]), pc[c], 0, 0, 0);
      if (fr < 8) {
#pragma unroll
        for (int c = 0; c < 4; ++c) {
          u64* dst = p.partg + ((size_t)(par * 128 + 32 * j + 8 * w + 2 * c + (fq >> 1)) * NH + h) * 64 + fr * 8 + 4 * (fq & 1);
          st_gran2(dst, epoch, __float_as_uint(pc[c][0]), __float_as_uint(pc[c][1]));
          st_gran2(dst + 2, epoch, __float_as_uint(pc[c][2]), __float_as_uint(pc[c][3]));
        }
      }
    }
    STAMP(4);
    __syncthreads();
    if (w >= 4) {
      u32 v[8];
      const int w4 = w - 4;
      const int hg = lane >> 3, u = lane & 7;
      const u32 gbase = (u32)(((par * 128 + R) * NH + 4 * hg) * 64 + (8 * w4 + u) * 2);
      if (!sweep2<4>(rs_part, gbase * 8, 64 * 8, epoch, v, tmo)) return;
      float f0 = 0.f, f1 = 0.f;
#pragma unroll
      for (int k = 0; k < 4; ++k) { f0 += __uint_as_float(v[2 * k]); f1 += __uint_as_float(v[2 * k + 1]); }
      f0 = ror8_sum(f0); f1 = ror8_sum(f1);
      f0 = xrow_sum(f0); f1 = xrow_sum(f1);
      const u32 pk = cvtpk(f0, f1);
      st_gran(p.memg + (size_t)(par * NREP + hg) * NG_MEM + R * 32 + 8 * w4 + u, epoch, pk);
      STAMP5;
    }
    if (!load_kv(s + 1)) return;
    __syncthreads();
  }
}

__device__ __forceinline__ void worker_role(const FusedP& p, char* smem, int gw) {
  const int t = threadIdx.x;
  u32* tmo = p.ctl;
  for (int i = 0; i < 4; ++i) {
    const int id = gw + 128 * i, rb = id >> 6, n = id & 63;
    const u16* B = (n < 32 ? p.Wk : p.Wv) + (n & 31) * 128;
    u16* C = (n < 32 ? p.Kx : p.Vx) + (size_t)rb * 256 * HD + (n & 31) * 128;
    gemm_tile<256, 128, 4, 2, 3>(smem, p.xn + (size_t)rb * 256 * DM, DM, B, HD, DM, C, HD, nullptr, 0, true);
    asm volatile("s_waitcnt vmcnt(0)" ::: "memory");
    __syncthreads();
    if (t == 0) __hip_atomic_store((gu32*)(p.kvflag + (rb * 64 + n) * 16), 1u, RLX_AGENT);
  }
  for (int id = gw; id < 480; id += 128) {
    if (id < 224) {
      const int rb = id / 28, n = id % 28;
      gemm_tile<256, 128, 4, 2, 3>(smem, p.xn + (size_t)rb * 256 * DM, DM, p.w1 + n * 128, FF, DM, p.Hb + (size_t)rb * 256 * FF + n * 128, FF, p.b1 + n * 128, 2, false);
    } else {
      const int q = id - 224, rb = q >> 5, n = q & 31;
      gemm_tile<256, 128, 4, 2, 3>(smem, p.xn + (size_t)rb * 256 * DM, DM, p.Wq + n * 128, HD, DM, p.Qx + (size_t)rb * 256 * HD + n * 128, HD, nullptr, 0, false);
    }
  }
  asm volatile("s_waitcnt vmcnt(0)" ::: "memory");
  __syncthreads();
  if (t == 0) {
    __builtin_amdgcn_fence(__ATOMIC_RELEASE, "agent");
    asm volatile("s_waitcnt vmcnt(0)" ::: "memory");
    __hip_atomic_fetch_add((gu32*)(p.ctl + 64), 1u, RLX_AGENT);
    for (u32 spins = 0;;) {
      if (__hip_atomic_load((gu32*)(p.ctl + 64), RLX_AGENT) >= 128u) break;
      if (++spins > SPIN_CAP) { __hip_atomic_store((gu32*)tmo, 1u, RLX_AGENT); break; }
      __builtin_amdgcn_s_sleep(16);
    }
    __builtin_amdgcn_fence(__ATOMIC_ACQUIRE, "agent");
    asm volatile("s_waitcnt vmcnt(0)" ::: "memory");
  }
  __syncthreads();
  {
    const int mt = gw >> 3, nt = gw & 7;
    gemm_tile<128, 128, 4, 2, 4>(smem, p.Hb + (size_t)mt * 128 * FF, FF, p.w2 + nt * 128, DM, FF, p.Bm + (size_t)mt * 128 * DM + nt * 128, DM, p.b2 + nt * 128, 1, false);
  }
}

__global__ __launch_bounds__(512, 2) void fused_kernel(FusedP p) {
  extern __shared__ __attribute__((aligned(16))) char smem[];
  const int b = blockIdx.x, xcd = b & 7, ib = b >> 3;
  if (ib < 16) chain_role(p, smem, xcd * 16 + ib, xcd * 4 + (ib >> 2), ib & 3);
  else worker_role(p, smem, (ib - 16) * 8 + xcd);
}

__global__ __launch_bounds__(256) void attn_x_kernel(const u16* __restrict__ Q, const u16* __restrict__ Kx, const u16* __restrict__ Vx,
                                                     const u16* __restrict__ KM, const u16* __restrict__ VM, u16* __restrict__ O) {
  constexpr int LVR = 136;
  __shared__ __attribute__((aligned(16))) u16 vS[4][48 * LVR];
  const int t = threadIdx.x, lane = t & 63, w = __builtin_amdgcn_readfirstlane(t >> 6);
  const int item = blockIdx.x * 4 + w, s = item >> 5, h = item & 31;
  const int fr = lane & 15, fq = lane >> 4;
  u16* vw = vS[w];
  const size_t xoff = (size_t)(s * SEG) * HD + h * DH, moff = (size_t)(s * MM) * HD + h * DH;
#pragma unroll
  for (int i = 0; i < 12; ++i) {
    const int e = lane + i * 64, r = e >> 4, c = (e & 15) * 8;
    u32x4 v = {0u, 0u, 0u, 0u};
    if (r < 32) v = *(const u32x4*)(Vx + xoff + (size_t)r * HD + c);
    else if (r < 40) v = *(const u32x4*)(VM + moff + (size_t)(r - 32) * HD + c);
    *(u32x4*)(&vw[r * LVR + c]) = v;
  }
  bf16x8 qf[2][4], kf[3][4];
#pragma unroll
  for (int ks = 0; ks < 4; ++ks) {
#pragma unroll
    for (int n = 0; n < 2; ++n) {
      qf[n][ks] = *(const bf16x8*)(Q + xoff + (size_t)(16 * n + fr) * HD + 32 * ks + 8 * fq);
      kf[n][ks] = *(const bf16x8*)(Kx + xoff + (size_t)(16 * n + fr) * HD + 32 * ks + 8 * fq);
    }
    kf[2][ks] = *(const bf16x8*)(KM + moff + (size_t)(fr & 7) * HD + 32 * ks + 8 * fq);
  }
  asm volatile("s_waitcnt lgkmcnt(0)" ::: "memory");
  __builtin_amdgcn_wave_barrier();
  bf16x8 vf[8][2];
  const int tq = fr >> 2, tp = fr & 3;
#pragma unroll
  for (int c = 0; c < 8; ++c) {
    const s16x4 a0 = __builtin_amdgcn_ds_read_tr16_b64_v4i16((LAS s16x4*)(&vw[(4 * fq + tq) * LVR + 16 * c + 4 * tp]));
    const s16x4 a1 = __builtin_amdgcn_ds_read_tr16_b64_v4i16((LAS s16x4*)(&vw[(16 + 4 * fq + tq) * LVR + 16 * c + 4 * tp]));
    const s16x4 a2 = __builtin_amdgcn_ds_read_tr16_b64_v4i16((LAS s16x4*)(&vw[(32 + 4 * fq + tq) * LVR + 16 * c + 4 * tp]));
    vf[c][0] = (bf16x8){a0[0], a0[1], a0[2], a0[3], a1[0], a1[1], a1[2], a1[3]};
    vf[c][1] = (bf16x8){a2[0], a2[1], a2[2], a2[3], 0, 0, 0, 0};
  }
  const float scale = 0.08838834764831845f;
#pragma unroll
  for (int qt = 0; qt < 2; ++qt) {
    f32x4 sc[3] = {{0.f, 0.f, 0.f, 0.f}, {0.f, 0.f, 0.f, 0.f}, {0.f, 0.f, 0.f, 0.f}};
#pragma unroll
    for (int ks = 0; ks < 4; ++ks)
#pragma unroll
      for (int n = 0; n < 3; ++n) sc[n] = __builtin_amdgcn_mfma_f32_16x16x32_bf16(kf[n][ks], qf[qt][ks], sc[n], 0, 0, 0);
    const int qi = 16 * qt + fr;
    float xs[3][4];
#pragma unroll
    for (int n = 0; n < 3; ++n)
#pragma unroll
      for (int r = 0; r < 4; r += 2) {
        const u32 a = cvtpk(sc[n][r], sc[n][r + 1]);
        const u32 c2 = cvtpk(lo2f(a) * scale, hi2f(a) * scale);
        xs[n][r] = lo2f(c2); xs[n][r + 1] = hi2f(c2);
      }
    float mx = -3.0e38f;
#pragma unroll
    for (int n = 0; n < 3; ++n)
#pragma unroll
      for (int r = 0; r < 4; ++r) {
        const bool live = (n < 2) ? (16 * n + 4 * fq + r <= qi) : (fq < 2);
        if (!live) xs[n][r] = -3.0e38f;
        mx = fmaxf(mx, xs[n][r]);
      }
    mx = xrow_max(mx);
    float sm = 0.f;
#pragma unroll
    for (int n = 0; n < 3; ++n)
#pragma unroll
      for (int r = 0; r < 4; ++r) { xs[n][r] = __expf(xs[n][r] - mx); sm += xs[n][r]; }
    sm = xrow_sum(sm);
    const float inv = 1.0f / sm;
    u32x4 pw0, pw1;
    pw0[0] = cvtpk(xs[0][0] * inv, xs[0][1] * inv); pw0[1] = cvtpk(xs[0][2] * inv, xs[0][3] * inv);
    pw0[2] = cvtpk(xs[1][0] * inv, xs[1][1] * inv); pw0[3] = cvtpk(xs[1][2] * inv, xs[1][3] * inv);
    pw1[0] = cvtpk(xs[2][0] * inv, xs[2][1] * inv); pw1[1] = cvtpk(xs[2][2] * inv, xs[2][3] * inv);
    pw1[2] = 0u; pw1[3] = 0u;
    const bf16x8 pf0 = __builtin_bit_cast(bf16x8, pw0), pf1 = __builtin_bit_cast(bf16x8, pw1);
    u16* orow = O + xoff + (size_t)qi * HD + 4 * fq;
#pragma unroll
    for (int c = 0; c < 8; ++c) {
      f32x4 oc = {0.f, 0.f, 0.f, 0.f};
      oc = __builtin_amdgcn_mfma_f32_16x16x32_bf16(vf[c][0], pf0, oc, 0, 0, 0);
      oc = __builtin_amdgcn_mfma_f32_16x16x32_bf16(vf[c][1], pf1, oc, 0, 0, 0);
      *(uint2*)(orow + 16 * c) = make_uint2(cvtpk(oc[0], oc[1]), cvtpk(oc[2], oc[3]));
    }
  }
}

extern "C" void kernel_launch(void* const* d_in, const int* in_sizes, int n_in,
                              void* d_out, int out_size, void* d_ws, size_t ws_size,
                              hipStream_t stream) {
  const u16* x = (const u16*)d_in[0];
  const u16* mem0 = (const u16*)d_in[1];
  const u16* Wq = (const u16*)d_in[2];
  const u16* Wk = (const u16*)d_in[3];
  const u16* Wv = (const u16*)d_in[4];
  const u16* Wo = (const u16*)d_in[5];
  const u16* w1 = (const u16*)d_in[8];
  const u16* b1 = (const u16*)d_in[9];
  const u16* w2 = (const u16*)d_in[10];
  const u16* b2 = (const u16*)d_in[11];
  u16* out = (u16*)d_out;

  char* ws = (char*)d_ws;
  size_t off = 0;
  auto carve = [&](size_t bytes) { char* q = ws + off; off += (bytes + 255) & ~(size_t)255; return q; };
  u64* memg = (u64*)carve((size_t)2 * NREP * NG_MEM * 8);
  u64* qkvg = (u64*)carve((size_t)2 * NH * 4 * NG_QKV * 8);
  u64* partg = (u64*)carve((size_t)2 * 128 * NH * 64 * 8);
  u32* kvflag = (u32*)carve((size_t)512 * 64);
  u32* ctl = (u32*)carve(1024);
  const size_t sync_bytes = off;
  u16* xn = (u16*)carve((size_t)S_ * DM * 2);
  u16* Qx = (u16*)carve((size_t)S_ * HD * 2);
  u16* Kx = (u16*)carve((size_t)S_ * HD * 2);
  u16* Vx = (u16*)carve((size_t)S_ * HD * 2);
  u16* Hb = (u16*)carve((size_t)S_ * FF * 2);
  u16* Ox = (u16*)carve((size_t)S_ * HD * 2);
  u16* KM = (u16*)carve((size_t)NSEG * MM * HD * 2);
  u16* VM = (u16*)carve((size_t)NSEG * MM * HD * 2);
  u16* Bm = (u16*)carve((size_t)S_ * DM * 2);

  ln_kernel<<<S_, 256, 0, stream>>>(x, xn, (u32x4*)ws, (int)(sync_bytes / 16));
  {
    FusedP f{xn, mem0, Wq, Wk, Wv, Wo, w1, b1, w2, b2, Qx, Kx, Vx, Hb, Bm, KM, VM, memg, qkvg, partg, kvflag, ctl};
    (void)hipFuncSetAttribute((const void*)fused_kernel, hipFuncAttributeMaxDynamicSharedMemorySize, FUSED_LDS);
    fused_kernel<<<256, 512, FUSED_LDS, stream>>>(f);
  }
  attn_x_kernel<<<NSEG * NH / 4, 256, 0, stream>>>(Qx, Kx, Vx, KM, VM, Ox);
  {
    constexpr int LDSW = gemm_lds(WO_BM, WO_BN, WO_WM, WO_WN, WO_NS);
    (void)hipFuncSetAttribute((const void*)gemm_wo_kernel, hipFuncAttributeMaxDynamicSharedMemorySize, LDSW);
    gemm_wo_kernel<<<(S_ / WO_BM) * (DM / WO_BN), WO_WM * WO_WN * 64, LDSW, stream>>>(Ox, Wo, x, Bm, out);
  }
}
```

```cpp
#include <hip/hip_runtime.h>
#include <hip/hip_bf16.h>
#include <stdint.h>

typedef unsigned short u16;
typedef unsigned int u32;
typedef unsigned long long u64;
#define GAS __attribute__((address_space(1)))
#define LAS __attribute__((address_space(3)))
typedef GAS u64 gu64;
typedef GAS u32 gu32;
typedef __attribute__((ext_vector_type(8))) short bf16x8;
typedef __attribute__((ext_vector_type(4))) short s16x4;
typedef __attribute__((ext_vector_type(4))) float f32x4;
typedef __attribute__((ext_vector_type(4))) unsigned int u32x4;
#define RLX_AGENT __ATOMIC_RELAXED, __HIP_MEMORY_SCOPE_AGENT

constexpr int S_ = 2048, DM = 1024, NH = 32, DH = 128, HD = 4096, MM = 8, SEG = 32, NSEG = 64, FF = 3584;

__device__ __forceinline__ float bf2f(u16 v) { return __uint_as_float(((u32)v) << 16); }
__device__ __forceinline__ u16 f2bf(float f) { u32 u = __float_as_uint(f); u += 0x7fffu + ((u >> 16) & 1u); return (u16)(u >> 16); }
__device__ __forceinline__ float rbf(float f) { return bf2f(f2bf(f)); }
typedef __bf16 hbf2 __attribute__((ext_vector_type(2)));
typedef float f32x2 __attribute__((ext_vector_type(2)));
__device__ __forceinline__ u32 cvtpk(float a, float b) { f32x2 v = {a, b}; return __builtin_bit_cast(u32, __builtin_convertvector(v, hbf2)); }
__device__ __forceinline__ float lo2f(u32 v) { return __uint_as_float(v << 16); }
__device__ __forceinline__ float hi2f(u32 v) { return __uint_as_float(v & 0xffff0000u); }

__device__ __forceinline__ float xrow_max(float v) {
  auto a = __builtin_amdgcn_permlane16_swap(__float_as_uint(v), __float_as_uint(v), false, false);
  v = fmaxf(__uint_as_float(a[0]), __uint_as_float(a[1]));
  auto c = __builtin_amdgcn_permlane32_swap(__float_as_uint(v), __float_as_uint(v), false, false);
  return fmaxf(__uint_as_float(c[0]), __uint_as_float(c[1]));
}
__device__ __forceinline__ float xrow_sum(float v) {
  auto a = __builtin_amdgcn_permlane16_swap(__float_as_uint(v), __float_as_uint(v), false, false);
  v = __uint_as_float(a[0]) + __uint_as_float(a[1]);
  auto c = __builtin_amdgcn_permlane32_swap(__float_as_uint(v), __float_as_uint(v), false, false);
  return __uint_as_float(c[0]) + __uint_as_float(c[1]);
}
__device__ __forceinline__ float ror8_sum(float v) {
  return v + __builtin_bit_cast(float, __builtin_amdgcn_update_dpp(0, __builtin_bit_cast(int, v), 0x128, 0xf, 0xf, false));
}

__global__ __launch_bounds__(256) void ln_kernel(const u16* __restrict__ x, u16* __restrict__ xn, u32x4* __restrict__ zero_base, int zero_units) {
  __shared__ float red[8];
  const int row = blockIdx.x, t = threadIdx.x, lane = t & 63, w = t >> 6;
  for (int i = row * 256 + t; i < zero_units; i += S_ * 256) zero_base[i] = (u32x4){0u, 0u, 0u, 0u};
  const ushort4 v = *(const ushort4*)(x + (size_t)row * DM + t * 4);
  float f0 = bf2f(v.x), f1 = bf2f(v.y), f2 = bf2f(v.z), f3 = bf2f(v.w);
  float s = (f0 + f1) + (f2 + f3);
#pragma unroll
  for (int m = 32; m >= 1; m >>= 1) s += __shfl_xor(s, m);
  if (lane == 0) red[w] = s;
  __syncthreads();
  const float mean = ((red[0] + red[1]) + (red[2] + red[3])) * (1.0f / DM);
  float d0 = f0 - mean, d1 = f1 - mean, d2 = f2 - mean, d3 = f3 - mean;
  float q = (d0 * d0 + d1 * d1) + (d2 * d2 + d3 * d3);
#pragma unroll
  for (int m = 32; m >= 1; m >>= 1) q += __shfl_xor(q, m);
  if (lane == 0) red[4 + w] = q;
  __syncthreads();
  const float var = ((red[4] + red[5]) + (red[6] + red[7])) * (1.0f / DM);
  const float mb = rbf(mean), vb = rbf(var);
  const float den = rbf(sqrtf(rbf(vb + 1e-5f)));
  ushort4 o;
  o.x = f2bf(rbf(f0 - mb) / den); o.y = f2bf(rbf(f1 - mb) / den);
  o.z = f2bf(rbf(f2 - mb) / den); o.w = f2bf(rbf(f3 - mb) / den);
  *(ushort4*)(xn + (size_t)row * DM + t * 4) = o;
}

__device__ __forceinline__ float gelu_tanh(float x) {
  const float k0 = 0.7978845608028654f, k1 = 0.044715f;
  const float u = k0 * (x + k1 * x * x * x);
  return 0.5f * x * (1.0f + tanhf(u));
}
__device__ __forceinline__ void glds16(const void* gsrc, u32 lds_dst) {
  u32 keep;
  asm volatile("s_mov_b32 %0, m0\n\ts_mov_b32 m0, %2\n\ts_nop 0\n\tglobal_load_lds_dwordx4 %1, off\n\ts_mov_b32 m0, %0"
               : "=&s"(keep) : "v"(gsrc), "s"(lds_dst) : "memory");
}
template <int N> __device__ __forceinline__ void wait_vm() { asm volatile("s_waitcnt vmcnt(%0)" :: "n"(N) : "memory"); }
__device__ __forceinline__ void st16_sc1(void* p, u32x4 v) {
  asm volatile("global_store_dwordx4 %0, %1, off sc1\n\ts_nop 1" :: "v"(p), "v"(v) : "memory");
}
constexpr int gemm_lds(int BM, int BN, int WM, int WN, int NSTAGE) {
  const int st = NSTAGE * (BM * 64 * 2 + 64 * BN * 2), ep = WM * WN * (BM / WM) * (BN / WN + 8) * 2;
  return st > ep ? st : ep;
}
template <int BM, int BN, int WM, int WN, int NSTAGE>
__device__ __forceinline__ void gemm_tile(char* smem, const u16* A, int lda, const u16* B, int ldb, int K,
                                          u16* C, int ldc, const u16* bias, int EPI, bool WT,
                                          const u16* rx = nullptr, const u16* rb = nullptr) {
  constexpr int NW = WM * WN, NT = NW * 64, BK = 64;
  constexpr int TM = BM / WM / 16, TN = BN / WN / 16;
  constexpr int A_BYTES = BM * BK * 2, B_BYTES = BK * BN * 2, STAGE = A_BYTES + B_BYTES;
  constexpr int NSUB = BN / 16;
  const int t = threadIdx.x, lane = t & 63, w = __builtin_amdgcn_readfirstlane(t >> 6);
  const int wr = w / WN, wc = w % WN, fr = lane & 15, fq = lane >> 4;
  constexpr int AU = BM * 8 / NT, BU = BK * BN / 8 / NT, LPS = AU + BU;
  const u16* asrc[AU]; const u16* bsrc[BU];
#pragma unroll
  for (int i = 0; i < AU; ++i) {
    const int u = i * NT + t, row = u >> 3, c = (u & 7) ^ (row & 7);
    asrc[i] = A + (size_t)row * lda + c * 8;
  }
#pragma unroll
  for (int i = 0; i < BU; ++i) {
    const int u = i * NT + t, hf = u & 1, rp = (u >> 1) & 7, st = u >> 4, kb = st / NSUB, nb = st % NSUB;
    const int r = rp ^ (4 * (kb & 1));
    bsrc[i] = B + (size_t)(kb * 8 + r) * ldb + nb * 16 + hf * 8;
  }
  const u32 lds0 = (u32)(uintptr_t)(LAS char*)smem + (u32)w * 1024u;
  auto stage = [&](int kt, int buf) {
    const u32 base = lds0 + buf * STAGE;
#pragma unroll
    for (int i = 0; i < AU; ++i) glds16(asrc[i] + kt * BK, base + i * NT * 16);
#pragma unroll
    for (int i = 0; i < BU; ++i) glds16(bsrc[i] + (size_t)kt * BK * ldb, base + A_BYTES + i * NT * 16);
  };
  f32x4 acc[TM][TN];
#pragma unroll
  for (int i = 0; i < TM; ++i)
#pragma unroll
    for (int j = 0; j < TN; ++j) acc[i][j] = (f32x4){0.f, 0.f, 0.f, 0.f};
  const int arow0 = (wr * (BM / WM) + fr) * 128;
  const int arx = fr & 7;
  const int tq = (lane & 15) >> 2, tp = lane & 3;
  const int nk = K / BK;
  wait_vm<0>();
#pragma unroll
  for (int k = 0; k < NSTAGE - 1; ++k) stage(k, k);
  int buf = 0;
  for (int kt = 0; kt < nk; ++kt) {
    if (kt + NSTAGE - 1 <= nk) wait_vm<LPS * (NSTAGE - 2)>(); else wait_vm<0>();
    asm volatile("s_waitcnt lgkmcnt(0)" ::: "memory");
    __builtin_amdgcn_s_barrier();
    asm volatile("" ::: "memory");
    if (kt + NSTAGE - 1 < nk) { int nb2 = buf + NSTAGE - 1; if (nb2 >= NSTAGE) nb2 -= NSTAGE; stage(kt + NSTAGE - 1, nb2); }
    const char* sa = smem + buf * STAGE;
    const char* sb = sa + A_BYTES;
#pragma unroll
    for (int ks = 0; ks < 2; ++ks) {
      bf16x8 af[TM], bfr[TN];
#pragma unroll
      for (int i = 0; i < TM; ++i) af[i] = *(const bf16x8*)(sa + arow0 + i * 2048 + (((4 * ks + fq) ^ arx) * 16));
      const int kb = 4 * ks + fq;
      const int rsw = 4 * (kb & 1);
#pragma unroll
      for (int j = 0; j < TN; ++j) {
        const int nb = wc * TN + j;
        const char* sub = sb + (kb * NSUB + nb) * 256 + tp * 8;
        const s16x4 lo = __builtin_amdgcn_ds_read_tr16_b64_v4i16((LAS s16x4*)(sub + ((tq) ^ rsw) * 32));
        const s16x4 hi = __builtin_amdgcn_ds_read_tr16_b64_v4i16((LAS s16x4*)(sub + ((4 + tq) ^ rsw) * 32));
        bfr[j] = (bf16x8){lo[0], lo[1], lo[2], lo[3], hi[0], hi[1], hi[2], hi[3]};
      }
#pragma unroll
      for (int i = 0; i < TM; ++i)
#pragma unroll
        for (int j = 0; j < TN; ++j) acc[i][j] = __builtin_amdgcn_mfma_f32_16x16x32_bf16(bfr[j], af[i], acc[i][j], 0, 0, 0);
    }
    if (++buf == NSTAGE) buf = 0;
  }
  asm volatile("s_waitcnt lgkmcnt(0)" ::: "memory");
  __builtin_amdgcn_s_barrier();
  asm volatile("" ::: "memory");
  constexpr int WROWS = BM / WM, WCOLS = BN / WN, LDW = WCOLS + 8;
  u16* ew = (u16*)smem + w * WROWS * LDW;
#pragma unroll
  for (int j = 0; j < TN; ++j) {
    float bv[4] = {0.f, 0.f, 0.f, 0.f};
    if (EPI) {
      const ushort4 bb = *(const ushort4*)(bias + wc * WCOLS + j * 16 + fq * 4);
      bv[0] = bf2f(bb.x); bv[1] = bf2f(bb.y); bv[2] = bf2f(bb.z); bv[3] = bf2f(bb.w);
    }
#pragma unroll
    for (int i = 0; i < TM; ++i) {
      float v[4];
#pragma unroll
      for (int r = 0; r < 4; ++r) v[r] = acc[i][j][r];
      if (EPI) {
        const u32 a01 = cvtpk(v[0], v[1]), a23 = cvtpk(v[2], v[3]);
        v[0] = lo2f(a01) + bv[0]; v[1] = hi2f(a01) + bv[1]; v[2] = lo2f(a23) + bv[2]; v[3] = hi2f(a23) + bv[3];
        if (EPI == 2) {
          const u32 b01 = cvtpk(v[0], v[1]), b23 = cvtpk(v[2], v[3]);
          v[0] = gelu_tanh(lo2f(b01)); v[1] = gelu_tanh(hi2f(b01)); v[2] = gelu_tanh(lo2f(b23)); v[3] = gelu_tanh(hi2f(b23));
        }
      }
      *(uint2*)(&ew[(i * 16 + fr) * LDW + j * 16 + fq * 4]) = make_uint2(cvtpk(v[0], v[1]), cvtpk(v[2], v[3]));
    }
  }
  asm volatile("s_waitcnt lgkmcnt(0)" ::: "memory");
  __builtin_amdgcn_wave_barrier();
  constexpr int CPR = WCOLS / 8;
#pragma unroll 4
  for (int e = lane; e < WROWS * CPR; e += 64) {
    const int r = e / CPR, c = e % CPR;
    u32x4 v = *(const u32x4*)(&ew[r * LDW + c * 8]);
    const size_t eo = (size_t)(wr * WROWS + r) * ldc + wc * WCOLS + c * 8;
    u16* dst = C + eo;
    if (rx) {
      const u32x4 xv = *(const u32x4*)(rx + eo), bv = *(const u32x4*)(rb + eo);
#pragma unroll
      for (int q = 0; q < 4; ++q) {
        const u32 s1 = cvtpk(lo2f(xv[q]) + lo2f(v[q]), hi2f(xv[q]) + hi2f(v[q]));
        v[q] = cvtpk(lo2f(s1) + lo2f(bv[q]), hi2f(s1) + hi2f(bv[q]));
      }
    }
    if (WT) st16_sc1(dst, v); else *(u32x4*)dst = v;
  }
  asm volatile("s_waitcnt lgkmcnt(0)" ::: "memory");
  __builtin_amdgcn_s_barrier();
  asm volatile("" ::: "memory");
}

#define WO_BM 128
#define WO_BN 64
#define WO_WM 2
#define WO_WN 2
#define WO_NS 4
__global__ __launch_bounds__(WO_WM * WO_WN * 64) void gemm_wo_kernel(const u16* __restrict__ Ox, const u16* __restrict__ Wo, const u16* __restrict__ x,
                                                       const u16* __restrict__ Bm, u16* __restrict__ out) {
  extern __shared__ __attribute__((aligned(16))) char smem[];
  constexpr int NMT = S_ / WO_BM;
  const int xq = blockIdx.x & 7, oq = blockIdx.x >> 3;
  const int mt = oq % NMT, nt = xq + 8 * (oq / NMT);
  const size_t co = (size_t)mt * WO_BM * DM + nt * WO_BN;
  gemm_tile<WO_BM, WO_BN, WO_WM, WO_WN, WO_NS>(smem, Ox + (size_t)mt * WO_BM * HD, HD, Wo + nt * WO_BN, DM, HD, out + co, DM, nullptr, 0, false, x + co, Bm + co);
}

struct FusedP {
  const u16* xn; const u16* mem0; const u16* Wq; const u16* Wk; const u16* Wv; const u16* Wo;
  const u16* w1; const u16* b1; const u16* w2; const u16* b2;
  u16* Qx; u16* Kx; u16* Vx; u16* Hb; u16* Bm; u16* KM; u16* VM;
  u64* memg; u64* qkvg; u64* partg; u32* kvflag; u32* ctl;
};
constexpr int NG_MEM = 4096;
constexpr int NG_QKV = 384;
constexpr u32 SPIN_CAP = 2000000u;
constexpr int NREP = 8;
#define H1W 4
#define H2W 4
#define H2W0 4
#define H3W 2
#define H1_DELAY 8

__device__ __forceinline__ void st_gran(u64* g, u32 tag, u32 val) {
  __hip_atomic_store((gu64*)g, ((u64)tag << 32) | (u64)val, RLX_AGENT);
}
__device__ __forceinline__ void st_gran2(u64* g, u32 tag, u32 v0, u32 v1) {
  const u32x4 d = {v0, tag, v1, tag};
  asm volatile("global_store_dwordx4 %0, %1, off sc1\n\ts_nop 1" :: "v"((gu64*)g), "v"(d) : "memory");
}
__device__ __forceinline__ bool spin_fail(u32& spins, u32* tmo) {
  ++spins;
  if (spins > SPIN_CAP) { __hip_atomic_store((gu32*)tmo, 1u, RLX_AGENT); return true; }
  if ((spins & 1023u) == 0u) { if (__builtin_amdgcn_readfirstlane(__hip_atomic_load((gu32*)tmo, RLX_AGENT))) return true; }
  return false;
}
template <int N>
__device__ __forceinline__ bool sweep2(__amdgpu_buffer_rsrc_t rs, u32 byte_off, u32 byte_stride, u32 tag, u32 (&v)[2 * N], u32* tmo) {
  for (u32 spins = 0;;) {
    bool ok = true;
#pragma unroll
    for (int k = 0; k < N; ++k) {
      const u32x4 x = __builtin_bit_cast(u32x4, __builtin_amdgcn_raw_buffer_load_b128(rs, byte_off + k * byte_stride, 0, 16));
      v[2 * k] = x[0]; v[2 * k + 1] = x[2]; ok &= (x[1] == tag) & (x[3] == tag);
    }
    if (__all(ok)) return true;
    if (spin_fail(spins, tmo)) return false;
  }
}
__device__ __forceinline__ bool wait_flag(const u32* f, u32* tmo) {
  for (u32 spins = 0;;) {
    if (__builtin_amdgcn_readfirstlane(__hip_atomic_load((gu32*)f, RLX_AGENT))) return true;
    if (spin_fail(spins, tmo)) return false;
    __builtin_amdgcn_s_sleep(8);
  }
}

#define STAMP(i) do { } while (0)
#define STAMP5 do { } while (0)

constexpr int C_LM = 1032, C_LQ = 136, C_LV = 72;
constexpr int C_OFF_WO = 0;
constexpr int C_OFF_MEM = C_OFF_WO + 65536;
constexpr int C_OFF_RED = C_OFF_MEM + 8 * C_LM * 2;
constexpr int C_OFF_Q = C_OFF_RED + 8 * 8 * 48 * 4;
constexpr int C_OFF_K = C_OFF_Q + 8 * C_LQ * 2;
constexpr int C_OFF_V = C_OFF_K + 48 * C_LQ * 2;
constexpr int C_LDS = C_OFF_V + 128 * C_LV * 2;
constexpr int W_LDS_A = gemm_lds(256, 128, 4, 2, 3), W_LDS_B = gemm_lds(128, 128, 4, 2, 4);
constexpr int FUSED_LDS = (C_LDS > W_LDS_A ? (C_LDS > W_LDS_B ? C_LDS : W_LDS_B) : (W_LDS_A > W_LDS_B ? W_LDS_A : W_LDS_B));
__device__ __forceinline__ float ror4_sum(float v) {
  return v + __builtin_bit_cast(float, __builtin_amdgcn_update_dpp(0, __builtin_bit_cast(int, v), 0x124, 0xf, 0xf, false));
}

__device__ __forceinline__ void chain_role(const FusedP& p, char* smem, int cb, int h, int j) {
  constexpr int LM = C_LM, LQ = C_LQ, LV = C_LV;
  u16* memS = (u16*)(smem + C_OFF_MEM);
  float* red = (float*)(smem + C_OFF_RED);
  u16* qS = (u16*)(smem + C_OFF_Q);
  u16* kS = (u16*)(smem + C_OFF_K);
  u16* vT = (u16*)(smem + C_OFF_V);
  const int t = threadIdx.x, lane = t & 63, w = __builtin_amdgcn_readfirstlane(t >> 6);
  const int wk = w & 3, wc = w >> 2;
  bf16x8* woS = (bf16x8*)(smem + C_OFF_WO) + (w & 3) * 16 * 64 + lane;
  const int R = h * 4 + j;
  const int fr = lane & 15, fq = lane >> 4;
  u32* tmo = p.ctl;
  const __amdgpu_buffer_rsrc_t rs_mem = __builtin_amdgcn_make_buffer_rsrc((void*)p.memg, 0, 2 * NREP * NG_MEM * 8, 0x00020000);
  const __amdgpu_buffer_rsrc_t rs_qkv = __builtin_amdgcn_make_buffer_rsrc((void*)p.qkvg, 0, 2 * NH * 4 * NG_QKV * 8, 0x00020000);
  const __amdgpu_buffer_rsrc_t rs_part = __builtin_amdgcn_make_buffer_rsrc((void*)p.partg, 0, 2 * 128 * NH * 64 * 8, 0x00020000);
  const __amdgpu_buffer_rsrc_t rs_k = __builtin_amdgcn_make_buffer_rsrc((void*)p.Kx, 0, S_ * HD * 2, 0x00020000);
  const __amdgpu_buffer_rsrc_t rs_v = __builtin_amdgcn_make_buffer_rsrc((void*)p.Vx, 0, S_ * HD * 2, 0x00020000);

  bf16x8 wf[3][8];
  {
    const u16* Ws[3] = {p.Wq, p.Wk, p.Wv};
#pragma unroll
    for (int s = 0; s < 8; ++s) {
#pragma unroll
      for (int m = 0; m < 3; ++m) {
        const u16* src = Ws[m] + (size_t)(256 * wk + 32 * s + 8 * fq) * HD + h * DH + 32 * j + 16 * wc + fr;
#pragma unroll
        for (int i = 0; i < 8; ++i) wf[m][s][i] = (short)src[(size_t)i * HD];
      }
      asm volatile("" : "+v"(wf[0][s]), "+v"(wf[1][s]), "+v"(wf[2][s]) :: "memory");
    }
  }
#pragma unroll
  for (int c2 = 0; c2 < 2; ++c2)
#pragma unroll
    for (int s = 0; s < 4; ++s) {
      const int c = 2 * (w >> 2) + c2;
      bf16x8 f;
#pragma unroll
      for (int i = 0; i < 8; ++i) {
        const int d = 32 * s + ((i < 4) ? (4 * fq + i) : (16 + 4 * fq + i - 4));
        f[i] = (short)p.Wo[(size_t)(h * DH + d) * DM + 256 * j + 64 * (w & 3) + 16 * c + fr];
      }
      woS[(c * 4 + s) * 64] = f;
      asm volatile("" ::: "memory");
    }
  for (int e = t; e < 128 * LV; e += 512) vT[e] = 0;
  for (int e = t; e < 48 * LQ; e += 512) kS[e] = 0;
  u32x4 kpre, vpre;
  auto load_kv = [&](int seg) -> bool {
    if ((seg & 7) == 0) {
      if (!wait_flag(p.kvflag + ((seg >> 3) * 64 + h) * 16, tmo)) return false;
      if (!wait_flag(p.kvflag + ((seg >> 3) * 64 + 32 + h) * 16, tmo)) return false;
    }
    const int r = t >> 4, c = (t & 15) * 8;
    const u32 off = (u32)(((seg * SEG + r) * HD + h * DH + c) * 2);
    kpre = __builtin_bit_cast(u32x4, __builtin_amdgcn_raw_buffer_load_b128(rs_k, off, 0, 16));
    vpre = __builtin_bit_cast(u32x4, __builtin_amdgcn_raw_buffer_load_b128(rs_v, off, 0, 16));
    return true;
  };
  if (!load_kv(0)) return;
  __syncthreads();

  for (int s = 0; s < NSEG; ++s) {
    const int par = s & 1;
    const u32 epoch = (u32)s + 1u;
    STAMP(0);
    if (s == 0) {
#pragma unroll
      for (int i = 0; i < 2; ++i) {
        const int e = t + i * 512, r = e >> 7, c = (e & 127) * 8;
        *(u32x4*)(&memS[r * LM + c]) = *(const u32x4*)(p.mem0 + r * DM + c);
      }
    } else {
      if (w < H1W) {
        constexpr int NL = 32 / H1W;
        u32 v[2 * NL];
        const u32 gbase = (u32)(((par ^ 1) * NREP + (cb & (NREP - 1))) * NG_MEM + w * (4096 / H1W));
        __builtin_amdgcn_s_sleep(H1_DELAY);
        if (!sweep2<NL>(rs_mem, (gbase + lane * 2) * 8, 128 * 8, (u32)s, v, tmo)) return;
#pragma unroll
        for (int k = 0; k < NL; ++k) {
          const int u = lane + k * 64;
          *(uint2*)(&memS[((u >> 1) & 7) * LM + (w * (128 / H1W) + (u >> 4)) * 8 + (u & 1) * 4]) = make_uint2(v[2 * k], v[2 * k + 1]);
        }
      }
    }
    {
      const int r = t >> 4, c = (t & 15) * 8;
      *(u32x4*)(&kS[r * LQ + c]) = kpre;
      const u16* vp = (const u16*)&vpre;
      const int pos = (r < 16) ? (8 * (r >> 2) + (r & 3)) : (8 * ((r - 16) >> 2) + 4 + (r & 3));
#pragma unroll
      for (int q = 0; q < 8; ++q) vT[(c + q) * LV + pos] = vp[q];
    }
    __syncthreads();
    STAMP(1);
    {
      f32x4 acc[3] = {{0.f, 0.f, 0.f, 0.f}, {0.f, 0.f, 0.f, 0.f}, {0.f, 0.f, 0.f, 0.f}};
      bf16x8 a[8];
#pragma unroll
      for (int ks = 0; ks < 8; ++ks) a[ks] = *(const bf16x8*)(&memS[(lane & 7) * LM + 256 * wk + 32 * ks + 8 * fq]);
      __builtin_amdgcn_sched_barrier(0);
#pragma unroll
      for (int ks = 0; ks < 8; ++ks)
#pragma unroll
        for (int m = 0; m < 3; ++m) acc[m] = __builtin_amdgcn_mfma_f32_16x16x32_bf16(a[ks], wf[m][ks], acc[m], 0, 0, 0);
      if (fq < 2) {
#pragma unroll
        for (int m = 0; m < 3; ++m)
#pragma unroll
          for (int r = 0; r < 4; ++r) red[(w * 8 + fq * 4 + r) * 48 + m * 16 + fr] = acc[m][r];
      }
    }
    __syncthreads();
    if (t < NG_QKV / 2) {
      const int r = t / 24, cq = t % 24;
      const int m = cq >> 3, c = (cq & 7) * 4, g = c >> 4;
      float4 v4 = make_float4(0.f, 0.f, 0.f, 0.f);
#pragma unroll
      for (int ww = 0; ww < 4; ++ww) { const float4 q4 = *(const float4*)(&red[((g * 4 + ww) * 8 + r) * 48 + m * 16 + (c & 15)]); v4.x += q4.x; v4.y += q4.y; v4.z += q4.z; v4.w += q4.w; }
      const u32 pk0 = cvtpk(v4.x, v4.y), pk1 = cvtpk(v4.z, v4.w);
      if (s < NSEG - 1) st_gran2(p.qkvg + ((size_t)(par * NH + h) * 4 + j) * NG_QKV + 2 * t, epoch, pk0, pk1);
      if (m == 1) *(uint2*)(p.KM + (size_t)(s * MM + r) * HD + h * DH + 32 * j + c) = make_uint2(pk0, pk1);
      if (m == 2) *(uint2*)(p.VM + (size_t)(s * MM + r) * HD + h * DH + 32 * j + c) = make_uint2(pk0, pk1);
    }
    STAMP(2);
    if (s == NSEG - 1) break;
    bf16x8 kx[2][4], vx[8];
#pragma unroll
    for (int ks = 0; ks < 4; ++ks)
#pragma unroll
      for (int n = 0; n < 2; ++n) kx[n][ks] = *(const bf16x8*)(&kS[(n * 16 + fr) * LQ + 32 * ks + 8 * fq]);
    __builtin_amdgcn_sched_barrier(0);
    if (w >= H2W0 && w < H2W0 + H2W) {
      constexpr int NL = 12 / H2W;
      u32 v[2 * NL];
      const int wv = w - H2W0;
      const u32 gbase = (u32)((par * NH + h) * 4 * NG_QKV + wv * NL * 128);
      if (!sweep2<NL>(rs_qkv, (gbase + lane * 2) * 8, 128 * 8, epoch, v, tmo)) return;
#pragma unroll
      for (int k = 0; k < NL; ++k) {
        const int ug = wv * NL * 64 + k * 64 + lane, pj = ug / 192, u = ug % 192;
        const int r = u / 24, cq = u % 24, m = cq >> 3, c = 32 * pj + (cq & 7) * 4;
        if (m == 0) *(uint2*)(&qS[r * LQ + c]) = make_uint2(v[2 * k], v[2 * k + 1]);
        else if (m == 1) *(uint2*)(&kS[(32 + r) * LQ + c]) = make_uint2(v[2 * k], v[2 * k + 1]);
        else {
          const int pos = 32 + 8 * (r >> 2) + (r & 3);
          vT[c * LV + pos] = (u16)(v[2 * k] & 0xffffu); vT[(c + 1) * LV + pos] = (u16)(v[2 * k] >> 16);
          vT[(c + 2) * LV + pos] = (u16)(v[2 * k + 1] & 0xffffu); vT[(c + 3) * LV + pos] = (u16)(v[2 * k + 1] >> 16);
        }
      }
    }
    __syncthreads();
    STAMP(3);
    if (w < 4) {
      f32x4 sc[3] = {{0.f, 0.f, 0.f, 0.f}, {0.f, 0.f, 0.f, 0.f}, {0.f, 0.f, 0.f, 0.f}};
      bf16x8 qf[4], km[4];
      uint2 vm[8];
#pragma unroll
      for (int ks = 0; ks < 4; ++ks) {
        qf[ks] = *(const bf16x8*)(&qS[(lane & 7) * LQ + 32 * ks + 8 * fq]);
        km[ks] = *(const bf16x8*)(&kS[(32 + fr) * LQ + 32 * ks + 8 * fq]);
      }
#pragma unroll
      for (int c = 0; c < 8; ++c) vm[c] = *(const uint2*)(&vT[(16 * c + fr) * LV + 32 + 8 * fq]);
      __builtin_amdgcn_sched_barrier(0);
#pragma unroll
      for (int ks = 0; ks < 4; ++ks) {
        sc[0] = __builtin_amdgcn_mfma_f32_16x16x32_bf16(kx[0][ks], qf[ks], sc[0], 0, 0, 0);
        sc[1] = __builtin_amdgcn_mfma_f32_16x16x32_bf16(kx[1][ks], qf[ks], sc[1], 0, 0, 0);
        sc[2] = __builtin_amdgcn_mfma_f32_16x16x32_bf16(km[ks], qf[ks], sc[2], 0, 0, 0);
      }
      __builtin_amdgcn_sched_barrier(0);
#pragma unroll
      for (int c = 0; c < 8; ++c) vx[c] = *(const bf16x8*)(&vT[(16 * c + fr) * LV + 8 * fq]);

      __builtin_amdgcn_sched_barrier(0);
      const float scale = 0.08838834764831845f;
      const bool live2 = fq < 2;
      float xs[3][4];
#pragma unroll
      for (int n = 0; n < 3; ++n)
#pragma unroll
        for (int r = 0; r < 4; r += 2) {
          const u32 a = cvtpk(sc[n][r], sc[n][r + 1]);
          const u32 c2 = cvtpk(lo2f(a) * scale, hi2f(a) * scale);
          xs[n][r] = lo2f(c2); xs[n][r + 1] = hi2f(c2);
        }
      if (!live2) { xs[2][0] = xs[2][1] = xs[2][2] = xs[2][3] = -3.0e38f; }
      float mx = fmaxf(fmaxf(fmaxf(xs[0][0], xs[0][1]), fmaxf(xs[0][2], xs[0][3])), fmaxf(fmaxf(xs[1][0], xs[1][1]), fmaxf(xs[1][2], xs[1][3])));
      mx = fmaxf(mx, fmaxf(fmaxf(xs[2][0], xs[2][1]), fmaxf(xs[2][2], xs[2][3])));
      mx = xrow_max(mx);
      float sm = 0.f;
#pragma unroll
      for (int n = 0; n < 3; ++n)
#pragma unroll
        for (int r = 0; r < 4; ++r) { xs[n][r] = __expf(xs[n][r] - mx); sm += xs[n][r]; }
      sm = xrow_sum(sm);
      const float inv = 1.0f / sm;
      u32x4 pw0, pw1;
      pw0[0] = cvtpk(xs[0][0] * inv, xs[0][1] * inv); pw0[1] = cvtpk(xs[0][2] * inv, xs[0][3] * inv);
      pw0[2] = cvtpk(xs[1][0] * inv, xs[1][1] * inv); pw0[3] = cvtpk(xs[1][2] * inv, xs[1][3] * inv);
      pw1[0] = cvtpk(xs[2][0] * inv, xs[2][1] * inv); pw1[1] = cvtpk(xs[2][2] * inv, xs[2][3] * inv);
      pw1[2] = 0u; pw1[3] = 0u;
      const bf16x8 pf0 = __builtin_bit_cast(bf16x8, pw0), pf1 = __builtin_bit_cast(bf16x8, pw1);
      u32x4 ow[4];
#pragma unroll
      for (int c = 0; c < 8; ++c) {
        const u32x4 vmw = {vm[c].x, vm[c].y, 0u, 0u};
        f32x4 oc = {0.f, 0.f, 0.f, 0.f};
        oc = __builtin_amdgcn_mfma_f32_16x16x32_bf16(vx[c], pf0, oc, 0, 0, 0);
        oc = __builtin_amdgcn_mfma_f32_16x16x32_bf16(__builtin_bit_cast(bf16x8, vmw), pf1, oc, 0, 0, 0);
        ow[c >> 1][(c & 1) * 2] = cvtpk(oc[0], oc[1]); ow[c >> 1][(c & 1) * 2 + 1] = cvtpk(oc[2], oc[3]);
      }
      f32x4 pc[4];
#pragma unroll
      for (int c = 0; c < 4; ++c) pc[c] = (f32x4){0.f, 0.f, 0.f, 0.f};
#pragma unroll
      for (int ks = 0; ks < 4; ++ks)
#pragma unroll
        for (int c = 0; c < 4; ++c) pc[c] = __builtin_amdgcn_mfma_f32_16x16x32_bf16(woS[(c * 4 + ks) * 64], __builtin_bit_cast(bf16x8, ow[ks]), pc[c], 0, 0, 0);
      if (fr < 8) {
#pragma unroll
        for (int c = 0; c < 4; ++c) {
          u64* dst = p.partg + ((size_t)(par * 128 + 32 * j + 8 * w + 2 * c + (fq >> 1)) * NH + h) * 64 + fr * 8 + 4 * (fq & 1);
          st_gran2(dst, epoch, __float_as_uint(pc[c][0]), __float_as_uint(pc[c][1]));
          st_gran2(dst + 2, epoch, __float_as_uint(pc[c][2]), __float_as_uint(pc[c][3]));
        }
      }
    }
    STAMP(4);
    __syncthreads();
    if (w >= 4 && w < 4 + H3W) {
      constexpr int UPW = 32 / H3W;
      constexpr int HG = 64 / UPW, NL = 32 / HG;
      u32 v[2 * NL];
      const int w4 = w - 4;
      const int hg = lane / UPW, u = lane % UPW;
      const u32 gbase = (u32)(((par * 128 + R) * NH + NL * hg) * 64 + (UPW * w4 + u) * 2);
      if (!sweep2<NL>(rs_part, gbase * 8, 64 * 8, epoch, v, tmo)) return;
      float f0 = 0.f, f1 = 0.f;
#pragma unroll
      for (int k = 0; k < NL; ++k) { f0 += __uint_as_float(v[2 * k]); f1 += __uint_as_float(v[2 * k + 1]); }
      if (UPW == 8) { f0 = ror8_sum(f0); f1 = ror8_sum(f1); }
      if (UPW <= 16) { f0 = xrow_sum(f0); f1 = xrow_sum(f1); }
      else {
        auto c0 = __builtin_amdgcn_permlane32_swap(__float_as_uint(f0), __float_as_uint(f0), false, false);
        auto c1 = __builtin_amdgcn_permlane32_swap(__float_as_uint(f1), __float_as_uint(f1), false, false);
        f0 = __uint_as_float(c0[0]) + __uint_as_float(c0[1]); f1 = __uint_as_float(c1[0]) + __uint_as_float(c1[1]);
      }
      const u32 pk = cvtpk(f0, f1);
#pragma unroll
      for (int q = 0; q < NREP / HG; ++q) st_gran(p.memg + (size_t)(par * NREP + q * HG + hg) * NG_MEM + R * 32 + UPW * w4 + u, epoch, pk);
      STAMP5;
    }
    if (!load_kv(s + 1)) return;
    __syncthreads();
  }
}

__device__ __forceinline__ void worker_role(const FusedP& p, char* smem, int gw) {
  const int t = threadIdx.x;
  u32* tmo = p.ctl;
  for (int i = 0; i < 4; ++i) {
    const int id = gw + 128 * i, rb = id >> 6, n = id & 63;
    const u16* B = (n < 32 ? p.Wk : p.Wv) + (n & 31) * 128;
    u16* C = (n < 32 ? p.Kx : p.Vx) + (size_t)rb * 256 * HD + (n & 31) * 128;
    gemm_tile<256, 128, 4, 2, 3>(smem, p.xn + (size_t)rb * 256 * DM, DM, B, HD, DM, C, HD, nullptr, 0, true);
    asm volatile("s_waitcnt vmcnt(0)" ::: "memory");
    __syncthreads();
    if (t == 0) __hip_atomic_store((gu32*)(p.kvflag + (rb * 64 + n) * 16), 1u, RLX_AGENT);
  }
  for (int id = gw; id < 480; id += 128) {
    if (id < 224) {
      const int rb = id / 28, n = id % 28;
      gemm_tile<256, 128, 4, 2, 3>(smem, p.xn + (size_t)rb * 256 * DM, DM, p.w1 + n * 128, FF, DM, p.Hb + (size_t)rb * 256 * FF + n * 128, FF, p.b1 + n * 128, 2, false);
    } else {
      const int q = id - 224, rb = q >> 5, n = q & 31;
      gemm_tile<256, 128, 4, 2, 3>(smem, p.xn + (size_t)rb * 256 * DM, DM, p.Wq + n * 128, HD, DM, p.Qx + (size_t)rb * 256 * HD + n * 128, HD, nullptr, 0, false);
    }
  }
  asm volatile("s_waitcnt vmcnt(0)" ::: "memory");
  __syncthreads();
  if (t == 0) {
    __builtin_amdgcn_fence(__ATOMIC_RELEASE, "agent");
    asm volatile("s_waitcnt vmcnt(0)" ::: "memory");
    __hip_atomic_fetch_add((gu32*)(p.ctl + 64), 1u, RLX_AGENT);
    for (u32 spins = 0;;) {
      if (__hip_atomic_load((gu32*)(p.ctl + 64), RLX_AGENT) >= 128u) break;
      if (++spins > SPIN_CAP) { __hip_atomic_store((gu32*)tmo, 1u, RLX_AGENT); break; }
      __builtin_amdgcn_s_sleep(16);
    }
    __builtin_amdgcn_fence(__ATOMIC_ACQUIRE, "agent");
    asm volatile("s_waitcnt vmcnt(0)" ::: "memory");
  }
  __syncthreads();
  {
    const int mt = gw >> 3, nt = gw & 7;
    gemm_tile<128, 128, 4, 2, 4>(smem, p.Hb + (size_t)mt * 128 * FF, FF, p.w2 + nt * 128, DM, FF, p.Bm + (size_t)mt * 128 * DM + nt * 128, DM, p.b2 + nt * 128, 1, false);
  }
}

__global__ __launch_bounds__(512, 2) void fused_kernel(FusedP p) {
  extern __shared__ __attribute__((aligned(16))) char smem[];
  const int b = blockIdx.x, xcd = b & 7, ib = b >> 3;
  if (ib < 16) chain_role(p, smem, xcd * 16 + ib, xcd * 4 + (ib >> 2), ib & 3);
  else worker_role(p, smem, (ib - 16) * 8 + xcd);
}

__global__ __launch_bounds__(256) void attn_x_kernel(const u16* __restrict__ Q, const u16* __restrict__ Kx, const u16* __restrict__ Vx,
                                                     const u16* __restrict__ KM, const u16* __restrict__ VM, u16* __restrict__ O) {
  constexpr int LVR = 136;
  __shared__ __attribute__((aligned(16))) u16 vS[4][48 * LVR];
  const int t = threadIdx.x, lane = t & 63, w = __builtin_amdgcn_readfirstlane(t >> 6);
  const int item = blockIdx.x * 4 + w, s = item >> 5, h = item & 31;
  const int fr = lane & 15, fq = lane >> 4;
  u16* vw = vS[w];
  const size_t xoff = (size_t)(s * SEG) * HD + h * DH, moff = (size_t)(s * MM) * HD + h * DH;
#pragma unroll
  for (int i = 0; i < 12; ++i) {
    const int e = lane + i * 64, r = e >> 4, c = (e & 15) * 8;
    u32x4 v = {0u, 0u, 0u, 0u};
    if (r < 32) v = *(const u32x4*)(Vx + xoff + (size_t)r * HD + c);
    else if (r < 40) v = *(const u32x4*)(VM + moff + (size_t)(r - 32) * HD + c);
    *(u32x4*)(&vw[r * LVR + c]) = v;
  }
  bf16x8 qf[2][4], kf[3][4];
#pragma unroll
  for (int ks = 0; ks < 4; ++ks) {
#pragma unroll
    for (int n = 0; n < 2; ++n) {
      qf[n][ks] = *(const bf16x8*)(Q + xoff + (size_t)(16 * n + fr) * HD + 32 * ks + 8 * fq);
      kf[n][ks] = *(const bf16x8*)(Kx + xoff + (size_t)(16 * n + fr) * HD + 32 * ks + 8 * fq);
    }
    kf[2][ks] = *(const bf16x8*)(KM + moff + (size_t)(fr & 7) * HD + 32 * ks + 8 * fq);
  }
  asm volatile("s_waitcnt lgkmcnt(0)" ::: "memory");
  __builtin_amdgcn_wave_barrier();
  bf16x8 vf[8][2];
  const int tq = fr >> 2, tp = fr & 3;
#pragma unroll
  for (int c = 0; c < 8; ++c) {
    const s16x4 a0 = __builtin_amdgcn_ds_read_tr16_b64_v4i16((LAS s16x4*)(&vw[(4 * fq + tq) * LVR + 16 * c + 4 * tp]));
    const s16x4 a1 = __builtin_amdgcn_ds_read_tr16_b64_v4i16((LAS s16x4*)(&vw[(16 + 4 * fq + tq) * LVR + 16 * c + 4 * tp]));
    const s16x4 a2 = __builtin_amdgcn_ds_read_tr16_b64_v4i16((LAS s16x4*)(&vw[(32 + 4 * fq + tq) * LVR + 16 * c + 4 * tp]));
    vf[c][0] = (bf16x8){a0[0], a0[1], a0[2], a0[3], a1[0], a1[1], a1[2], a1[3]};
    vf[c][1] = (bf16x8){a2[0], a2[1], a2[2], a2[3], 0, 0, 0, 0};
  }
  const float scale = 0.08838834764831845f;
#pragma unroll
  for (int qt = 0; qt < 2; ++qt) {
    f32x4 sc[3] = {{0.f, 0.f, 0.f, 0.f}, {0.f, 0.f, 0.f, 0.f}, {0.f, 0.f, 0.f, 0.f}};
#pragma unroll
    for (int ks = 0; ks < 4; ++ks)
#pragma unroll
      for (int n = 0; n < 3; ++n) sc[n] = __builtin_amdgcn_mfma_f32_16x16x32_bf16(kf[n][ks], qf[qt][ks], sc[n], 0, 0, 0);
    const int qi = 16 * qt + fr;
    float xs[3][4];
#pragma unroll
    for (int n = 0; n < 3; ++n)
#pragma unroll
      for (int r = 0; r < 4; r += 2) {
        const u32 a = cvtpk(sc[n][r], sc[n][r + 1]);
        const u32 c2 = cvtpk(lo2f(a) * scale, hi2f(a) * scale);
        xs[n][r] = lo2f(c2); xs[n][r + 1] = hi2f(c2);
      }
    float mx = -3.0e38f;
#pragma unroll
    for (int n = 0; n < 3; ++n)
#pragma unroll
      for (int r = 0; r < 4; ++r) {
        const bool live = (n < 2) ? (16 * n + 4 * fq + r <= qi) : (fq < 2);
        if (!live) xs[n][r] = -3.0e38f;
        mx = fmaxf(mx, xs[n][r]);
      }
    mx = xrow_max(mx);
    float sm = 0.f;
#pragma unroll
    for (int n = 0; n < 3; ++n)
#pragma unroll
      for (int r = 0; r < 4; ++r) { xs[n][r] = __expf(xs[n][r] - mx); sm += xs[n][r]; }
    sm = xrow_sum(sm);
    const float inv = 1.0f / sm;
    u32x4 pw0, pw1;
    pw0[0] = cvtpk(xs[0][0] * inv, xs[0][1] * inv); pw0[1] = cvtpk(xs[0][2] * inv, xs[0][3] * inv);
    pw0[2] = cvtpk(xs[1][0] * inv, xs[1][1] * inv); pw0[3] = cvtpk(xs[1][2] * inv, xs[1][3] * inv);
    pw1[0] = cvtpk(xs[2][0] * inv, xs[2][1] * inv); pw1[1] = cvtpk(xs[2][2] * inv, xs[2][3] * inv);
    pw1[2] = 0u; pw1[3] = 0u;
    const bf16x8 pf0 = __builtin_bit_cast(bf16x8, pw0), pf1 = __builtin_bit_cast(bf16x8, pw1);
    u16* orow = O + xoff + (size_t)qi * HD + 4 * fq;
#pragma unroll
    for (int c = 0; c < 8; ++c) {
      f32x4 oc = {0.f, 0.f, 0.f, 0.f};
      oc = __builtin_amdgcn_mfma_f32_16x16x32_bf16(vf[c][0], pf0, oc, 0, 0, 0);
      oc = __builtin_amdgcn_mfma_f32_16x16x32_bf16(vf[c][1], pf1, oc, 0, 0, 0);
      *(uint2*)(orow + 16 * c) = make_uint2(cvtpk(oc[0], oc[1]), cvtpk(oc[2], oc[3]));
    }
  }
}

extern "C" void kernel_launch(void* const* d_in, const int* in_sizes, int n_in,
                              void* d_out, int out_size, void* d_ws, size_t ws_size,
                              hipStream_t stream) {
  const u16* x = (const u16*)d_in[0];
  const u16* mem0 = (const u16*)d_in[1];
  const u16* Wq = (const u16*)d_in[2];
  const u16* Wk = (const u16*)d_in[3];
  const u16* Wv = (const u16*)d_in[4];
  const u16* Wo = (const u16*)d_in[5];
  const u16* w1 = (const u16*)d_in[8];
  const u16* b1 = (const u16*)d_in[9];
  const u16* w2 = (const u16*)d_in[10];
  const u16* b2 = (const u16*)d_in[11];
  u16* out = (u16*)d_out;

  char* ws = (char*)d_ws;
  size_t off = 0;
  auto carve = [&](size_t bytes) { char* q = ws + off; off += (bytes + 255) & ~(size_t)255; return q; };
  u64* memg = (u64*)carve((size_t)2 * NREP * NG_MEM * 8);
  u64* qkvg = (u64*)carve((size_t)2 * NH * 4 * NG_QKV * 8);
  u64* partg = (u64*)carve((size_t)2 * 128 * NH * 64 * 8);
  u32* kvflag = (u32*)carve((size_t)512 * 64);
  u32* ctl = (u32*)carve(1024);
  const size_t sync_bytes = off;
  u16* xn = (u16*)carve((size_t)S_ * DM * 2);
  u16* Qx = (u16*)carve((size_t)S_ * HD * 2);
  u16* Kx = (u16*)carve((size_t)S_ * HD * 2);
  u16* Vx = (u16*)carve((size_t)S_ * HD * 2);
  u16* Hb = (u16*)carve((size_t)S_ * FF * 2);
  u16* Ox = (u16*)carve((size_t)S_ * HD * 2);
  u16* KM = (u16*)carve((size_t)NSEG * MM * HD * 2);
  u16* VM = (u16*)carve((size_t)NSEG * MM * HD * 2);
  u16* Bm = (u16*)carve((size_t)S_ * DM * 2);

  ln_kernel<<<S_, 256, 0, stream>>>(x, xn, (u32x4*)ws, (int)(sync_bytes / 16));
  {
    FusedP f{xn, mem0, Wq, Wk, Wv, Wo, w1, b1, w2, b2, Qx, Kx, Vx, Hb, Bm, KM, VM, memg, qkvg, partg, kvflag, ctl};
    (void)hipFuncSetAttribute((const void*)fused_kernel, hipFuncAttributeMaxDynamicSharedMemorySize, FUSED_LDS);
    fused_kernel<<<256, 512, FUSED_LDS, stream>>>(f);
  }
  attn_x_kernel<<<NSEG * NH / 4, 256, 0, stream>>>(Qx, Kx, Vx, KM, VM, Ox);
  {
    constexpr int LDSW = gemm_lds(WO_BM, WO_BN, WO_WM, WO_WN, WO_NS);
    (void)hipFuncSetAttribute((const void*)gemm_wo_kernel, hipFuncAttributeMaxDynamicSharedMemorySize, LDSW);
    gemm_wo_kernel<<<(S_ / WO_BM) * (DM / WO_BN), WO_WM * WO_WN * 64, LDSW, stream>>>(Ox, Wo, x, Bm, out);
  }
}
```

```cpp
#include <hip/hip_runtime.h>
#include <hip/hip_bf16.h>
#include <stdint.h>

typedef unsigned short u16;
typedef unsigned int u32;
typedef unsigned long long u64;
#define GAS __attribute__((address_space(1)))
#define LAS __attribute__((address_space(3)))
typedef GAS u64 gu64;
typedef GAS u32 gu32;
typedef __attribute__((ext_vector_type(8))) short bf16x8;
typedef __attribute__((ext_vector_type(4))) short s16x4;
typedef __attribute__((ext_vector_type(4))) float f32x4;
typedef __attribute__((ext_vector_type(4))) unsigned int u32x4;
#define RLX_AGENT __ATOMIC_RELAXED, __HIP_MEMORY_SCOPE_AGENT

constexpr int S_ = 2048, DM = 1024, NH = 32, DH = 128, HD = 4096, MM = 8, SEG = 32, NSEG = 64, FF = 3584;

__device__ __forceinline__ float bf2f(u16 v) { return __uint_as_float(((u32)v) << 16); }
__device__ __forceinline__ u16 f2bf(float f) { u32 u = __float_as_uint(f); u += 0x7fffu + ((u >> 16) & 1u); return (u16)(u >> 16); }
__device__ __forceinline__ float rbf(float f) { return bf2f(f2bf(f)); }
typedef __bf16 hbf2 __attribute__((ext_vector_type(2)));
typedef float f32x2 __attribute__((ext_vector_type(2)));
__device__ __forceinline__ u32 cvtpk(float a, float b) { f32x2 v = {a, b}; return __builtin_bit_cast(u32, __builtin_convertvector(v, hbf2)); }
__device__ __forceinline__ float lo2f(u32 v) { return __uint_as_float(v << 16); }
__device__ __forceinline__ float hi2f(u32 v) { return __uint_as_float(v & 0xffff0000u); }

__device__ __forceinline__ float xrow_max(float v) {
  auto a = __builtin_amdgcn_permlane16_swap(__float_as_uint(v), __float_as_uint(v), false, false);
  v = fmaxf(__uint_as_float(a[0]), __uint_as_float(a[1]));
  auto c = __builtin_amdgcn_permlane32_swap(__float_as_uint(v), __float_as_uint(v), false, false);
  return fmaxf(__uint_as_float(c[0]), __uint_as_float(c[1]));
}
__device__ __forceinline__ float xrow_sum(float v) {
  auto a = __builtin_amdgcn_permlane16_swap(__float_as_uint(v), __float_as_uint(v), false, false);
  v = __uint_as_float(a[0]) + __uint_as_float(a[1]);
  auto c = __builtin_amdgcn_permlane32_swap(__float_as_uint(v), __float_as_uint(v), false, false);
  return __uint_as_float(c[0]) + __uint_as_float(c[1]);
}
__device__ __forceinline__ float ror8_sum(float v) {
  return v + __builtin_bit_cast(float, __builtin_amdgcn_update_dpp(0, __builtin_bit_cast(int, v), 0x128, 0xf, 0xf, false));
}

__global__ __launch_bounds__(256) void ln_kernel(const u16* __restrict__ x, u16* __restrict__ xn, u32x4* __restrict__ zero_base, int zero_units) {
  __shared__ float red[8];
  const int row = blockIdx.x, t = threadIdx.x, lane = t & 63, w = t >> 6;
  for (int i = row * 256 + t; i < zero_units; i += S_ * 256) zero_base[i] = (u32x4){0u, 0u, 0u, 0u};
  const ushort4 v = *(const ushort4*)(x + (size_t)row * DM + t * 4);
  float f0 = bf2f(v.x), f1 = bf2f(v.y), f2 = bf2f(v.z), f3 = bf2f(v.w);
  float s = (f0 + f1) + (f2 + f3);
#pragma unroll
  for (int m = 32; m >= 1; m >>= 1) s += __shfl_xor(s, m);
  if (lane == 0) red[w] = s;
  __syncthreads();
  const float mean = ((red[0] + red[1]) + (red[2] + red[3])) * (1.0f / DM);
  float d0 = f0 - mean, d1 = f1 - mean, d2 = f2 - mean, d3 = f3 - mean;
  float q = (d0 * d0 + d1 * d1) + (d2 * d2 + d3 * d3);
#pragma unroll
  for (int m = 32; m >= 1; m >>= 1) q += __shfl_xor(q, m);
  if (lane == 0) red[4 + w] = q;
  __syncthreads();
  const float var = ((red[4] + red[5]) + (red[6] + red[7])) * (1.0f / DM);
  const float mb = rbf(mean), vb = rbf(var);
  const float den = rbf(sqrtf(rbf(vb + 1e-5f)));
  ushort4 o;
  o.x = f2bf(rbf(f0 - mb) / den); o.y = f2bf(rbf(f1 - mb) / den);
  o.z = f2bf(rbf(f2 - mb) / den); o.w = f2bf(rbf(f3 - mb) / den);
  *(ushort4*)(xn + (size_t)row * DM + t * 4) = o;
}

__device__ __forceinline__ float gelu_tanh(float x) {
  const float k0 = 0.7978845608028654f, k1 = 0.044715f;
  const float u = k0 * (x + k1 * x * x * x);
  return 0.5f * x * (1.0f + tanhf(u));
}
__device__ __forceinline__ void glds16(const void* gsrc, u32 lds_dst) {
  u32 keep;
  asm volatile("s_mov_b32 %0, m0\n\ts_mov_b32 m0, %2\n\ts_nop 0\n\tglobal_load_lds_dwordx4 %1, off\n\ts_mov_b32 m0, %0"
               : "=&s"(keep) : "v"(gsrc), "s"(lds_dst) : "memory");
}
template <int N> __device__ __forceinline__ void wait_vm() { asm volatile("s_waitcnt vmcnt(%0)" :: "n"(N) : "memory"); }
__device__ __forceinline__ void st16_sc1(void* p, u32x4 v) {
  asm volatile("global_store_dwordx4 %0, %1, off sc1\n\ts_nop 1" :: "v"(p), "v"(v) : "memory");
}
constexpr int gemm_lds(int BM, int BN, int WM, int WN, int NSTAGE) {
  const int st = NSTAGE * (BM * 64 * 2 + 64 * BN * 2), ep = WM * WN * (BM / WM) * (BN / WN + 8) * 2;
  return st > ep ? st : ep;
}
template <int BM, int BN, int WM, int WN, int NSTAGE>
__device__ __forceinline__ void gemm_tile(char* smem, const u16* A, int lda, const u16* B, int ldb, int K,
                                          u16* C, int ldc, const u16* bias, int EPI, bool WT,
                                          const u16* rx = nullptr, const u16* rb = nullptr) {
  constexpr int NW = WM * WN, NT = NW * 64, BK = 64;
  constexpr int TM = BM / WM / 16, TN = BN / WN / 16;
  constexpr int A_BYTES = BM * BK * 2, B_BYTES = BK * BN * 2, STAGE = A_BYTES + B_BYTES;
  constexpr int NSUB = BN / 16;
  const int t = threadIdx.x, lane = t & 63, w = __builtin_amdgcn_readfirstlane(t >> 6);
  const int wr = w / WN, wc = w % WN, fr = lane & 15, fq = lane >> 4;
  constexpr int AU = BM * 8 / NT, BU = BK * BN / 8 / NT, LPS = AU + BU;
  const u16* asrc[AU]; const u16* bsrc[BU];
#pragma unroll
  for (int i = 0; i < AU; ++i) {
    const int u = i * NT + t, row = u >> 3, c = (u & 7) ^ (row & 7);
    asrc[i] = A + (size_t)row * lda + c * 8;
  }
#pragma unroll
  for (int i = 0; i < BU; ++i) {
    const int u = i * NT + t, hf = u & 1, rp = (u >> 1) & 7, st = u >> 4, kb = st / NSUB, nb = st % NSUB;
    const int r = rp ^ (4 * (kb & 1));
    bsrc[i] = B + (size_t)(kb * 8 + r) * ldb + nb * 16 + hf * 8;
  }
  const u32 lds0 = (u32)(uintptr_t)(LAS char*)smem + (u32)w * 1024u;
  auto stage = [&](int kt, int buf) {
    const u32 base = lds0 + buf * STAGE;
#pragma unroll
    for (int i = 0; i < AU; ++i) glds16(asrc[i] + kt * BK, base + i * NT * 16);
#pragma unroll
    for (int i = 0; i < BU; ++i) glds16(bsrc[i] + (size_t)kt * BK * ldb, base + A_BYTES + i * NT * 16);
  };
  f32x4 acc[TM][TN];
#pragma unroll
  for (int i = 0; i < TM; ++i)
#pragma unroll
    for (int j = 0; j < TN; ++j) acc[i][j] = (f32x4){0.f, 0.f, 0.f, 0.f};
  const int arow0 = (wr * (BM / WM) + fr) * 128;
  const int arx = fr & 7;
  const int tq = (lane & 15) >> 2, tp = lane & 3;
  const int nk = K / BK;
  wait_vm<0>();
#pragma unroll
  for (int k = 0; k < NSTAGE - 1; ++k) stage(k, k);
  int buf = 0;
  for (int kt = 0; kt < nk; ++kt) {
    if (kt + NSTAGE - 1 <= nk) wait_vm<LPS * (NSTAGE - 2)>(); else wait_vm<0>();
    asm volatile("s_waitcnt lgkmcnt(0)" ::: "memory");
    __builtin_amdgcn_s_barrier();
    asm volatile("" ::: "memory");
    if (kt + NSTAGE - 1 < nk) { int nb2 = buf + NSTAGE - 1; if (nb2 >= NSTAGE) nb2 -= NSTAGE; stage(kt + NSTAGE - 1, nb2); }
    const char* sa = smem + buf * STAGE;
    const char* sb = sa + A_BYTES;
#pragma unroll
    for (int ks = 0; ks < 2; ++ks) {
      bf16x8 af[TM], bfr[TN];
#pragma unroll
      for (int i = 0; i < TM; ++i) af[i] = *(const bf16x8*)(sa + arow0 + i * 2048 + (((4 * ks + fq) ^ arx) * 16));
      const int kb = 4 * ks + fq;
      const int rsw = 4 * (kb & 1);
#pragma unroll
      for (int j = 0; j < TN; ++j) {
        const int nb = wc * TN + j;
        const char* sub = sb + (kb * NSUB + nb) * 256 + tp * 8;
        const s16x4 lo = __builtin_amdgcn_ds_read_tr16_b64_v4i16((LAS s16x4*)(sub + ((tq) ^ rsw) * 32));
        const s16x4 hi = __builtin_amdgcn_ds_read_tr16_b64_v4i16((LAS s16x4*)(sub + ((4 + tq) ^ rsw) * 32));
        bfr[j] = (bf16x8){lo[0], lo[1], lo[2], lo[3], hi[0], hi[1], hi[2], hi[3]};
      }
#pragma unroll
      for (int i = 0; i < TM; ++i)
#pragma unroll
        for (int j = 0; j < TN; ++j) acc[i][j] = __builtin_amdgcn_mfma_f32_16x16x32_bf16(bfr[j], af[i], acc[i][j], 0, 0, 0);
    }
    if (++buf == NSTAGE) buf = 0;
  }
  asm volatile("s_waitcnt lgkmcnt(0)" ::: "memory");
  __builtin_amdgcn_s_barrier();
  asm volatile("" ::: "memory");
  constexpr int WROWS = BM / WM, WCOLS = BN / WN, LDW = WCOLS + 8;
  u16* ew = (u16*)smem + w * WROWS * LDW;
#pragma unroll
  for (int j = 0; j < TN; ++j) {
    float bv[4] = {0.f, 0.f, 0.f, 0.f};
    if (EPI) {
      const ushort4 bb = *(const ushort4*)(bias + wc * WCOLS + j * 16 + fq * 4);
      bv[0] = bf2f(bb.x); bv[1] = bf2f(bb.y); bv[2] = bf2f(bb.z); bv[3] = bf2f(bb.w);
    }
#pragma unroll
    for (int i = 0; i < TM; ++i) {
      float v[4];
#pragma unroll
      for (int r = 0; r < 4; ++r) v[r] = acc[i][j][r];
      if (EPI) {
        const u32 a01 = cvtpk(v[0], v[1]), a23 = cvtpk(v[2], v[3]);
        v[0] = lo2f(a01) + bv[0]; v[1] = hi2f(a01) + bv[1]; v[2] = lo2f(a23) + bv[2]; v[3] = hi2f(a23) + bv[3];
        if (EPI == 2) {
          const u32 b01 = cvtpk(v[0], v[1]), b23 = cvtpk(v[2], v[3]);
          v[0] = gelu_tanh(lo2f(b01)); v[1] = gelu_tanh(hi2f(b01)); v[2] = gelu_tanh(lo2f(b23)); v[3] = gelu_tanh(hi2f(b23));
        }
      }
      *(uint2*)(&ew[(i * 16 + fr) * LDW + j * 16 + fq * 4]) = make_uint2(cvtpk(v[0], v[1]), cvtpk(v[2], v[3]));
    }
  }
  asm volatile("s_waitcnt lgkmcnt(0)" ::: "memory");
  __builtin_amdgcn_wave_barrier();
  constexpr int CPR = WCOLS / 8;
#pragma unroll 4
  for (int e = lane; e < WROWS * CPR; e += 64) {
    const int r = e / CPR, c = e % CPR;
    u32x4 v = *(const u32x4*)(&ew[r * LDW + c * 8]);
    const size_t eo = (size_t)(wr * WROWS + r) * ldc + wc * WCOLS + c * 8;
    u16* dst = C + eo;
    if (rx) {
      const u32x4 xv = *(const u32x4*)(rx + eo), bv = *(const u32x4*)(rb + eo);
#pragma unroll
      for (int q = 0; q < 4; ++q) {
        const u32 s1 = cvtpk(lo2f(xv[q]) + lo2f(v[q]), hi2f(xv[q]) + hi2f(v[q]));
        v[q] = cvtpk(lo2f(s1) + lo2f(bv[q]), hi2f(s1) + hi2f(bv[q]));
      }
    }
    if (WT) st16_sc1(dst, v); else *(u32x4*)dst = v;
  }
  asm volatile("s_waitcnt lgkmcnt(0)" ::: "memory");
  __builtin_amdgcn_s_barrier();
  asm volatile("" ::: "memory");
}

#define WO_BM 128
#define WO_BN 64
#define WO_WM 2
#define WO_WN 2
#define WO_NS 4
__global__ __launch_bounds__(WO_WM * WO_WN * 64) void gemm_wo_kernel(const u16* __restrict__ Ox, const u16* __restrict__ Wo, const u16* __restrict__ x,
                                                       const u16* __restrict__ Bm, u16* __restrict__ out) {
  extern __shared__ __attribute__((aligned(16))) char smem[];
  constexpr int NMT = S_ / WO_BM;
  const int xq = blockIdx.x & 7, oq = blockIdx.x >> 3;
  const int mt = oq % NMT, nt = xq + 8 * (oq / NMT);
  const size_t co = (size_t)mt * WO_BM * DM + nt * WO_BN;
  gemm_tile<WO_BM, WO_BN, WO_WM, WO_WN, WO_NS>(smem, Ox + (size_t)mt * WO_BM * HD, HD, Wo + nt * WO_BN, DM, HD, out + co, DM, nullptr, 0, false, x + co, Bm + co);
}

__device__ __forceinline__ void attn_item(const u16* Q, const u16* Kx, const u16* Vx, const u16* KM, const u16* VM, u16* O,
                                          u16* vw, int s, int h) {
  constexpr int LVR = 136;
  const int lane = threadIdx.x & 63;
  const int fr = lane & 15, fq = lane >> 4;
  const size_t xoff = (size_t)(s * SEG) * HD + h * DH, moff = (size_t)(s * MM) * HD + h * DH;
#pragma unroll
  for (int i = 0; i < 12; ++i) {
    const int e = lane + i * 64, r = e >> 4, c = (e & 15) * 8;
    u32x4 v = {0u, 0u, 0u, 0u};
    if (r < 32) v = *(const u32x4*)(Vx + xoff + (size_t)r * HD + c);
    else if (r < 40) v = *(const u32x4*)(VM + moff + (size_t)(r - 32) * HD + c);
    *(u32x4*)(&vw[r * LVR + c]) = v;
  }
  bf16x8 qf[2][4], kf[3][4];
#pragma unroll
  for (int ks = 0; ks < 4; ++ks) {
#pragma unroll
    for (int n = 0; n < 2; ++n) {
      qf[n][ks] = *(const bf16x8*)(Q + xoff + (size_t)(16 * n + fr) * HD + 32 * ks + 8 * fq);
      kf[n][ks] = *(const bf16x8*)(Kx + xoff + (size_t)(16 * n + fr) * HD + 32 * ks + 8 * fq);
    }
    kf[2][ks] = *(const bf16x8*)(KM + moff + (size_t)(fr & 7) * HD + 32 * ks + 8 * fq);
  }
  asm volatile("s_waitcnt lgkmcnt(0)" ::: "memory");
  __builtin_amdgcn_wave_barrier();
  bf16x8 vf[8][2];
  const int tq = fr >> 2, tp = fr & 3;
#pragma unroll
  for (int c = 0; c < 8; ++c) {
    const s16x4 a0 = __builtin_amdgcn_ds_read_tr16_b64_v4i16((LAS s16x4*)(&vw[(4 * fq + tq) * LVR + 16 * c + 4 * tp]));
    const s16x4 a1 = __builtin_amdgcn_ds_read_tr16_b64_v4i16((LAS s16x4*)(&vw[(16 + 4 * fq + tq) * LVR + 16 * c + 4 * tp]));
    const s16x4 a2 = __builtin_amdgcn_ds_read_tr16_b64_v4i16((LAS s16x4*)(&vw[(32 + 4 * fq + tq) * LVR + 16 * c + 4 * tp]));
    vf[c][0] = (bf16x8){a0[0], a0[1], a0[2], a0[3], a1[0], a1[1], a1[2], a1[3]};
    vf[c][1] = (bf16x8){a2[0], a2[1], a2[2], a2[3], 0, 0, 0, 0};
  }
  const float scale = 0.08838834764831845f;
#pragma unroll
  for (int qt = 0; qt < 2; ++qt) {
    f32x4 sc[3] = {{0.f, 0.f, 0.f, 0.f}, {0.f, 0.f, 0.f, 0.f}, {0.f, 0.f, 0.f, 0.f}};
#pragma unroll
    for (int ks = 0; ks < 4; ++ks)
#pragma unroll
      for (int n = 0; n < 3; ++n) sc[n] = __builtin_amdgcn_mfma_f32_16x16x32_bf16(kf[n][ks], qf[qt][ks], sc[n], 0, 0, 0);
    const int qi = 16 * qt + fr;
    float xs[3][4];
#pragma unroll
    for (int n = 0; n < 3; ++n)
#pragma unroll
      for (int r = 0; r < 4; r += 2) {
        const u32 a = cvtpk(sc[n][r], sc[n][r + 1]);
        const u32 c2 = cvtpk(lo2f(a) * scale, hi2f(a) * scale);
        xs[n][r] = lo2f(c2); xs[n][r + 1] = hi2f(c2);
      }
    float mx = -3.0e38f;
#pragma unroll
    for (int n = 0; n < 3; ++n)
#pragma unroll
      for (int r = 0; r < 4; ++r) {
        const bool live = (n < 2) ? (16 * n + 4 * fq + r <= qi) : (fq < 2);
        if (!live) xs[n][r] = -3.0e38f;
        mx = fmaxf(mx, xs[n][r]);
      }
    mx = xrow_max(mx);
    float sm = 0.f;
#pragma unroll
    for (int n = 0; n < 3; ++n)
#pragma unroll
      for (int r = 0; r < 4; ++r) { xs[n][r] = __expf(xs[n][r] - mx); sm += xs[n][r]; }
    sm = xrow_sum(sm);
    const float inv = 1.0f / sm;
    u32x4 pw0, pw1;
    pw0[0] = cvtpk(xs[0][0] * inv, xs[0][1] * inv); pw0[1] = cvtpk(xs[0][2] * inv, xs[0][3] * inv);
    pw0[2] = cvtpk(xs[1][0] * inv, xs[1][1] * inv); pw0[3] = cvtpk(xs[1][2] * inv, xs[1][3] * inv);
    pw1[0] = cvtpk(xs[2][0] * inv, xs[2][1] * inv); pw1[1] = cvtpk(xs[2][2] * inv, xs[2][3] * inv);
    pw1[2] = 0u; pw1[3] = 0u;
    const bf16x8 pf0 = __builtin_bit_cast(bf16x8, pw0), pf1 = __builtin_bit_cast(bf16x8, pw1);
    u16* orow = O + xoff + (size_t)qi * HD + 4 * fq;
#pragma unroll
    for (int c = 0; c < 8; ++c) {
      f32x4 oc = {0.f, 0.f, 0.f, 0.f};
      oc = __builtin_amdgcn_mfma_f32_16x16x32_bf16(vf[c][0], pf0, oc, 0, 0, 0);
      oc = __builtin_amdgcn_mfma_f32_16x16x32_bf16(vf[c][1], pf1, oc, 0, 0, 0);
      *(uint2*)(orow + 16 * c) = make_uint2(cvtpk(oc[0], oc[1]), cvtpk(oc[2], oc[3]));
    }
  }
}

struct FusedP {
  const u16* xn; const u16* mem0; const u16* Wq; const u16* Wk; const u16* Wv; const u16* Wo;
  const u16* w1; const u16* b1; const u16* w2; const u16* b2;
  u16* Qx; u16* Kx; u16* Vx; u16* Hb; u16* Bm; u16* KM; u16* VM; u16* Ox;
  u64* memg; u64* qkvg; u64* partg; u32* kvflag; u32* ctl;
};
constexpr int NG_MEM = 4096;
constexpr int NG_QKV = 384;
constexpr u32 SPIN_CAP = 2000000u;
#define NREP_ 8
constexpr int NREP = NREP_;
#define H1W 4
#define H2W 4
#define H2W0 4
#define H3W 2
#define H1_DELAY 8

__device__ __forceinline__ void st_gran(u64* g, u32 tag, u32 val) {
  __hip_atomic_store((gu64*)g, ((u64)tag << 32) | (u64)val, RLX_AGENT);
}
__device__ __forceinline__ void st_gran2(u64* g, u32 tag, u32 v0, u32 v1) {
  const u32x4 d = {v0, tag, v1, tag};
  asm volatile("global_store_dwordx4 %0, %1, off sc1\n\ts_nop 1" :: "v"((gu64*)g), "v"(d) : "memory");
}
__device__ __forceinline__ bool spin_fail(u32& spins, u32* tmo) {
  ++spins;
  if (spins > SPIN_CAP) { __hip_atomic_store((gu32*)tmo, 1u, RLX_AGENT); return true; }
  if ((spins & 1023u) == 0u) { if (__builtin_amdgcn_readfirstlane(__hip_atomic_load((gu32*)tmo, RLX_AGENT))) return true; }
  return false;
}
template <int N>
__device__ __forceinline__ bool sweep2(__amdgpu_buffer_rsrc_t rs, u32 byte_off, u32 byte_stride, u32 tag, u32 (&v)[2 * N], u32* tmo) {
  for (u32 spins = 0;;) {
    bool ok = true;
#pragma unroll
    for (int k = 0; k < N; ++k) {
      const u32x4 x = __builtin_bit_cast(u32x4, __builtin_amdgcn_raw_buffer_load_b128(rs, byte_off + k * byte_stride, 0, 16));
      v[2 * k] = x[0]; v[2 * k + 1] = x[2]; ok &= (x[1] == tag) & (x[3] == tag);
    }
    if (__all(ok)) return true;
    if (spin_fail(spins, tmo)) return false;
  }
}
__device__ __forceinline__ bool wait_flag(const u32* f, u32* tmo) {
  for (u32 spins = 0;;) {
    if (__builtin_amdgcn_readfirstlane(__hip_atomic_load((gu32*)f, RLX_AGENT))) return true;
    if (spin_fail(spins, tmo)) return false;
    __builtin_amdgcn_s_sleep(8);
  }
}

#define STAMP(i) do { } while (0)
#define STAMP5 do { } while (0)

constexpr int C_LM = 1032, C_LQ = 136, C_LV = 72;
constexpr int C_OFF_WO = 0;
constexpr int C_OFF_MEM = C_OFF_WO + 65536;
constexpr int C_OFF_RED = C_OFF_MEM + 8 * C_LM * 2;
constexpr int C_OFF_Q = C_OFF_RED + 8 * 8 * 48 * 4;
constexpr int C_OFF_K = C_OFF_Q + 8 * C_LQ * 2;
constexpr int C_OFF_V = C_OFF_K + 48 * C_LQ * 2;
constexpr int C_LDS = C_OFF_V + 128 * C_LV * 2;
constexpr int W_LDS_A = gemm_lds(256, 128, 4, 2, 3), W_LDS_B = gemm_lds(128, 128, 4, 2, 4);
constexpr int FUSED_LDS = (C_LDS > W_LDS_A ? (C_LDS > W_LDS_B ? C_LDS : W_LDS_B) : (W_LDS_A > W_LDS_B ? W_LDS_A : W_LDS_B));
__device__ __forceinline__ float ror4_sum(float v) {
  return v + __builtin_bit_cast(float, __builtin_amdgcn_update_dpp(0, __builtin_bit_cast(int, v), 0x124, 0xf, 0xf, false));
}

__device__ __forceinline__ void chain_role(const FusedP& p, char* smem, int cb, int h, int j) {
  constexpr int LM = C_LM, LQ = C_LQ, LV = C_LV;
  u16* memS = (u16*)(smem + C_OFF_MEM);
  float* red = (float*)(smem + C_OFF_RED);
  u16* qS = (u16*)(smem + C_OFF_Q);
  u16* kS = (u16*)(smem + C_OFF_K);
  u16* vT = (u16*)(smem + C_OFF_V);
  const int t = threadIdx.x, lane = t & 63, w = __builtin_amdgcn_readfirstlane(t >> 6);
  const int wk = w & 3, wc = w >> 2;
  bf16x8* woS = (bf16x8*)(smem + C_OFF_WO) + (w & 3) * 16 * 64 + lane;
  const int R = h * 4 + j;
  const int fr = lane & 15, fq = lane >> 4;
  u32* tmo = p.ctl;
  const __amdgpu_buffer_rsrc_t rs_mem = __builtin_amdgcn_make_buffer_rsrc((void*)p.memg, 0, 2 * NREP * NG_MEM * 8, 0x00020000);
  const __amdgpu_buffer_rsrc_t rs_qkv = __builtin_amdgcn_make_buffer_rsrc((void*)p.qkvg, 0, 2 * NH * 4 * NG_QKV * 8, 0x00020000);
  const __amdgpu_buffer_rsrc_t rs_part = __builtin_amdgcn_make_buffer_rsrc((void*)p.partg, 0, 2 * 128 * NH * 64 * 8, 0x00020000);
  const __amdgpu_buffer_rsrc_t rs_k = __builtin_amdgcn_make_buffer_rsrc((void*)p.Kx, 0, S_ * HD * 2, 0x00020000);
  const __amdgpu_buffer_rsrc_t rs_v = __builtin_amdgcn_make_buffer_rsrc((void*)p.Vx, 0, S_ * HD * 2, 0x00020000);

  bf16x8 wf[3][8];
  {
    const u16* Ws[3] = {p.Wq, p.Wk, p.Wv};
#pragma unroll
    for (int s = 0; s < 8; ++s) {
#pragma unroll
      for (int m = 0; m < 3; ++m) {
        const u16* src = Ws[m] + (size_t)(256 * wk + 32 * s + 8 * fq) * HD + h * DH + 32 * j + 16 * wc + fr;
#pragma unroll
        for (int i = 0; i < 8; ++i) wf[m][s][i] = (short)src[(size_t)i * HD];
      }
      asm volatile("" : "+v"(wf[0][s]), "+v"(wf[1][s]), "+v"(wf[2][s]) :: "memory");
    }
  }
#pragma unroll
  for (int c2 = 0; c2 < 2; ++c2)
#pragma unroll
    for (int s = 0; s < 4; ++s) {
      const int c = 2 * (w >> 2) + c2;
      bf16x8 f;
#pragma unroll
      for (int i = 0; i < 8; ++i) {
        const int d = 32 * s + ((i < 4) ? (4 * fq + i) : (16 + 4 * fq + i - 4));
        f[i] = (short)p.Wo[(size_t)(h * DH + d) * DM + 256 * j + 64 * (w & 3) + 16 * c + fr];
      }
      woS[(c * 4 + s) * 64] = f;
      asm volatile("" ::: "memory");
    }
  for (int e = t; e < 128 * LV; e += 512) vT[e] = 0;
  for (int e = t; e < 48 * LQ; e += 512) kS[e] = 0;
  u32x4 kpre, vpre;
  auto load_kv = [&](int seg) -> bool {
    if ((seg & 7) == 0) {
      if (!wait_flag(p.kvflag + ((seg >> 3) * 64 + h) * 16, tmo)) return false;
      if (!wait_flag(p.kvflag + ((seg >> 3) * 64 + 32 + h) * 16, tmo)) return false;
    }
    const int r = t >> 4, c = (t & 15) * 8;
    const u32 off = (u32)(((seg * SEG + r) * HD + h * DH + c) * 2);
    kpre = __builtin_bit_cast(u32x4, __builtin_amdgcn_raw_buffer_load_b128(rs_k, off, 0, 16));
    vpre = __builtin_bit_cast(u32x4, __builtin_amdgcn_raw_buffer_load_b128(rs_v, off, 0, 16));
    return true;
  };
  if (!load_kv(0)) return;
  __syncthreads();

  for (int s = 0; s < NSEG; ++s) {
    const int par = s & 1;
    const u32 epoch = (u32)s + 1u;
    STAMP(0);
    if (s == 0) {
#pragma unroll
      for (int i = 0; i < 2; ++i) {
        const int e = t + i * 512, r = e >> 7, c = (e & 127) * 8;
        *(u32x4*)(&memS[r * LM + c]) = *(const u32x4*)(p.mem0 + r * DM + c);
      }
    } else {
      if (w < H1W) {
        constexpr int NL = 32 / H1W;
        u32 v[2 * NL];
        const u32 gbase = (u32)(((par ^ 1) * NREP + (cb & (NREP - 1))) * NG_MEM + w * (4096 / H1W));
        __builtin_amdgcn_s_sleep(H1_DELAY);
        if (!sweep2<NL>(rs_mem, (gbase + lane * 2) * 8, 128 * 8, (u32)s, v, tmo)) return;
#pragma unroll
        for (int k = 0; k < NL; ++k) {
          const int u = lane + k * 64;
          *(uint2*)(&memS[((u >> 1) & 7) * LM + (w * (128 / H1W) + (u >> 4)) * 8 + (u & 1) * 4]) = make_uint2(v[2 * k], v[2 * k + 1]);
        }
      }
    }
    {
      const int r = t >> 4, c = (t & 15) * 8;
      *(u32x4*)(&kS[r * LQ + c]) = kpre;
      const u16* vp = (const u16*)&vpre;
      const int pos = (r < 16) ? (8 * (r >> 2) + (r & 3)) : (8 * ((r - 16) >> 2) + 4 + (r & 3));
#pragma unroll
      for (int q = 0; q < 8; ++q) vT[(c + q) * LV + pos] = vp[q];
    }
    __syncthreads();
    STAMP(1);
    {
      f32x4 acc[3] = {{0.f, 0.f, 0.f, 0.f}, {0.f, 0.f, 0.f, 0.f}, {0.f, 0.f, 0.f, 0.f}};
      bf16x8 a[8];
#pragma unroll
      for (int ks = 0; ks < 8; ++ks) a[ks] = *(const bf16x8*)(&memS[(lane & 7) * LM + 256 * wk + 32 * ks + 8 * fq]);
      __builtin_amdgcn_sched_barrier(0);
#pragma unroll
      for (int ks = 0; ks < 8; ++ks)
#pragma unroll
        for (int m = 0; m < 3; ++m) acc[m] = __builtin_amdgcn_mfma_f32_16x16x32_bf16(a[ks], wf[m][ks], acc[m], 0, 0, 0);
      if (fq < 2) {
#pragma unroll
        for (int m = 0; m < 3; ++m)
#pragma unroll
          for (int r = 0; r < 4; ++r) red[(w * 8 + fq * 4 + r) * 48 + m * 16 + fr] = acc[m][r];
      }
    }
    __syncthreads();
    if (t < NG_QKV / 2) {
      const int r = t / 24, cq = t % 24;
      const int m = cq >> 3, c = (cq & 7) * 4, g = c >> 4;
      float4 v4 = make_float4(0.f, 0.f, 0.f, 0.f);
#pragma unroll
      for (int ww = 0; ww < 4; ++ww) { const float4 q4 = *(const float4*)(&red[((g * 4 + ww) * 8 + r) * 48 + m * 16 + (c & 15)]); v4.x += q4.x; v4.y += q4.y; v4.z += q4.z; v4.w += q4.w; }
      const u32 pk0 = cvtpk(v4.x, v4.y), pk1 = cvtpk(v4.z, v4.w);
      if (s < NSEG - 1) st_gran2(p.qkvg + ((size_t)(par * NH + h) * 4 + j) * NG_QKV + 2 * t, epoch, pk0, pk1);
      if (m != 0) __hip_atomic_store((gu64*)((m == 1 ? p.KM : p.VM) + (size_t)(s * MM + r) * HD + h * DH + 32 * j + c), (u64)pk0 | ((u64)pk1 << 32), RLX_AGENT);
    }
    STAMP(2);
    if (s == NSEG - 1) {
      asm volatile("s_waitcnt vmcnt(0)" ::: "memory");
      __syncthreads();
      if (t == 0) __hip_atomic_fetch_add((gu32*)(p.ctl + 128 + 16 * 7), 1u, RLX_AGENT);
      break;
    }
    bf16x8 kx[2][4], vx[8];
#pragma unroll
    for (int ks = 0; ks < 4; ++ks)
#pragma unroll
      for (int n = 0; n < 2; ++n) kx[n][ks] = *(const bf16x8*)(&kS[(n * 16 + fr) * LQ + 32 * ks + 8 * fq]);
    __builtin_amdgcn_sched_barrier(0);
    if (w >= H2W0 && w < H2W0 + H2W) {
      constexpr int NL = 12 / H2W;
      u32 v[2 * NL];
      const int wv = w - H2W0;
      const u32 gbase = (u32)((par * NH + h) * 4 * NG_QKV + wv * NL * 128);
      if (!sweep2<NL>(rs_qkv, (gbase + lane * 2) * 8, 128 * 8, epoch, v, tmo)) return;
#pragma unroll
      for (int k = 0; k < NL; ++k) {
        const int ug = wv * NL * 64 + k * 64 + lane, pj = ug / 192, u = ug % 192;
        const int r = u / 24, cq = u % 24, m = cq >> 3, c = 32 * pj + (cq & 7) * 4;
        if (m == 0) *(uint2*)(&qS[r * LQ + c]) = make_uint2(v[2 * k], v[2 * k + 1]);
        else if (m == 1) *(uint2*)(&kS[(32 + r) * LQ + c]) = make_uint2(v[2 * k], v[2 * k + 1]);
        else {
          const int pos = 32 + 8 * (r >> 2) + (r & 3);
          vT[c * LV + pos] = (u16)(v[2 * k] & 0xffffu); vT[(c + 1) * LV + pos] = (u16)(v[2 * k] >> 16);
          vT[(c + 2) * LV + pos] = (u16)(v[2 * k + 1] & 0xffffu); vT[(c + 3) * LV + pos] = (u16)(v[2 * k + 1] >> 16);
        }
      }
    }
    __syncthreads();
    STAMP(3);
    if (w < 4) {
      f32x4 sc[3] = {{0.f, 0.f, 0.f, 0.f}, {0.f, 0.f, 0.f, 0.f}, {0.f, 0.f, 0.f, 0.f}};
      bf16x8 qf[4], km[4];
      uint2 vm[8];
#pragma unroll
      for (int ks = 0; ks < 4; ++ks) {
        qf[ks] = *(const bf16x8*)(&qS[(lane & 7) * LQ + 32 * ks + 8 * fq]);
        km[ks] = *(const bf16x8*)(&kS[(32 + fr) * LQ + 32 * ks + 8 * fq]);
      }
#pragma unroll
      for (int c = 0; c < 8; ++c) vm[c] = *(const uint2*)(&vT[(16 * c + fr) * LV + 32 + 8 * fq]);
      __builtin_amdgcn_sched_barrier(0);
#pragma unroll
      for (int ks = 0; ks < 4; ++ks) {
        sc[0] = __builtin_amdgcn_mfma_f32_16x16x32_bf16(kx[0][ks], qf[ks], sc[0], 0, 0, 0);
        sc[1] = __builtin_amdgcn_mfma_f32_16x16x32_bf16(kx[1][ks], qf[ks], sc[1], 0, 0, 0);
        sc[2] = __builtin_amdgcn_mfma_f32_16x16x32_bf16(km[ks], qf[ks], sc[2], 0, 0, 0);
      }
      __builtin_amdgcn_sched_barrier(0);
#pragma unroll
      for (int c = 0; c < 8; ++c) vx[c] = *(const bf16x8*)(&vT[(16 * c + fr) * LV + 8 * fq]);

      __builtin_amdgcn_sched_barrier(0);
      const float scale = 0.08838834764831845f;
      const bool live2 = fq < 2;
      float xs[3][4];
#pragma unroll
      for (int n = 0; n < 3; ++n)
#pragma unroll
        for (int r = 0; r < 4; r += 2) {
          const u32 a = cvtpk(sc[n][r], sc[n][r + 1]);
          const u32 c2 = cvtpk(lo2f(a) * scale, hi2f(a) * scale);
          xs[n][r] = lo2f(c2); xs[n][r + 1] = hi2f(c2);
        }
      if (!live2) { xs[2][0] = xs[2][1] = xs[2][2] = xs[2][3] = -3.0e38f; }
      float mx = fmaxf(fmaxf(fmaxf(xs[0][0], xs[0][1]), fmaxf(xs[0][2], xs[0][3])), fmaxf(fmaxf(xs[1][0], xs[1][1]), fmaxf(xs[1][2], xs[1][3])));
      mx = fmaxf(mx, fmaxf(fmaxf(xs[2][0], xs[2][1]), fmaxf(xs[2][2], xs[2][3])));
      mx = xrow_max(mx);
      float sm = 0.f;
#pragma unroll
      for (int n = 0; n < 3; ++n)
#pragma unroll
        for (int r = 0; r < 4; ++r) { xs[n][r] = __expf(xs[n][r] - mx); sm += xs[n][r]; }
      sm = xrow_sum(sm);
      const float inv = 1.0f / sm;
      u32x4 pw0, pw1;
      pw0[0] = cvtpk(xs[0][0] * inv, xs[0][1] * inv); pw0[1] = cvtpk(xs[0][2] * inv, xs[0][3] * inv);
      pw0[2] = cvtpk(xs[1][0] * inv, xs[1][1] * inv); pw0[3] = cvtpk(xs[1][2] * inv, xs[1][3] * inv);
      pw1[0] = cvtpk(xs[2][0] * inv, xs[2][1] * inv); pw1[1] = cvtpk(xs[2][2] * inv, xs[2][3] * inv);
      pw1[2] = 0u; pw1[3] = 0u;
      const bf16x8 pf0 = __builtin_bit_cast(bf16x8, pw0), pf1 = __builtin_bit_cast(bf16x8, pw1);
      u32x4 ow[4];
#pragma unroll
      for (int c = 0; c < 8; ++c) {
        const u32x4 vmw = {vm[c].x, vm[c].y, 0u, 0u};
        f32x4 oc = {0.f, 0.f, 0.f, 0.f};
        oc = __builtin_amdgcn_mfma_f32_16x16x32_bf16(vx[c], pf0, oc, 0, 0, 0);
        oc = __builtin_amdgcn_mfma_f32_16x16x32_bf16(__builtin_bit_cast(bf16x8, vmw), pf1, oc, 0, 0, 0);
        ow[c >> 1][(c & 1) * 2] = cvtpk(oc[0], oc[1]); ow[c >> 1][(c & 1) * 2 + 1] = cvtpk(oc[2], oc[3]);
      }
      f32x4 pc[4];
#pragma unroll
      for (int c = 0; c < 4; ++c) pc[c] = (f32x4){0.f, 0.f, 0.f, 0.f};
#pragma unroll
      for (int ks = 0; ks < 4; ++ks)
#pragma unroll
        for (int c = 0; c < 4; ++c) pc[c] = __builtin_amdgcn_mfma_f32_16x16x32_bf16(woS[(c * 4 + ks) * 64], __builtin_bit_cast(bf16x8, ow[ks]), pc[c], 0, 0, 0);
      if (fr < 8) {
#pragma unroll
        for (int c = 0; c < 4; ++c) {
          u64* dst = p.partg + ((size_t)(par * 128 + 32 * j + 8 * w + 2 * c + (fq >> 1)) * NH + h) * 64 + fr * 8 + 4 * (fq & 1);
          st_gran2(dst, epoch, __float_as_uint(pc[c][0]), __float_as_uint(pc[c][1]));
          st_gran2(dst + 2, epoch, __float_as_uint(pc[c][2]), __float_as_uint(pc[c][3]));
        }
      }
    }
    STAMP(4);
    __syncthreads();
    if (w >= 4 && w < 4 + H3W) {
      constexpr int UPW = 32 / H3W;
      constexpr int HG = 64 / UPW, NL = 32 / HG;
      u32 v[2 * NL];
      const int w4 = w - 4;
      const int hg = lane / UPW, u = lane % UPW;
      const u32 gbase = (u32)(((par * 128 + R) * NH + NL * hg) * 64 + (UPW * w4 + u) * 2);
      if (!sweep2<NL>(rs_part, gbase * 8, 64 * 8, epoch, v, tmo)) return;
      float f0 = 0.f, f1 = 0.f;
#pragma unroll
      for (int k = 0; k < NL; ++k) { f0 += __uint_as_float(v[2 * k]); f1 += __uint_as_float(v[2 * k + 1]); }
      if (UPW == 8) { f0 = ror8_sum(f0); f1 = ror8_sum(f1); }
      if (UPW <= 16) { f0 = xrow_sum(f0); f1 = xrow_sum(f1); }
      else {
        auto c0 = __builtin_amdgcn_permlane32_swap(__float_as_uint(f0), __float_as_uint(f0), false, false);
        auto c1 = __builtin_amdgcn_permlane32_swap(__float_as_uint(f1), __float_as_uint(f1), false, false);
        f0 = __uint_as_float(c0[0]) + __uint_as_float(c0[1]); f1 = __uint_as_float(c1[0]) + __uint_as_float(c1[1]);
      }
      const u32 pk = cvtpk(f0, f1);
#pragma unroll
      for (int q = 0; q < NREP / HG; ++q) st_gran(p.memg + (size_t)(par * NREP + q * HG + hg) * NG_MEM + R * 32 + UPW * w4 + u, epoch, pk);
      STAMP5;
    }
    if (!load_kv(s + 1)) return;
    asm volatile("s_waitcnt vmcnt(0)" ::: "memory");
    __syncthreads();
    if ((s & 7) == 7 && t == 0) __hip_atomic_fetch_add((gu32*)(p.ctl + 128 + 16 * (s >> 3)), 1u, RLX_AGENT);
  }
}

__device__ __forceinline__ void worker_role(const FusedP& p, char* smem, int gw) {
  const int t = threadIdx.x;
  u32* tmo = p.ctl;
  for (int i = 0; i < 4; ++i) {
    const int id = gw + 128 * i, rb = id >> 6, n = id & 63;
    const u16* B = (n < 32 ? p.Wk : p.Wv) + (n & 31) * 128;
    u16* C = (n < 32 ? p.Kx : p.Vx) + (size_t)rb * 256 * HD + (n & 31) * 128;
    gemm_tile<256, 128, 4, 2, 3>(smem, p.xn + (size_t)rb * 256 * DM, DM, B, HD, DM, C, HD, nullptr, 0, true);
    asm volatile("s_waitcnt vmcnt(0)" ::: "memory");
    __syncthreads();
    if (t == 0) __hip_atomic_store((gu32*)(p.kvflag + (rb * 64 + n) * 16), 1u, RLX_AGENT);
  }
  for (int id = gw; id < 480; id += 128) {
    if (id < 224) {
      const int rb = id / 28, n = id % 28;
      gemm_tile<256, 128, 4, 2, 3>(smem, p.xn + (size_t)rb * 256 * DM, DM, p.w1 + n * 128, FF, DM, p.Hb + (size_t)rb * 256 * FF + n * 128, FF, p.b1 + n * 128, 2, false);
    } else {
      const int q = id - 224, rb = q >> 5, n = q & 31;
      gemm_tile<256, 128, 4, 2, 3>(smem, p.xn + (size_t)rb * 256 * DM, DM, p.Wq + n * 128, HD, DM, p.Qx + (size_t)rb * 256 * HD + n * 128, HD, nullptr, 0, false);
    }
  }
  asm volatile("s_waitcnt vmcnt(0)" ::: "memory");
  __syncthreads();
  if (t == 0) {
    __builtin_amdgcn_fence(__ATOMIC_RELEASE, "agent");
    asm volatile("s_waitcnt vmcnt(0)" ::: "memory");
    __hip_atomic_fetch_add((gu32*)(p.ctl + 64), 1u, RLX_AGENT);
    for (u32 spins = 0;;) {
      if (__hip_atomic_load((gu32*)(p.ctl + 64), RLX_AGENT) >= 128u) break;
      if (++spins > SPIN_CAP) { __hip_atomic_store((gu32*)tmo, 1u, RLX_AGENT); break; }
      __builtin_amdgcn_s_sleep(16);
    }
    __builtin_amdgcn_fence(__ATOMIC_ACQUIRE, "agent");
    asm volatile("s_waitcnt vmcnt(0)" ::: "memory");
  }
  __syncthreads();
  {
    const int mt = gw >> 3, nt = gw & 7;
    gemm_tile<128, 128, 4, 2, 4>(smem, p.Hb + (size_t)mt * 128 * FF, FF, p.w2 + nt * 128, DM, FF, p.Bm + (size_t)mt * 128 * DM + nt * 128, DM, p.b2 + nt * 128, 1, false);
  }
  const int w = __builtin_amdgcn_readfirstlane(t >> 6);
  for (int rb = 0; rb < 8; ++rb) {
    if (t == 0) {
      for (u32 spins = 0;;) {
        if (__hip_atomic_load((gu32*)(p.ctl + 128 + 16 * rb), RLX_AGENT) >= 128u) break;
        if (++spins > SPIN_CAP) { __hip_atomic_store((gu32*)tmo, 1u, RLX_AGENT); break; }
        if ((spins & 255u) == 0u && __hip_atomic_load((gu32*)tmo, RLX_AGENT)) break;
        __builtin_amdgcn_s_sleep(64);
      }
      __builtin_amdgcn_fence(__ATOMIC_ACQUIRE, "agent");
      asm volatile("s_waitcnt vmcnt(0)" ::: "memory");
    }
    __syncthreads();
    if (w < 2) {
      const int item = gw * 2 + w;
      attn_item(p.Qx, p.Kx, p.Vx, p.KM, p.VM, p.Ox, (u16*)smem + w * 48 * 136, rb * 8 + (item >> 5), item & 31);
    }
  }
}

__global__ __launch_bounds__(512, 2) void fused_kernel(FusedP p) {
  extern __shared__ __attribute__((aligned(16))) char smem[];
  const int b = blockIdx.x, xcd = b & 7, ib = b >> 3;
  if (ib < 16) chain_role(p, smem, xcd * 16 + ib, xcd * 4 + (ib >> 2), ib & 3);
  else worker_role(p, smem, (ib - 16) * 8 + xcd);
}

extern "C" void kernel_launch(void* const* d_in, const int* in_sizes, int n_in,
                              void* d_out, int out_size, void* d_ws, size_t ws_size,
                              hipStream_t stream) {
  const u16* x = (const u16*)d_in[0];
  const u16* mem0 = (const u16*)d_in[1];
  const u16* Wq = (const u16*)d_in[2];
  const u16* Wk = (const u16*)d_in[3];
  const u16* Wv = (const u16*)d_in[4];
  const u16* Wo = (const u16*)d_in[5];
  const u16* w1 = (const u16*)d_in[8];
  const u16* b1 = (const u16*)d_in[9];
  const u16* w2 = (const u16*)d_in[10];
  const u16* b2 = (const u16*)d_in[11];
  u16* out = (u16*)d_out;

  char* ws = (char*)d_ws;
  size_t off = 0;
  auto carve = [&](size_t bytes) { char* q = ws + off; off += (bytes + 255) & ~(size_t)255; return q; };
  u64* memg = (u64*)carve((size_t)2 * NREP * NG_MEM * 8);
  u64* qkvg = (u64*)carve((size_t)2 * NH * 4 * NG_QKV * 8);
  u64* partg = (u64*)carve((size_t)2 * 128 * NH * 64 * 8);
  u32* kvflag = (u32*)carve((size_t)512 * 64);
  u32* ctl = (u32*)carve(2048);
  const size_t sync_bytes = off;
  u16* xn = (u16*)carve((size_t)S_ * DM * 2);
  u16* Qx = (u16*)carve((size_t)S_ * HD * 2);
  u16* Kx = (u16*)carve((size_t)S_ * HD * 2);
  u16* Vx = (u16*)carve((size_t)S_ * HD * 2);
  u16* Hb = (u16*)carve((size_t)S_ * FF * 2);
  u16* Ox = (u16*)carve((size_t)S_ * HD * 2);
  u16* KM = (u16*)carve((size_t)NSEG * MM * HD * 2);
  u16* VM = (u16*)carve((size_t)NSEG * MM * HD * 2);
  u16* Bm = (u16*)carve((size_t)S_ * DM * 2);

  ln_kernel<<<S_, 256, 0, stream>>>(x, xn, (u32x4*)ws, (int)(sync_bytes / 16));
  {
    FusedP f{xn, mem0, Wq, Wk, Wv, Wo, w1, b1, w2, b2, Qx, Kx, Vx, Hb, Bm, KM, VM, Ox, memg, qkvg, partg, kvflag, ctl};
    (void)hipFuncSetAttribute((const void*)fused_kernel, hipFuncAttributeMaxDynamicSharedMemorySize, FUSED_LDS);
    fused_kernel<<<256, 512, FUSED_LDS, stream>>>(f);
  }
  {
    constexpr int LDSW = gemm_lds(WO_BM, WO_BN, WO_WM, WO_WN, WO_NS);
    (void)hipFuncSetAttribute((const void*)gemm_wo_kernel, hipFuncAttributeMaxDynamicSharedMemorySize, LDSW);
    gemm_wo_kernel<<<(S_ / WO_BM) * (DM / WO_BN), WO_WM * WO_WN * 64, LDSW, stream>>>(Ox, Wo, x, Bm, out);
  }
}
```

```cpp
#include <hip/hip_runtime.h>
#include <hip/hip_bf16.h>
#include <stdint.h>

typedef unsigned short u16;
typedef unsigned int u32;
typedef unsigned long long u64;
#define GAS __attribute__((address_space(1)))
#define LAS __attribute__((address_space(3)))
typedef GAS u64 gu64;
typedef GAS u32 gu32;
typedef __attribute__((ext_vector_type(8))) short bf16x8;
typedef __attribute__((ext_vector_type(4))) short s16x4;
typedef __attribute__((ext_vector_type(4))) float f32x4;
typedef __attribute__((ext_vector_type(4))) unsigned int u32x4;
#define RLX_AGENT __ATOMIC_RELAXED, __HIP_MEMORY_SCOPE_AGENT

constexpr int S_ = 2048, DM = 1024, NH = 32, DH = 128, HD = 4096, MM = 8, SEG = 32, NSEG = 64, FF = 3584;

__device__ __forceinline__ float bf2f(u16 v) { return __uint_as_float(((u32)v) << 16); }
__device__ __forceinline__ u16 f2bf(float f) { u32 u = __float_as_uint(f); u += 0x7fffu + ((u >> 16) & 1u); return (u16)(u >> 16); }
__device__ __forceinline__ float rbf(float f) { return bf2f(f2bf(f)); }
typedef __bf16 hbf2 __attribute__((ext_vector_type(2)));
typedef float f32x2 __attribute__((ext_vector_type(2)));
__device__ __forceinline__ u32 cvtpk(float a, float b) { f32x2 v = {a, b}; return __builtin_bit_cast(u32, __builtin_convertvector(v, hbf2)); }
__device__ __forceinline__ float lo2f(u32 v) { return __uint_as_float(v << 16); }
__device__ __forceinline__ float hi2f(u32 v) { return __uint_as_float(v & 0xffff0000u); }

__device__ __forceinline__ float xrow_max(float v) {
  auto a = __builtin_amdgcn_permlane16_swap(__float_as_uint(v), __float_as_uint(v), false, false);
  v = fmaxf(__uint_as_float(a[0]), __uint_as_float(a[1]));
  auto c = __builtin_amdgcn_permlane32_swap(__float_as_uint(v), __float_as_uint(v), false, false);
  return fmaxf(__uint_as_float(c[0]), __uint_as_float(c[1]));
}
__device__ __forceinline__ float xrow_sum(float v) {
  auto a = __builtin_amdgcn_permlane16_swap(__float_as_uint(v), __float_as_uint(v), false, false);
  v = __uint_as_float(a[0]) + __uint_as_float(a[1]);
  auto c = __builtin_amdgcn_permlane32_swap(__float_as_uint(v), __float_as_uint(v), false, false);
  return __uint_as_float(c[0]) + __uint_as_float(c[1]);
}
__device__ __forceinline__ float ror8_sum(float v) {
  return v + __builtin_bit_cast(float, __builtin_amdgcn_update_dpp(0, __builtin_bit_cast(int, v), 0x128, 0xf, 0xf, false));
}

__global__ __launch_bounds__(256) void ln_kernel(const u16* __restrict__ x, u16* __restrict__ xn, u32x4* __restrict__ zero_base, int zero_units) {
  __shared__ float red[8];
  const int row = blockIdx.x, t = threadIdx.x, lane = t & 63, w = t >> 6;
  for (int i = row * 256 + t; i < zero_units; i += S_ * 256) zero_base[i] = (u32x4){0u, 0u, 0u, 0u};
  const ushort4 v = *(const ushort4*)(x + (size_t)row * DM + t * 4);
  float f0 = bf2f(v.x), f1 = bf2f(v.y), f2 = bf2f(v.z), f3 = bf2f(v.w);
  float s = (f0 + f1) + (f2 + f3);
#pragma unroll
  for (int m = 32; m >= 1; m >>= 1) s += __shfl_xor(s, m);
  if (lane == 0) red[w] = s;
  __syncthreads();
  const float mean = ((red[0] + red[1]) + (red[2] + red[3])) * (1.0f / DM);
  float d0 = f0 - mean, d1 = f1 - mean, d2 = f2 - mean, d3 = f3 - mean;
  float q = (d0 * d0 + d1 * d1) + (d2 * d2 + d3 * d3);
#pragma unroll
  for (int m = 32; m >= 1; m >>= 1) q += __shfl_xor(q, m);
  if (lane == 0) red[4 + w] = q;
  __syncthreads();
  const float var = ((red[4] + red[5]) + (red[6] + red[7])) * (1.0f / DM);
  const float mb = rbf(mean), vb = rbf(var);
  const float den = rbf(sqrtf(rbf(vb + 1e-5f)));
  ushort4 o;
  o.x = f2bf(rbf(f0 - mb) / den); o.y = f2bf(rbf(f1 - mb) / den);
  o.z = f2bf(rbf(f2 - mb) / den); o.w = f2bf(rbf(f3 - mb) / den);
  *(ushort4*)(xn + (size_t)row * DM + t * 4) = o;
}

__device__ __forceinline__ float gelu_tanh(float x) {
  const float k0 = 0.7978845608028654f, k1 = 0.044715f;
  const float u = k0 * (x + k1 * x * x * x);
  return 0.5f * x * (1.0f + tanhf(u));
}
__device__ __forceinline__ void glds16(const void* gsrc, u32 lds_dst) {
  u32 keep;
  asm volatile("s_mov_b32 %0, m0\n\ts_mov_b32 m0, %2\n\ts_nop 0\n\tglobal_load_lds_dwordx4 %1, off\n\ts_mov_b32 m0, %0"
               : "=&s"(keep) : "v"(gsrc), "s"(lds_dst) : "memory");
}
template <int N> __device__ __forceinline__ void wait_vm() { asm volatile("s_waitcnt vmcnt(%0)" :: "n"(N) : "memory"); }
__device__ __forceinline__ void st16_sc1(void* p, u32x4 v) {
  asm volatile("global_store_dwordx4 %0, %1, off sc1\n\ts_nop 1" :: "v"(p), "v"(v) : "memory");
}
constexpr int gemm_lds(int BM, int BN, int WM, int WN, int NSTAGE) {
  const int st = NSTAGE * (BM * 64 * 2 + 64 * BN * 2), ep = WM * WN * (BM / WM) * (BN / WN + 8) * 2;
  return st > ep ? st : ep;
}
template <int BM, int BN, int WM, int WN, int NSTAGE>
__device__ __forceinline__ void gemm_tile(char* smem, const u16* A, int lda, const u16* B, int ldb, int K,
                                          u16* C, int ldc, const u16* bias, int EPI, bool WT,
                                          const u16* rx = nullptr, const u16* rb = nullptr) {
  constexpr int NW = WM * WN, NT = NW * 64, BK = 64;
  constexpr int TM = BM / WM / 16, TN = BN / WN / 16;
  constexpr int A_BYTES = BM * BK * 2, B_BYTES = BK * BN * 2, STAGE = A_BYTES + B_BYTES;
  constexpr int NSUB = BN / 16;
  const int t = threadIdx.x, lane = t & 63, w = __builtin_amdgcn_readfirstlane(t >> 6);
  const int wr = w / WN, wc = w % WN, fr = lane & 15, fq = lane >> 4;
  constexpr int AU = BM * 8 / NT, BU = BK * BN / 8 / NT, LPS = AU + BU;
  const u16* asrc[AU]; const u16* bsrc[BU];
#pragma unroll
  for (int i = 0; i < AU; ++i) {
    const int u = i * NT + t, row = u >> 3, c = (u & 7) ^ (row & 7);
    asrc[i] = A + (size_t)row * lda + c * 8;
  }
#pragma unroll
  for (int i = 0; i < BU; ++i) {
    const int u = i * NT + t, hf = u & 1, rp = (u >> 1) & 7, st = u >> 4, kb = st / NSUB, nb = st % NSUB;
    const int r = rp ^ (4 * (kb & 1));
    bsrc[i] = B + (size_t)(kb * 8 + r) * ldb + nb * 16 + hf * 8;
  }
  const u32 lds0 = (u32)(uintptr_t)(LAS char*)smem + (u32)w * 1024u;
  auto stage = [&](int kt, int buf) {
    const u32 base = lds0 + buf * STAGE;
#pragma unroll
    for (int i = 0; i < AU; ++i) glds16(asrc[i] + kt * BK, base + i * NT * 16);
#pragma unroll
    for (int i = 0; i < BU; ++i) glds16(bsrc[i] + (size_t)kt * BK * ldb, base + A_BYTES + i * NT * 16);
  };
  f32x4 acc[TM][TN];
#pragma unroll
  for (int i = 0; i < TM; ++i)
#pragma unroll
    for (int j = 0; j < TN; ++j) acc[i][j] = (f32x4){0.f, 0.f, 0.f, 0.f};
  const int arow0 = (wr * (BM / WM) + fr) * 128;
  const int arx = fr & 7;
  const int tq = (lane & 15) >> 2, tp = lane & 3;
  const int nk = K / BK;
  wait_vm<0>();
#pragma unroll
  for (int k = 0; k < NSTAGE - 1; ++k) stage(k, k);
  int buf = 0;
  for (int kt = 0; kt < nk; ++kt) {
    if (kt + NSTAGE - 1 <= nk) wait_vm<LPS * (NSTAGE - 2)>(); else wait_vm<0>();
    asm volatile("s_waitcnt lgkmcnt(0)" ::: "memory");
    __builtin_amdgcn_s_barrier();
    asm volatile("" ::: "memory");
    if (kt + NSTAGE - 1 < nk) { int nb2 = buf + NSTAGE - 1; if (nb2 >= NSTAGE) nb2 -= NSTAGE; stage(kt + NSTAGE - 1, nb2); }
    const char* sa = smem + buf * STAGE;
    const char* sb = sa + A_BYTES;
#pragma unroll
    for (int ks = 0; ks < 2; ++ks) {
      bf16x8 af[TM], bfr[TN];
#pragma unroll
      for (int i = 0; i < TM; ++i) af[i] = *(const bf16x8*)(sa + arow0 + i * 2048 + (((4 * ks + fq) ^ arx) * 16));
      const int kb = 4 * ks + fq;
      const int rsw = 4 * (kb & 1);
#pragma unroll
      for (int j = 0; j < TN; ++j) {
        const int nb = wc * TN + j;
        const char* sub = sb + (kb * NSUB + nb) * 256 + tp * 8;
        const s16x4 lo = __builtin_amdgcn_ds_read_tr16_b64_v4i16((LAS s16x4*)(sub + ((tq) ^ rsw) * 32));
        const s16x4 hi = __builtin_amdgcn_ds_read_tr16_b64_v4i16((LAS s16x4*)(sub + ((4 + tq) ^ rsw) * 32));
        bfr[j] = (bf16x8){lo[0], lo[1], lo[2], lo[3], hi[0], hi[1], hi[2], hi[3]};
      }
#pragma unroll
      for (int i = 0; i < TM; ++i)
#pragma unroll
        for (int j = 0; j < TN; ++j) acc[i][j] = __builtin_amdgcn_mfma_f32_16x16x32_bf16(bfr[j], af[i], acc[i][j], 0, 0, 0);
    }
    if (++buf == NSTAGE) buf = 0;
  }
  asm volatile("s_waitcnt lgkmcnt(0)" ::: "memory");
  __builtin_amdgcn_s_barrier();
  asm volatile("" ::: "memory");
  constexpr int WROWS = BM / WM, WCOLS = BN / WN, LDW = WCOLS + 8;
  u16* ew = (u16*)smem + w * WROWS * LDW;
#pragma unroll
  for (int j = 0; j < TN; ++j) {
    float bv[4] = {0.f, 0.f, 0.f, 0.f};
    if (EPI) {
      const ushort4 bb = *(const ushort4*)(bias + wc * WCOLS + j * 16 + fq * 4);
      bv[0] = bf2f(bb.x); bv[1] = bf2f(bb.y); bv[2] = bf2f(bb.z); bv[3] = bf2f(bb.w);
    }
#pragma unroll
    for (int i = 0; i < TM; ++i) {
      float v[4];
#pragma unroll
      for (int r = 0; r < 4; ++r) v[r] = acc[i][j][r];
      if (EPI) {
        const u32 a01 = cvtpk(v[0], v[1]), a23 = cvtpk(v[2], v[3]);
        v[0] = lo2f(a01) + bv[0]; v[1] = hi2f(a01) + bv[1]; v[2] = lo2f(a23) + bv[2]; v[3] = hi2f(a23) + bv[3];
        if (EPI == 2) {
          const u32 b01 = cvtpk(v[0], v[1]), b23 = cvtpk(v[2], v[3]);
          v[0] = gelu_tanh(lo2f(b01)); v[1] = gelu_tanh(hi2f(b01)); v[2] = gelu_tanh(lo2f(b23)); v[3] = gelu_tanh(hi2f(b23));
        }
      }
      *(uint2*)(&ew[(i * 16 + fr) * LDW + j * 16 + fq * 4]) = make_uint2(cvtpk(v[0], v[1]), cvtpk(v[2], v[3]));
    }
  }
  asm volatile("s_waitcnt lgkmcnt(0)" ::: "memory");
  __builtin_amdgcn_wave_barrier();
  constexpr int CPR = WCOLS / 8;
#pragma unroll 4
  for (int e = lane; e < WROWS * CPR; e += 64) {
    const int r = e / CPR, c = e % CPR;
    u32x4 v = *(const u32x4*)(&ew[r * LDW + c * 8]);
    const size_t eo = (size_t)(wr * WROWS + r) * ldc + wc * WCOLS + c * 8;
    u16* dst = C + eo;
    if (rx) {
      const u32x4 xv = *(const u32x4*)(rx + eo), bv = *(const u32x4*)(rb + eo);
#pragma unroll
      for (int q = 0; q < 4; ++q) {
        const u32 s1 = cvtpk(lo2f(xv[q]) + lo2f(v[q]), hi2f(xv[q]) + hi2f(v[q]));
        v[q] = cvtpk(lo2f(s1) + lo2f(bv[q]), hi2f(s1) + hi2f(bv[q]));
      }
    }
    if (WT) st16_sc1(dst, v); else *(u32x4*)dst = v;
  }
  asm volatile("s_waitcnt lgkmcnt(0)" ::: "memory");
  __builtin_amdgcn_s_barrier();
  asm volatile("" ::: "memory");
}

#define WO_BM 128
#define WO_BN 64
#define WO_WM 2
#define WO_WN 2
#define WO_NS 4
__global__ __launch_bounds__(WO_WM * WO_WN * 64) void gemm_wo_kernel(const u16* __restrict__ Ox, const u16* __restrict__ Wo, const u16* __restrict__ x,
                                                       const u16* __restrict__ Bm, u16* __restrict__ out) {
  extern __shared__ __attribute__((aligned(16))) char smem[];
  constexpr int NMT = S_ / WO_BM;
  const int xq = blockIdx.x & 7, oq = blockIdx.x >> 3;
  const int mt = oq % NMT, nt = xq + 8 * (oq / NMT);
  const size_t co = (size_t)mt * WO_BM * DM + nt * WO_BN;
  gemm_tile<WO_BM, WO_BN, WO_WM, WO_WN, WO_NS>(smem, Ox + (size_t)mt * WO_BM * HD, HD, Wo + nt * WO_BN, DM, HD, out + co, DM, nullptr, 0, false, x + co, Bm + co);
}

struct FusedP {
  const u16* xn; const u16* mem0; const u16* Wq; const u16* Wk; const u16* Wv; const u16* Wo;
  const u16* w1; const u16* b1; const u16* w2; const u16* b2;
  u16* Qx; u16* Kx; u16* Vx; u16* Hb; u16* Bm; u16* KM; u16* VM;
  u64* memg; u64* qkvg; u64* partg; u32* kvflag; u32* ctl;
};
constexpr int NG_MEM = 4096;
constexpr int NG_QKV = 384;
constexpr u32 SPIN_CAP = 2000000u;
constexpr int NREP = 8;
#define H1W 4
#define H2W 4
#define H2W0 4
#define H3W 2
#define H2SYNC 1
#define H2DELAY 6
#define H3DELAY 16
#define H1_DELAY 8

__device__ __forceinline__ void st_gran(u64* g, u32 tag, u32 val) {
  __hip_atomic_store((gu64*)g, ((u64)tag << 32) | (u64)val, RLX_AGENT);
}
__device__ __forceinline__ void st_gran2(u64* g, u32 tag, u32 v0, u32 v1) {
  const u32x4 d = {v0, tag, v1, tag};
  asm volatile("global_store_dwordx4 %0, %1, off sc1\n\ts_nop 1" :: "v"((gu64*)g), "v"(d) : "memory");
}
__device__ __forceinline__ bool spin_fail(u32& spins, u32* tmo) {
  ++spins;
  if (spins > SPIN_CAP) { __hip_atomic_store((gu32*)tmo, 1u, RLX_AGENT); return true; }
  if ((spins & 1023u) == 0u) { if (__builtin_amdgcn_readfirstlane(__hip_atomic_load((gu32*)tmo, RLX_AGENT))) return true; }
  return false;
}
template <int N>
__device__ __forceinline__ bool sweep2(__amdgpu_buffer_rsrc_t rs, u32 byte_off, u32 byte_stride, u32 tag, u32 (&v)[2 * N], u32* tmo) {
  for (u32 spins = 0;;) {
    bool ok = true;
#pragma unroll
    for (int k = 0; k < N; ++k) {
      const u32x4 x = __builtin_bit_cast(u32x4, __builtin_amdgcn_raw_buffer_load_b128(rs, byte_off + k * byte_stride, 0, 16));
      v[2 * k] = x[0]; v[2 * k + 1] = x[2]; ok &= (x[1] == tag) & (x[3] == tag);
    }
    if (__all(ok)) return true;
    if (spin_fail(spins, tmo)) return false;
  }
}
__device__ __forceinline__ bool wait_flag(const u32* f, u32* tmo) {
  for (u32 spins = 0;;) {
    if (__builtin_amdgcn_readfirstlane(__hip_atomic_load((gu32*)f, RLX_AGENT))) return true;
    if (spin_fail(spins, tmo)) return false;
    __builtin_amdgcn_s_sleep(8);
  }
}

#define STAMP(i) do { } while (0)
#define STAMP5 do { } while (0)

constexpr int C_LM = 1032, C_LQ = 136, C_LV = 72;
constexpr int C_OFF_WO = 0;
constexpr int C_OFF_MEM = C_OFF_WO + 65536;
constexpr int C_OFF_RED = C_OFF_MEM + 8 * C_LM * 2;
constexpr int C_OFF_Q = C_OFF_RED + 8 * 8 * 48 * 4;
constexpr int C_OFF_K = C_OFF_Q + 8 * C_LQ * 2;
constexpr int C_OFF_V = C_OFF_K + 48 * C_LQ * 2;
constexpr int C_LDS = C_OFF_V + 128 * C_LV * 2;
constexpr int W_LDS_A = gemm_lds(256, 128, 4, 2, 3), W_LDS_B = gemm_lds(128, 128, 4, 2, 4);
constexpr int FUSED_LDS = (C_LDS > W_LDS_A ? (C_LDS > W_LDS_B ? C_LDS : W_LDS_B) : (W_LDS_A > W_LDS_B ? W_LDS_A : W_LDS_B));
__device__ __forceinline__ float ror4_sum(float v) {
  return v + __builtin_bit_cast(float, __builtin_amdgcn_update_dpp(0, __builtin_bit_cast(int, v), 0x124, 0xf, 0xf, false));
}

__device__ __forceinline__ void chain_role(const FusedP& p, char* smem, int cb, int h, int j) {
  constexpr int LM = C_LM, LQ = C_LQ, LV = C_LV;
  u16* memS = (u16*)(smem + C_OFF_MEM);
  float* red = (float*)(smem + C_OFF_RED);
  u16* qS = (u16*)(smem + C_OFF_Q);
  u16* kS = (u16*)(smem + C_OFF_K);
  u16* vT = (u16*)(smem + C_OFF_V);
  const int t = threadIdx.x, lane = t & 63, w = __builtin_amdgcn_readfirstlane(t >> 6);
  const int wk = w & 3, wc = w >> 2;
  bf16x8* woS = (bf16x8*)(smem + C_OFF_WO) + (w & 3) * 16 * 64 + lane;
  const int R = h * 4 + j;
  const int fr = lane & 15, fq = lane >> 4;
  u32* tmo = p.ctl;
  const __amdgpu_buffer_rsrc_t rs_mem = __builtin_amdgcn_make_buffer_rsrc((void*)p.memg, 0, 2 * NREP * NG_MEM * 8, 0x00020000);
  const __amdgpu_buffer_rsrc_t rs_qkv = __builtin_amdgcn_make_buffer_rsrc((void*)p.qkvg, 0, 2 * NH * 4 * NG_QKV * 8, 0x00020000);
  const __amdgpu_buffer_rsrc_t rs_part = __builtin_amdgcn_make_buffer_rsrc((void*)p.partg, 0, 2 * 128 * NH * 64 * 8, 0x00020000);
  const __amdgpu_buffer_rsrc_t rs_k = __builtin_amdgcn_make_buffer_rsrc((void*)p.Kx, 0, S_ * HD * 2, 0x00020000);
  const __amdgpu_buffer_rsrc_t rs_v = __builtin_amdgcn_make_buffer_rsrc((void*)p.Vx, 0, S_ * HD * 2, 0x00020000);

  bf16x8 wf[3][8];
  {
    const u16* Ws[3] = {p.Wq, p.Wk, p.Wv};
#pragma unroll
    for (int s = 0; s < 8; ++s) {
#pragma unroll
      for (int m = 0; m < 3; ++m) {
        const u16* src = Ws[m] + (size_t)(256 * wk + 32 * s + 8 * fq) * HD + h * DH + 32 * j + 16 * wc + fr;
#pragma unroll
        for (int i = 0; i < 8; ++i) wf[m][s][i] = (short)src[(size_t)i * HD];
      }
      asm volatile("" : "+v"(wf[0][s]), "+v"(wf[1][s]), "+v"(wf[2][s]) :: "memory");
    }
  }
#pragma unroll
  for (int c2 = 0; c2 < 2; ++c2)
#pragma unroll
    for (int s = 0; s < 4; ++s) {
      const int c = 2 * (w >> 2) + c2;
      bf16x8 f;
#pragma unroll
      for (int i = 0; i < 8; ++i) {
        const int d = 32 * s + ((i < 4) ? (4 * fq + i) : (16 + 4 * fq + i - 4));
        f[i] = (short)p.Wo[(size_t)(h * DH + d) * DM + 256 * j + 64 * (w & 3) + 16 * c + fr];
      }
      woS[(c * 4 + s) * 64] = f;
      asm volatile("" ::: "memory");
    }
  for (int e = t; e < 128 * LV; e += 512) vT[e] = 0;
  for (int e = t; e < 48 * LQ; e += 512) kS[e] = 0;
  u32x4 kpre, vpre;
  auto load_kv = [&](int seg) -> bool {
    if ((seg & 7) == 0) {
      if (!wait_flag(p.kvflag + ((seg >> 3) * 64 + h) * 16, tmo)) return false;
      if (!wait_flag(p.kvflag + ((seg >> 3) * 64 + 32 + h) * 16, tmo)) return false;
    }
    const int r = t >> 4, c = (t & 15) * 8;
    const u32 off = (u32)(((seg * SEG + r) * HD + h * DH + c) * 2);
    kpre = __builtin_bit_cast(u32x4, __builtin_amdgcn_raw_buffer_load_b128(rs_k, off, 0, 16));
    vpre = __builtin_bit_cast(u32x4, __builtin_amdgcn_raw_buffer_load_b128(rs_v, off, 0, 16));
    return true;
  };
  if (!load_kv(0)) return;
  __syncthreads();

  for (int s = 0; s < NSEG; ++s) {
    const int par = s & 1;
    const u32 epoch = (u32)s + 1u;
    STAMP(0);
    if (s == 0) {
#pragma unroll
      for (int i = 0; i < 2; ++i) {
        const int e = t + i * 512, r = e >> 7, c = (e & 127) * 8;
        *(u32x4*)(&memS[r * LM + c]) = *(const u32x4*)(p.mem0 + r * DM + c);
      }
    } else {
      if (w < H1W) {
        constexpr int NL = 32 / H1W;
        u32 v[2 * NL];
        const u32 gbase = (u32)(((par ^ 1) * NREP + (cb & (NREP - 1))) * NG_MEM + w * (4096 / H1W));
        __builtin_amdgcn_s_sleep(H1_DELAY);
        if (!sweep2<NL>(rs_mem, (gbase + lane * 2) * 8, 128 * 8, (u32)s, v, tmo)) return;
#pragma unroll
        for (int k = 0; k < NL; ++k) {
          const int u = lane + k * 64;
          *(uint2*)(&memS[((u >> 1) & 7) * LM + (w * (128 / H1W) + (u >> 4)) * 8 + (u & 1) * 4]) = make_uint2(v[2 * k], v[2 * k + 1]);
        }
      }
    }
    {
      const int r = t >> 4, c = (t & 15) * 8;
      *(u32x4*)(&kS[r * LQ + c]) = kpre;
      const u16* vp = (const u16*)&vpre;
      const int pos = (r < 16) ? (8 * (r >> 2) + (r & 3)) : (8 * ((r - 16) >> 2) + 4 + (r & 3));
#pragma unroll
      for (int q = 0; q < 8; ++q) vT[(c + q) * LV + pos] = vp[q];
    }
    __syncthreads();
    STAMP(1);
    {
      f32x4 acc[3] = {{0.f, 0.f, 0.f, 0.f}, {0.f, 0.f, 0.f, 0.f}, {0.f, 0.f, 0.f, 0.f}};
      bf16x8 a[8];
#pragma unroll
      for (int ks = 0; ks < 8; ++ks) a[ks] = *(const bf16x8*)(&memS[(lane & 7) * LM + 256 * wk + 32 * ks + 8 * fq]);
      __builtin_amdgcn_sched_barrier(0);
#pragma unroll
      for (int ks = 0; ks < 8; ++ks)
#pragma unroll
        for (int m = 0; m < 3; ++m) acc[m] = __builtin_amdgcn_mfma_f32_16x16x32_bf16(a[ks], wf[m][ks], acc[m], 0, 0, 0);
      if (fq < 2) {
#pragma unroll
        for (int m = 0; m < 3; ++m)
#pragma unroll
          for (int r = 0; r < 4; ++r) red[(w * 8 + fq * 4 + r) * 48 + m * 16 + fr] = acc[m][r];
      }
    }
    __syncthreads();
    if (t < NG_QKV / 2) {
      const int r = t / 24, cq = t % 24;
      const int m = cq >> 3, c = (cq & 7) * 4, g = c >> 4;
      float4 v4 = make_float4(0.f, 0.f, 0.f, 0.f);
#pragma unroll
      for (int ww = 0; ww < 4; ++ww) { const float4 q4 = *(const float4*)(&red[((g * 4 + ww) * 8 + r) * 48 + m * 16 + (c & 15)]); v4.x += q4.x; v4.y += q4.y; v4.z += q4.z; v4.w += q4.w; }
      const u32 pk0 = cvtpk(v4.x, v4.y), pk1 = cvtpk(v4.z, v4.w);
      if (s < NSEG - 1) st_gran2(p.qkvg + ((size_t)(par * NH + h) * 4 + j) * NG_QKV + 2 * t, epoch, pk0, pk1);
      if (m == 1) *(uint2*)(p.KM + (size_t)(s * MM + r) * HD + h * DH + 32 * j + c) = make_uint2(pk0, pk1);
      if (m == 2) *(uint2*)(p.VM + (size_t)(s * MM + r) * HD + h * DH + 32 * j + c) = make_uint2(pk0, pk1);
    }
    STAMP(2);
    if (s == NSEG - 1) break;
    bf16x8 kx[2][4], vx[8];
#pragma unroll
    for (int ks = 0; ks < 4; ++ks)
#pragma unroll
      for (int n = 0; n < 2; ++n) kx[n][ks] = *(const bf16x8*)(&kS[(n * 16 + fr) * LQ + 32 * ks + 8 * fq]);
    __builtin_amdgcn_sched_barrier(0);
    __builtin_amdgcn_s_barrier();
    __builtin_amdgcn_s_sleep(H2DELAY);
    if (w >= H2W0 && w < H2W0 + H2W) {
      constexpr int NL = 12 / H2W;
      u32 v[2 * NL];
      const int wv = w - H2W0;
      const u32 gbase = (u32)((par * NH + h) * 4 * NG_QKV + wv * NL * 128);
      if (!sweep2<NL>(rs_qkv, (gbase + lane * 2) * 8, 128 * 8, epoch, v, tmo)) return;
#pragma unroll
      for (int k = 0; k < NL; ++k) {
        const int ug = wv * NL * 64 + k * 64 + lane, pj = ug / 192, u = ug % 192;
        const int r = u / 24, cq = u % 24, m = cq >> 3, c = 32 * pj + (cq & 7) * 4;
        if (m == 0) *(uint2*)(&qS[r * LQ + c]) = make_uint2(v[2 * k], v[2 * k + 1]);
        else if (m == 1) *(uint2*)(&kS[(32 + r) * LQ + c]) = make_uint2(v[2 * k], v[2 * k + 1]);
        else {
          const int pos = 32 + 8 * (r >> 2) + (r & 3);
          vT[c * LV + pos] = (u16)(v[2 * k] & 0xffffu); vT[(c + 1) * LV + pos] = (u16)(v[2 * k] >> 16);
          vT[(c + 2) * LV + pos] = (u16)(v[2 * k + 1] & 0xffffu); vT[(c + 3) * LV + pos] = (u16)(v[2 * k + 1] >> 16);
        }
      }
    }
    __syncthreads();
    STAMP(3);
    if (w < 4) {
      f32x4 sc[3] = {{0.f, 0.f, 0.f, 0.f}, {0.f, 0.f, 0.f, 0.f}, {0.f, 0.f, 0.f, 0.f}};
      bf16x8 qf[4], km[4];
      uint2 vm[8];
#pragma unroll
      for (int ks = 0; ks < 4; ++ks) {
        qf[ks] = *(const bf16x8*)(&qS[(lane & 7) * LQ + 32 * ks + 8 * fq]);
        km[ks] = *(const bf16x8*)(&kS[(32 + fr) * LQ + 32 * ks + 8 * fq]);
      }
#pragma unroll
      for (int c = 0; c < 8; ++c) vm[c] = *(const uint2*)(&vT[(16 * c + fr) * LV + 32 + 8 * fq]);
      __builtin_amdgcn_sched_barrier(0);
#pragma unroll
      for (int ks = 0; ks < 4; ++ks) {
        sc[0] = __builtin_amdgcn_mfma_f32_16x16x32_bf16(kx[0][ks], qf[ks], sc[0], 0, 0, 0);
        sc[1] = __builtin_amdgcn_mfma_f32_16x16x32_bf16(kx[1][ks], qf[ks], sc[1], 0, 0, 0);
        sc[2] = __builtin_amdgcn_mfma_f32_16x16x32_bf16(km[ks], qf[ks], sc[2], 0, 0, 0);
      }
      __builtin_amdgcn_sched_barrier(0);
#pragma unroll
      for (int c = 0; c < 8; ++c) vx[c] = *(const bf16x8*)(&vT[(16 * c + fr) * LV + 8 * fq]);

      __builtin_amdgcn_sched_barrier(0);
      const float scale = 0.08838834764831845f;
      const bool live2 = fq < 2;
      float xs[3][4];
#pragma unroll
      for (int n = 0; n < 3; ++n)
#pragma unroll
        for (int r = 0; r < 4; r += 2) {
          const u32 a = cvtpk(sc[n][r], sc[n][r + 1]);
          const u32 c2 = cvtpk(lo2f(a) * scale, hi2f(a) * scale);
          xs[n][r] = lo2f(c2); xs[n][r + 1] = hi2f(c2);
        }
      if (!live2) { xs[2][0] = xs[2][1] = xs[2][2] = xs[2][3] = -3.0e38f; }
      float mx = fmaxf(fmaxf(fmaxf(xs[0][0], xs[0][1]), fmaxf(xs[0][2], xs[0][3])), fmaxf(fmaxf(xs[1][0], xs[1][1]), fmaxf(xs[1][2], xs[1][3])));
      mx = fmaxf(mx, fmaxf(fmaxf(xs[2][0], xs[2][1]), fmaxf(xs[2][2], xs[2][3])));
      mx = xrow_max(mx);
      float sm = 0.f;
#pragma unroll
      for (int n = 0; n < 3; ++n)
#pragma unroll
        for (int r = 0; r < 4; ++r) { xs[n][r] = __expf(xs[n][r] - mx); sm += xs[n][r]; }
      sm = xrow_sum(sm);
      const float inv = 1.0f / sm;
      u32x4 pw0, pw1;
      pw0[0] = cvtpk(xs[0][0] * inv, xs[0][1] * inv); pw0[1] = cvtpk(xs[0][2] * inv, xs[0][3] * inv);
      pw0[2] = cvtpk(xs[1][0] * inv, xs[1][1] * inv); pw0[3] = cvtpk(xs[1][2] * inv, xs[1][3] * inv);
      pw1[0] = cvtpk(xs[2][0] * inv, xs[2][1] * inv); pw1[1] = cvtpk(xs[2][2] * inv, xs[2][3] * inv);
      pw1[2] = 0u; pw1[3] = 0u;
      const bf16x8 pf0 = __builtin_bit_cast(bf16x8, pw0), pf1 = __builtin_bit_cast(bf16x8, pw1);
      u32x4 ow[4];
#pragma unroll
      for (int c = 0; c < 8; ++c) {
        const u32x4 vmw = {vm[c].x, vm[c].y, 0u, 0u};
        f32x4 oc = {0.f, 0.f, 0.f, 0.f};
        oc = __builtin_amdgcn_mfma_f32_16x16x32_bf16(vx[c], pf0, oc, 0, 0, 0);
        oc = __builtin_amdgcn_mfma_f32_16x16x32_bf16(__builtin_bit_cast(bf16x8, vmw), pf1, oc, 0, 0, 0);
        ow[c >> 1][(c & 1) * 2] = cvtpk(oc[0], oc[1]); ow[c >> 1][(c & 1) * 2 + 1] = cvtpk(oc[2], oc[3]);
      }
      f32x4 pc[4];
#pragma unroll
      for (int c = 0; c < 4; ++c) pc[c] = (f32x4){0.f, 0.f, 0.f, 0.f};
#pragma unroll
      for (int ks = 0; ks < 4; ++ks)
#pragma unroll
        for (int c = 0; c < 4; ++c) pc[c] = __builtin_amdgcn_mfma_f32_16x16x32_bf16(woS[(c * 4 + ks) * 64], __builtin_bit_cast(bf16x8, ow[ks]), pc[c], 0, 0, 0);
      if (fr < 8) {
#pragma unroll
        for (int c = 0; c < 4; ++c) {
          u64* dst = p.partg + ((size_t)(par * 128 + 32 * j + 8 * w + 2 * c + (fq >> 1)) * NH + h) * 64 + fr * 8 + 4 * (fq & 1);
          st_gran2(dst, epoch, __float_as_uint(pc[c][0]), __float_as_uint(pc[c][1]));
          st_gran2(dst + 2, epoch, __float_as_uint(pc[c][2]), __float_as_uint(pc[c][3]));
        }
      }
    }
    STAMP(4);
    __syncthreads();
    if (w >= 4 && w < 4 + H3W) {
      constexpr int UPW = 32 / H3W;
      constexpr int HG = 64 / UPW, NL = 32 / HG;
      u32 v[2 * NL];
      const int w4 = w - 4;
      const int hg = lane / UPW, u = lane % UPW;
      const u32 gbase = (u32)(((par * 128 + R) * NH + NL * hg) * 64 + (UPW * w4 + u) * 2);
      __builtin_amdgcn_s_sleep(H3DELAY);
      if (!sweep2<NL>(rs_part, gbase * 8, 64 * 8, epoch, v, tmo)) return;
      float f0 = 0.f, f1 = 0.f;
#pragma unroll
      for (int k = 0; k < NL; ++k) { f0 += __uint_as_float(v[2 * k]); f1 += __uint_as_float(v[2 * k + 1]); }
      if (UPW == 8) { f0 = ror8_sum(f0); f1 = ror8_sum(f1); }
      if (UPW <= 16) { f0 = xrow_sum(f0); f1 = xrow_sum(f1); }
      else {
        auto c0 = __builtin_amdgcn_permlane32_swap(__float_as_uint(f0), __float_as_uint(f0), false, false);
        auto c1 = __builtin_amdgcn_permlane32_swap(__float_as_uint(f1), __float_as_uint(f1), false, false);
        f0 = __uint_as_float(c0[0]) + __uint_as_float(c0[1]); f1 = __uint_as_float(c1[0]) + __uint_as_float(c1[1]);
      }
      const u32 pk = cvtpk(f0, f1);
#pragma unroll
      for (int q = 0; q < NREP / HG; ++q) st_gran(p.memg + (size_t)(par * NREP + q * HG + hg) * NG_MEM + R * 32 + UPW * w4 + u, epoch, pk);
      STAMP5;
    }
    if (!load_kv(s + 1)) return;
    __syncthreads();
  }
}

__device__ __forceinline__ void worker_role(const FusedP& p, char* smem, int gw) {
  const int t = threadIdx.x;
  u32* tmo = p.ctl;
  for (int i = 0; i < 4; ++i) {
    const int id = gw + 128 * i, rb = id >> 6, n = id & 63;
    const u16* B = (n < 32 ? p.Wk : p.Wv) + (n & 31) * 128;
    u16* C = (n < 32 ? p.Kx : p.Vx) + (size_t)rb * 256 * HD + (n & 31) * 128;
    gemm_tile<256, 128, 4, 2, 3>(smem, p.xn + (size_t)rb * 256 * DM, DM, B, HD, DM, C, HD, nullptr, 0, true);
    asm volatile("s_waitcnt vmcnt(0)" ::: "memory");
    __syncthreads();
    if (t == 0) __hip_atomic_store((gu32*)(p.kvflag + (rb * 64 + n) * 16), 1u, RLX_AGENT);
  }
  for (int id = gw; id < 480; id += 128) {
    if (id < 224) {
      const int rb = id / 28, n = id % 28;
      gemm_tile<256, 128, 4, 2, 3>(smem, p.xn + (size_t)rb * 256 * DM, DM, p.w1 + n * 128, FF, DM, p.Hb + (size_t)rb * 256 * FF + n * 128, FF, p.b1 + n * 128, 2, false);
    } else {
      const int q = id - 224, rb = q >> 5, n = q & 31;
      gemm_tile<256, 128, 4, 2, 3>(smem, p.xn + (size_t)rb * 256 * DM, DM, p.Wq + n * 128, HD, DM, p.Qx + (size_t)rb * 256 * HD + n * 128, HD, nullptr, 0, false);
    }
  }
  asm volatile("s_waitcnt vmcnt(0)" ::: "memory");
  __syncthreads();
  if (t == 0) {
    __builtin_amdgcn_fence(__ATOMIC_RELEASE, "agent");
    asm volatile("s_waitcnt vmcnt(0)" ::: "memory");
    __hip_atomic_fetch_add((gu32*)(p.ctl + 64), 1u, RLX_AGENT);
    for (u32 spins = 0;;) {
      if (__hip_atomic_load((gu32*)(p.ctl + 64), RLX_AGENT) >= 128u) break;
      if (++spins > SPIN_CAP) { __hip_atomic_store((gu32*)tmo, 1u, RLX_AGENT); break; }
      __builtin_amdgcn_s_sleep(16);
    }
    __builtin_amdgcn_fence(__ATOMIC_ACQUIRE, "agent");
    asm volatile("s_waitcnt vmcnt(0)" ::: "memory");
  }
  __syncthreads();
  {
    const int mt = gw >> 3, nt = gw & 7;
    gemm_tile<128, 128, 4, 2, 4>(smem, p.Hb + (size_t)mt * 128 * FF, FF, p.w2 + nt * 128, DM, FF, p.Bm + (size_t)mt * 128 * DM + nt * 128, DM, p.b2 + nt * 128, 1, false);
  }
}

__global__ __launch_bounds__(512, 2) void fused_kernel(FusedP p) {
  extern __shared__ __attribute__((aligned(16))) char smem[];
  const int b = blockIdx.x, xcd = b & 7, ib = b >> 3;
  if (ib < 16) chain_role(p, smem, xcd * 16 + ib, xcd * 4 + (ib >> 2), ib & 3);
  else worker_role(p, smem, (ib - 16) * 8 + xcd);
}

__global__ __launch_bounds__(256) void attn_x_kernel(const u16* __restrict__ Q, const u16* __restrict__ Kx, const u16* __restrict__ Vx,
                                                     const u16* __restrict__ KM, const u16* __restrict__ VM, u16* __restrict__ O) {
  constexpr int LVR = 136;
  __shared__ __attribute__((aligned(16))) u16 vS[4][48 * LVR];
  const int t = threadIdx.x, lane = t & 63, w = __builtin_amdgcn_readfirstlane(t >> 6);
  const int item = blockIdx.x * 4 + w, s = item >> 5, h = item & 31;
  const int fr = lane & 15, fq = lane >> 4;
  u16* vw = vS[w];
  const size_t xoff = (size_t)(s * SEG) * HD + h * DH, moff = (size_t)(s * MM) * HD + h * DH;
#pragma unroll
  for (int i = 0; i < 12; ++i) {
    const int e = lane + i * 64, r = e >> 4, c = (e & 15) * 8;
    u32x4 v = {0u, 0u, 0u, 0u};
    if (r < 32) v = *(const u32x4*)(Vx + xoff + (size_t)r * HD + c);
    else if (r < 40) v = *(const u32x4*)(VM + moff + (size_t)(r - 32) * HD + c);
    *(u32x4*)(&vw[r * LVR + c]) = v;
  }
  bf16x8 qf[2][4], kf[3][4];
#pragma unroll
  for (int ks = 0; ks < 4; ++ks) {
#pragma unroll
    for (int n = 0; n < 2; ++n) {
      qf[n][ks] = *(const bf16x8*)(Q + xoff + (size_t)(16 * n + fr) * HD + 32 * ks + 8 * fq);
      kf[n][ks] = *(const bf16x8*)(Kx + xoff + (size_t)(16 * n + fr) * HD + 32 * ks + 8 * fq);
    }
    kf[2][ks] = *(const bf16x8*)(KM + moff + (size_t)(fr & 7) * HD + 32 * ks + 8 * fq);
  }
  asm volatile("s_waitcnt lgkmcnt(0)" ::: "memory");
  __builtin_amdgcn_wave_barrier();
  bf16x8 vf[8][2];
  const int tq = fr >> 2, tp = fr & 3;
#pragma unroll
  for (int c = 0; c < 8; ++c) {
    const s16x4 a0 = __builtin_amdgcn_ds_read_tr16_b64_v4i16((LAS s16x4*)(&vw[(4 * fq + tq) * LVR + 16 * c + 4 * tp]));
    const s16x4 a1 = __builtin_amdgcn_ds_read_tr16_b64_v4i16((LAS s16x4*)(&vw[(16 + 4 * fq + tq) * LVR + 16 * c + 4 * tp]));
    const s16x4 a2 = __builtin_amdgcn_ds_read_tr16_b64_v4i16((LAS s16x4*)(&vw[(32 + 4 * fq + tq) * LVR + 16 * c + 4 * tp]));
    vf[c][0] = (bf16x8){a0[0], a0[1], a0[2], a0[3], a1[0], a1[1], a1[2], a1[3]};
    vf[c][1] = (bf16x8){a2[0], a2[1], a2[2], a2[3], 0, 0, 0, 0};
  }
  const float scale = 0.08838834764831845f;
#pragma unroll
  for (int qt = 0; qt < 2; ++qt) {
    f32x4 sc[3] = {{0.f, 0.f, 0.f, 0.f}, {0.f, 0.f, 0.f, 0.f}, {0.f, 0.f, 0.f, 0.f}};
#pragma unroll
    for (int ks = 0; ks < 4; ++ks)
#pragma unroll
      for (int n = 0; n < 3; ++n) sc[n] = __builtin_amdgcn_mfma_f32_16x16x32_bf16(kf[n][ks], qf[qt][ks], sc[n], 0, 0, 0);
    const int qi = 16 * qt + fr;
    float xs[3][4];
#pragma unroll
    for (int n = 0; n < 3; ++n)
#pragma unroll
      for (int r = 0; r < 4; r += 2) {
        const u32 a = cvtpk(sc[n][r], sc[n][r + 1]);
        const u32 c2 = cvtpk(lo2f(a) * scale, hi2f(a) * scale);
        xs[n][r] = lo2f(c2); xs[n][r + 1] = hi2f(c2);
      }
    float mx = -3.0e38f;
#pragma unroll
    for (int n = 0; n < 3; ++n)
#pragma unroll
      for (int r = 0; r < 4; ++r) {
        const bool live = (n < 2) ? (16 * n + 4 * fq + r <= qi) : (fq < 2);
        if (!live) xs[n][r] = -3.0e38f;
        mx = fmaxf(mx, xs[n][r]);
      }
    mx = xrow_max(mx);
    float sm = 0.f;
#pragma unroll
    for (int n = 0; n < 3; ++n)
#pragma unroll
      for (int r = 0; r < 4; ++r) { xs[n][r] = __expf(xs[n][r] - mx); sm += xs[n][r]; }
    sm = xrow_sum(sm);
    const float inv = 1.0f / sm;
    u32x4 pw0, pw1;
    pw0[0] = cvtpk(xs[0][0] * inv, xs[0][1] * inv); pw0[1] = cvtpk(xs[0][2] * inv, xs[0][3] * inv);
    pw0[2] = cvtpk(xs[1][0] * inv, xs[1][1] * inv); pw0[3] = cvtpk(xs[1][2] * inv, xs[1][3] * inv);
    pw1[0] = cvtpk(xs[2][0] * inv, xs[2][1] * inv); pw1[1] = cvtpk(xs[2][2] * inv, xs[2][3] * inv);
    pw1[2] = 0u; pw1[3] = 0u;
    const bf16x8 pf0 = __builtin_bit_cast(bf16x8, pw0), pf1 = __builtin_bit_cast(bf16x8, pw1);
    u16* orow = O + xoff + (size_t)qi * HD + 4 * fq;
#pragma unroll
    for (int c = 0; c < 8; ++c) {
      f32x4 oc = {0.f, 0.f, 0.f, 0.f};
      oc = __builtin_amdgcn_mfma_f32_16x16x32_bf16(vf[c][0], pf0, oc, 0, 0, 0);
      oc = __builtin_amdgcn_mfma_f32_16x16x32_bf16(vf[c][1], pf1, oc, 0, 0, 0);
      *(uint2*)(orow + 16 * c) = make_uint2(cvtpk(oc[0], oc[1]), cvtpk(oc[2], oc[3]));
    }
  }
}

extern "C" void kernel_launch(void* const* d_in, const int* in_sizes, int n_in,
                              void* d_out, int out_size, void* d_ws, size_t ws_size,
                              hipStream_t stream) {
  const u16* x = (const u16*)d_in[0];
  const u16* mem0 = (const u16*)d_in[1];
  const u16* Wq = (const u16*)d_in[2];
  const u16* Wk = (const u16*)d_in[3];
  const u16* Wv = (const u16*)d_in[4];
  const u16* Wo = (const u16*)d_in[5];
  const u16* w1 = (const u16*)d_in[8];
  const u16* b1 = (const u16*)d_in[9];
  const u16* w2 = (const u16*)d_in[10];
  const u16* b2 = (const u16*)d_in[11];
  u16* out = (u16*)d_out;

  char* ws = (char*)d_ws;
  size_t off = 0;
  auto carve = [&](size_t bytes) { char* q = ws + off; off += (bytes + 255) & ~(size_t)255; return q; };
  u64* memg = (u64*)carve((size_t)2 * NREP * NG_MEM * 8);
  u64* qkvg = (u64*)carve((size_t)2 * NH * 4 * NG_QKV * 8);
  u64* partg = (u64*)carve((size_t)2 * 128 * NH * 64 * 8);
  u32* kvflag = (u32*)carve((size_t)512 * 64);
  u32* ctl = (u32*)carve(1024);
  const size_t sync_bytes = off;
  u16* xn = (u16*)carve((size_t)S_ * DM * 2);
  u16* Qx = (u16*)carve((size_t)S_ * HD * 2);
  u16* Kx = (u16*)carve((size_t)S_ * HD * 2);
  u16* Vx = (u16*)carve((size_t)S_ * HD * 2);
  u16* Hb = (u16*)carve((size_t)S_ * FF * 2);
  u16* Ox = (u16*)carve((size_t)S_ * HD * 2);
  u16* KM = (u16*)carve((size_t)NSEG * MM * HD * 2);
  u16* VM = (u16*)carve((size_t)NSEG * MM * HD * 2);
  u16* Bm = (u16*)carve((size_t)S_ * DM * 2);

  ln_kernel<<<S_, 256, 0, stream>>>(x, xn, (u32x4*)ws, (int)(sync_bytes / 16));
  {
    FusedP f{xn, mem0, Wq, Wk, Wv, Wo, w1, b1, w2, b2, Qx, Kx, Vx, Hb, Bm, KM, VM, memg, qkvg, partg, kvflag, ctl};
    (void)hipFuncSetAttribute((const void*)fused_kernel, hipFuncAttributeMaxDynamicSharedMemorySize, FUSED_LDS);
    fused_kernel<<<256, 512, FUSED_LDS, stream>>>(f);
  }
  attn_x_kernel<<<NSEG * NH / 4, 256, 0, stream>>>(Qx, Kx, Vx, KM, VM, Ox);
  {
    constexpr int LDSW = gemm_lds(WO_BM, WO_BN, WO_WM, WO_WN, WO_NS);
    (void)hipFuncSetAttribute((const void*)gemm_wo_kernel, hipFuncAttributeMaxDynamicSharedMemorySize, LDSW);
    gemm_wo_kernel<<<(S_ / WO_BM) * (DM / WO_BN), WO_WM * WO_WN * 64, LDSW, stream>>>(Ox, Wo, x, Bm, out);
  }
}
```

```cpp
#include <hip/hip_runtime.h>
#include <hip/hip_bf16.h>
#include <stdint.h>

typedef unsigned short u16;
typedef unsigned int u32;
typedef unsigned long long u64;
#define GAS __attribute__((address_space(1)))
#define LAS __attribute__((address_space(3)))
typedef GAS u64 gu64;
typedef GAS u32 gu32;
typedef __attribute__((ext_vector_type(8))) short bf16x8;
typedef __attribute__((ext_vector_type(4))) short s16x4;
typedef __attribute__((ext_vector_type(4))) float f32x4;
typedef __attribute__((ext_vector_type(4))) unsigned int u32x4;
#define RLX_AGENT __ATOMIC_RELAXED, __HIP_MEMORY_SCOPE_AGENT

constexpr int S_ = 2048, DM = 1024, NH = 32, DH = 128, HD = 4096, MM = 8, SEG = 32, NSEG = 64, FF = 3584;

__device__ __forceinline__ float bf2f(u16 v) { return __uint_as_float(((u32)v) << 16); }
__device__ __forceinline__ u16 f2bf(float f) { u32 u = __float_as_uint(f); u += 0x7fffu + ((u >> 16) & 1u); return (u16)(u >> 16); }
__device__ __forceinline__ float rbf(float f) { return bf2f(f2bf(f)); }
typedef __bf16 hbf2 __attribute__((ext_vector_type(2)));
typedef float f32x2 __attribute__((ext_vector_type(2)));
__device__ __forceinline__ u32 cvtpk(float a, float b) { f32x2 v = {a, b}; return __builtin_bit_cast(u32, __builtin_convertvector(v, hbf2)); }
__device__ __forceinline__ float lo2f(u32 v) { return __uint_as_float(v << 16); }
__device__ __forceinline__ float hi2f(u32 v) { return __uint_as_float(v & 0xffff0000u); }

__device__ __forceinline__ float xrow_max(float v) {
  auto a = __builtin_amdgcn_permlane16_swap(__float_as_uint(v), __float_as_uint(v), false, false);
  v = fmaxf(__uint_as_float(a[0]), __uint_as_float(a[1]));
  auto c = __builtin_amdgcn_permlane32_swap(__float_as_uint(v), __float_as_uint(v), false, false);
  return fmaxf(__uint_as_float(c[0]), __uint_as_float(c[1]));
}
__device__ __forceinline__ float xrow_sum(float v) {
  auto a = __builtin_amdgcn_permlane16_swap(__float_as_uint(v), __float_as_uint(v), false, false);
  v = __uint_as_float(a[0]) + __uint_as_float(a[1]);
  auto c = __builtin_amdgcn_permlane32_swap(__float_as_uint(v), __float_as_uint(v), false, false);
  return __uint_as_float(c[0]) + __uint_as_float(c[1]);
}
__device__ __forceinline__ float ror8_sum(float v) {
  return v + __builtin_bit_cast(float, __builtin_amdgcn_update_dpp(0, __builtin_bit_cast(int, v), 0x128, 0xf, 0xf, false));
}

__global__ __launch_bounds__(256) void ln_kernel(const u16* __restrict__ x, u16* __restrict__ xn, u32x4* __restrict__ zero_base, int zero_units) {
  __shared__ float red[8];
  const int row = blockIdx.x, t = threadIdx.x, lane = t & 63, w = t >> 6;
  for (int i = row * 256 + t; i < zero_units; i += S_ * 256) zero_base[i] = (u32x4){0u, 0u, 0u, 0u};
  const ushort4 v = *(const ushort4*)(x + (size_t)row * DM + t * 4);
  float f0 = bf2f(v.x), f1 = bf2f(v.y), f2 = bf2f(v.z), f3 = bf2f(v.w);
  float s = (f0 + f1) + (f2 + f3);
#pragma unroll
  for (int m = 32; m >= 1; m >>= 1) s += __shfl_xor(s, m);
  if (lane == 0) red[w] = s;
  __syncthreads();
  const float mean = ((red[0] + red[1]) + (red[2] + red[3])) * (1.0f / DM);
  float d0 = f0 - mean, d1 = f1 - mean, d2 = f2 - mean, d3 = f3 - mean;
  float q = (d0 * d0 + d1 * d1) + (d2 * d2 + d3 * d3);
#pragma unroll
  for (int m = 32; m >= 1; m >>= 1) q += __shfl_xor(q, m);
  if (lane == 0) red[4 + w] = q;
  __syncthreads();
  const float var = ((red[4] + red[5]) + (red[6] + red[7])) * (1.0f / DM);
  const float mb = rbf(mean), vb = rbf(var);
  const float den = rbf(sqrtf(rbf(vb + 1e-5f)));
  ushort4 o;
  o.x = f2bf(rbf(f0 - mb) / den); o.y = f2bf(rbf(f1 - mb) / den);
  o.z = f2bf(rbf(f2 - mb) / den); o.w = f2bf(rbf(f3 - mb) / den);
  *(ushort4*)(xn + (size_t)row * DM + t * 4) = o;
}

__device__ __forceinline__ float gelu_tanh(float x) {
  const float k0 = 0.7978845608028654f, k1 = 0.044715f;
  const float u = k0 * (x + k1 * x * x * x);
  return 0.5f * x * (1.0f + tanhf(u));
}
__device__ __forceinline__ void glds16(const void* gsrc, u32 lds_dst) {
  u32 keep;
  asm volatile("s_mov_b32 %0, m0\n\ts_mov_b32 m0, %2\n\ts_nop 0\n\tglobal_load_lds_dwordx4 %1, off\n\ts_mov_b32 m0, %0"
               : "=&s"(keep) : "v"(gsrc), "s"(lds_dst) : "memory");
}
template <int N> __device__ __forceinline__ void wait_vm() { asm volatile("s_waitcnt vmcnt(%0)" :: "n"(N) : "memory"); }
__device__ __forceinline__ void st16_sc1(void* p, u32x4 v) {
  asm volatile("global_store_dwordx4 %0, %1, off sc1\n\ts_nop 1" :: "v"(p), "v"(v) : "memory");
}
constexpr int gemm_lds(int BM, int BN, int WM, int WN, int NSTAGE) {
  const int st = NSTAGE * (BM * 64 * 2 + 64 * BN * 2), ep = WM * WN * (BM / WM) * (BN / WN + 8) * 2;
  return st > ep ? st : ep;
}
template <int BM, int BN, int WM, int WN, int NSTAGE>
__device__ __forceinline__ void gemm_tile(char* smem, const u16* A, int lda, const u16* B, int ldb, int K,
                                          u16* C, int ldc, const u16* bias, int EPI, bool WT,
                                          const u16* rx = nullptr, const u16* rb = nullptr) {
  constexpr int NW = WM * WN, NT = NW * 64, BK = 64;
  constexpr int TM = BM / WM / 16, TN = BN / WN / 16;
  constexpr int A_BYTES = BM * BK * 2, B_BYTES = BK * BN * 2, STAGE = A_BYTES + B_BYTES;
  constexpr int NSUB = BN / 16;
  const int t = threadIdx.x, lane = t & 63, w = __builtin_amdgcn_readfirstlane(t >> 6);
  const int wr = w / WN, wc = w % WN, fr = lane & 15, fq = lane >> 4;
  constexpr int AU = BM * 8 / NT, BU = BK * BN / 8 / NT, LPS = AU + BU;
  const u16* asrc[AU]; const u16* bsrc[BU];
#pragma unroll
  for (int i = 0; i < AU; ++i) {
    const int u = i * NT + t, row = u >> 3, c = (u & 7) ^ (row & 7);
    asrc[i] = A + (size_t)row * lda + c * 8;
  }
#pragma unroll
  for (int i = 0; i < BU; ++i) {
    const int u = i * NT + t, hf = u & 1, rp = (u >> 1) & 7, st = u >> 4, kb = st / NSUB, nb = st % NSUB;
    const int r = rp ^ (4 * (kb & 1));
    bsrc[i] = B + (size_t)(kb * 8 + r) * ldb + nb * 16 + hf * 8;
  }
  const u32 lds0 = (u32)(uintptr_t)(LAS char*)smem + (u32)w * 1024u;
  auto stage = [&](int kt, int buf) {
    const u32 base = lds0 + buf * STAGE;
#pragma unroll
    for (int i = 0; i < AU; ++i) glds16(asrc[i] + kt * BK, base + i * NT * 16);
#pragma unroll
    for (int i = 0; i < BU; ++i) glds16(bsrc[i] + (size_t)kt * BK * ldb, base + A_BYTES + i * NT * 16);
  };
  f32x4 acc[TM][TN];
#pragma unroll
  for (int i = 0; i < TM; ++i)
#pragma unroll
    for (int j = 0; j < TN; ++j) acc[i][j] = (f32x4){0.f, 0.f, 0.f, 0.f};
  const int arow0 = (wr * (BM / WM) + fr) * 128;
  const int arx = fr & 7;
  const int tq = (lane & 15) >> 2, tp = lane & 3;
  const int nk = K / BK;
  wait_vm<0>();
#pragma unroll
  for (int k = 0; k < NSTAGE - 1; ++k) stage(k, k);
  int buf = 0;
  for (int kt = 0; kt < nk; ++kt) {
    if (kt + NSTAGE - 1 <= nk) wait_vm<LPS * (NSTAGE - 2)>(); else wait_vm<0>();
    asm volatile("s_waitcnt lgkmcnt(0)" ::: "memory");
    __builtin_amdgcn_s_barrier();
    asm volatile("" ::: "memory");
    if (kt + NSTAGE - 1 < nk) { int nb2 = buf + NSTAGE - 1; if (nb2 >= NSTAGE) nb2 -= NSTAGE; stage(kt + NSTAGE - 1, nb2); }
    const char* sa = smem + buf * STAGE;
    const char* sb = sa + A_BYTES;
#pragma unroll
    for (int ks = 0; ks < 2; ++ks) {
      bf16x8 af[TM], bfr[TN];
#pragma unroll
      for (int i = 0; i < TM; ++i) af[i] = *(const bf16x8*)(sa + arow0 + i * 2048 + (((4 * ks + fq) ^ arx) * 16));
      const int kb = 4 * ks + fq;
      const int rsw = 4 * (kb & 1);
#pragma unroll
      for (int j = 0; j < TN; ++j) {
        const int nb = wc * TN + j;
        const char* sub = sb + (kb * NSUB + nb) * 256 + tp * 8;
        const s16x4 lo = __builtin_amdgcn_ds_read_tr16_b64_v4i16((LAS s16x4*)(sub + ((tq) ^ rsw) * 32));
        const s16x4 hi = __builtin_amdgcn_ds_read_tr16_b64_v4i16((LAS s16x4*)(sub + ((4 + tq) ^ rsw) * 32));
        bfr[j] = (bf16x8){lo[0], lo[1], lo[2], lo[3], hi[0], hi[1], hi[2], hi[3]};
      }
#pragma unroll
      for (int i = 0; i < TM; ++i)
#pragma unroll
        for (int j = 0; j < TN; ++j) acc[i][j] = __builtin_amdgcn_mfma_f32_16x16x32_bf16(bfr[j], af[i], acc[i][j], 0, 0, 0);
    }
    if (++buf == NSTAGE) buf = 0;
  }
  asm volatile("s_waitcnt lgkmcnt(0)" ::: "memory");
  __builtin_amdgcn_s_barrier();
  asm volatile("" ::: "memory");
  constexpr int WROWS = BM / WM, WCOLS = BN / WN, LDW = WCOLS + 8;
  u16* ew = (u16*)smem + w * WROWS * LDW;
#pragma unroll
  for (int j = 0; j < TN; ++j) {
    float bv[4] = {0.f, 0.f, 0.f, 0.f};
    if (EPI) {
      const ushort4 bb = *(const ushort4*)(bias + wc * WCOLS + j * 16 + fq * 4);
      bv[0] = bf2f(bb.x); bv[1] = bf2f(bb.y); bv[2] = bf2f(bb.z); bv[3] = bf2f(bb.w);
    }
#pragma unroll
    for (int i = 0; i < TM; ++i) {
      float v[4];
#pragma unroll
      for (int r = 0; r < 4; ++r) v[r] = acc[i][j][r];
      if (EPI) {
        const u32 a01 = cvtpk(v[0], v[1]), a23 = cvtpk(v[2], v[3]);
        v[0] = lo2f(a01) + bv[0]; v[1] = hi2f(a01) + bv[1]; v[2] = lo2f(a23) + bv[2]; v[3] = hi2f(a23) + bv[3];
        if (EPI == 2) {
          const u32 b01 = cvtpk(v[0], v[1]), b23 = cvtpk(v[2], v[3]);
          v[0] = gelu_tanh(lo2f(b01)); v[1] = gelu_tanh(hi2f(b01)); v[2] = gelu_tanh(lo2f(b23)); v[3] = gelu_tanh(hi2f(b23));
        }
      }
      *(uint2*)(&ew[(i * 16 + fr) * LDW + j * 16 + fq * 4]) = make_uint2(cvtpk(v[0], v[1]), cvtpk(v[2], v[3]));
    }
  }
  asm volatile("s_waitcnt lgkmcnt(0)" ::: "memory");
  __builtin_amdgcn_wave_barrier();
  constexpr int CPR = WCOLS / 8;
#pragma unroll 4
  for (int e = lane; e < WROWS * CPR; e += 64) {
    const int r = e / CPR, c = e % CPR;
    u32x4 v = *(const u32x4*)(&ew[r * LDW + c * 8]);
    const size_t eo = (size_t)(wr * WROWS + r) * ldc + wc * WCOLS + c * 8;
    u16* dst = C + eo;
    if (rx) {
      const u32x4 xv = *(const u32x4*)(rx + eo), bv = *(const u32x4*)(rb + eo);
#pragma unroll
      for (int q = 0; q < 4; ++q) {
        const u32 s1 = cvtpk(lo2f(xv[q]) + lo2f(v[q]), hi2f(xv[q]) + hi2f(v[q]));
        v[q] = cvtpk(lo2f(s1) + lo2f(bv[q]), hi2f(s1) + hi2f(bv[q]));
      }
    }
    if (WT) st16_sc1(dst, v); else *(u32x4*)dst = v;
  }
  asm volatile("s_waitcnt lgkmcnt(0)" ::: "memory");
  __builtin_amdgcn_s_barrier();
  asm volatile("" ::: "memory");
}

#define WO_BM 128
#define WO_BN 64
#define WO_WM 2
#define WO_WN 2
#define WO_NS 4
__global__ __launch_bounds__(WO_WM * WO_WN * 64) void gemm_wo_kernel(const u16* __restrict__ Ox, const u16* __restrict__ Wo, const u16* __restrict__ x,
                                                       const u16* __restrict__ Bm, u16* __restrict__ out) {
  extern __shared__ __attribute__((aligned(16))) char smem[];
  constexpr int NMT = S_ / WO_BM;
  const int xq = blockIdx.x & 7, oq = blockIdx.x >> 3;
  const int mt = oq % NMT, nt = xq + 8 * (oq / NMT);
  const size_t co = (size_t)mt * WO_BM * DM + nt * WO_BN;
  gemm_tile<WO_BM, WO_BN, WO_WM, WO_WN, WO_NS>(smem, Ox + (size_t)mt * WO_BM * HD, HD, Wo + nt * WO_BN, DM, HD, out + co, DM, nullptr, 0, false, x + co, Bm + co);
}

struct FusedP {
  const u16* xn; const u16* mem0; const u16* Wq; const u16* Wk; const u16* Wv; const u16* Wo;
  const u16* w1; const u16* b1; const u16* w2; const u16* b2;
  u16* Qx; u16* Kx; u16* Vx; u16* Hb; u16* Bm; u16* KM; u16* VM;
  u64* memg; u64* qkvg; u64* partg; u32* kvflag; u32* ctl;
};
constexpr int NG_MEM = 4096;
constexpr int NG_QKV = 384;
constexpr u32 SPIN_CAP = 2000000u;
constexpr int NREP = 8;
#define RETRY_SLEEP 0
#define H1W 4
#define H2W 4
#define H2W0 4
#define H3W 2
#define H2SYNC 1
#define H2DELAY 6
#define H3DELAY 16
#define H1_DELAY 8

__device__ __forceinline__ void st_gran(u64* g, u32 tag, u32 val) {
  __hip_atomic_store((gu64*)g, ((u64)tag << 32) | (u64)val, RLX_AGENT);
}
__device__ __forceinline__ void st_gran2(u64* g, u32 tag, u32 v0, u32 v1) {
  const u32x4 d = {v0, tag, v1, tag};
  asm volatile("global_store_dwordx4 %0, %1, off sc1\n\ts_nop 1" :: "v"((gu64*)g), "v"(d) : "memory");
}
__device__ __forceinline__ bool spin_fail(u32& spins, u32* tmo) {
  ++spins;
  if (spins > SPIN_CAP) { __hip_atomic_store((gu32*)tmo, 1u, RLX_AGENT); return true; }
  if ((spins & 1023u) == 0u) { if (__builtin_amdgcn_readfirstlane(__hip_atomic_load((gu32*)tmo, RLX_AGENT))) return true; }
  return false;
}
template <int N>
__device__ __forceinline__ bool sweep2(__amdgpu_buffer_rsrc_t rs, u32 byte_off, u32 byte_stride, u32 tag, u32 (&v)[2 * N], u32* tmo) {
  for (u32 spins = 0;;) {
    bool ok = true;
#pragma unroll
    for (int k = 0; k < N; ++k) {
      const u32x4 x = __builtin_bit_cast(u32x4, __builtin_amdgcn_raw_buffer_load_b128(rs, byte_off + k * byte_stride, 0, 16));
      v[2 * k] = x[0]; v[2 * k + 1] = x[2]; ok &= (x[1] == tag) & (x[3] == tag);
    }
    if (__all(ok)) return true;
    if (spin_fail(spins, tmo)) return false;
  }
}
__device__ __forceinline__ bool wait_flag(const u32* f, u32* tmo) {
  for (u32 spins = 0;;) {
    if (__builtin_amdgcn_readfirstlane(__hip_atomic_load((gu32*)f, RLX_AGENT))) return true;
    if (spin_fail(spins, tmo)) return false;
    __builtin_amdgcn_s_sleep(8);
  }
}

#define STAMP(i) do { } while (0)
#define STAMP5 do { } while (0)

constexpr int C_LM = 1032, C_LQ = 136, C_LV = 72;
constexpr int C_OFF_WO = 0;
constexpr int C_OFF_MEM = C_OFF_WO + 65536;
constexpr int C_OFF_RED = C_OFF_MEM + 8 * C_LM * 2;
constexpr int C_OFF_Q = C_OFF_RED + 8 * 8 * 48 * 4;
constexpr int C_OFF_K = C_OFF_Q + 8 * C_LQ * 2;
constexpr int C_OFF_V = C_OFF_K + 48 * C_LQ * 2;
constexpr int C_LDS = C_OFF_V + 128 * C_LV * 2;
constexpr int W_LDS_A = gemm_lds(256, 128, 4, 2, 3), W_LDS_B = gemm_lds(128, 128, 4, 2, 4);
constexpr int FUSED_LDS = (C_LDS > W_LDS_A ? (C_LDS > W_LDS_B ? C_LDS : W_LDS_B) : (W_LDS_A > W_LDS_B ? W_LDS_A : W_LDS_B));
__device__ __forceinline__ float ror4_sum(float v) {
  return v + __builtin_bit_cast(float, __builtin_amdgcn_update_dpp(0, __builtin_bit_cast(int, v), 0x124, 0xf, 0xf, false));
}

__device__ __forceinline__ void chain_role(const FusedP& p, char* smem, int cb, int h, int j) {
  constexpr int LM = C_LM, LQ = C_LQ, LV = C_LV;
  u16* memS = (u16*)(smem + C_OFF_MEM);
  float* red = (float*)(smem + C_OFF_RED);
  u16* qS = (u16*)(smem + C_OFF_Q);
  u16* kS = (u16*)(smem + C_OFF_K);
  u16* vT = (u16*)(smem + C_OFF_V);
  const int t = threadIdx.x, lane = t & 63, w = __builtin_amdgcn_readfirstlane(t >> 6);
  const int wk = w & 3, wc = w >> 2;
  bf16x8* woS = (bf16x8*)(smem + C_OFF_WO) + (w & 3) * 16 * 64 + lane;
  const int R = h * 4 + j;
  const int fr = lane & 15, fq = lane >> 4;
  u32* tmo = p.ctl;
  const __amdgpu_buffer_rsrc_t rs_mem = __builtin_amdgcn_make_buffer_rsrc((void*)p.memg, 0, 2 * NREP * NG_MEM * 8, 0x00020000);
  const __amdgpu_buffer_rsrc_t rs_qkv = __builtin_amdgcn_make_buffer_rsrc((void*)p.qkvg, 0, 2 * NH * 4 * NG_QKV * 8, 0x00020000);
  const __amdgpu_buffer_rsrc_t rs_part = __builtin_amdgcn_make_buffer_rsrc((void*)p.partg, 0, 2 * 128 * NH * 64 * 8, 0x00020000);
  const __amdgpu_buffer_rsrc_t rs_k = __builtin_amdgcn_make_buffer_rsrc((void*)p.Kx, 0, S_ * HD * 2, 0x00020000);
  const __amdgpu_buffer_rsrc_t rs_v = __builtin_amdgcn_make_buffer_rsrc((void*)p.Vx, 0, S_ * HD * 2, 0x00020000);

  bf16x8 wf[3][8];
  {
    const u16* Ws[3] = {p.Wq, p.Wk, p.Wv};
#pragma unroll
    for (int s = 0; s < 8; ++s) {
#pragma unroll
      for (int m = 0; m < 3; ++m) {
        const u16* src = Ws[m] + (size_t)(256 * wk + 32 * s + 8 * fq) * HD + h * DH + 32 * j + 16 * wc + fr;
#pragma unroll
        for (int i = 0; i < 8; ++i) wf[m][s][i] = (short)src[(size_t)i * HD];
      }
      asm volatile("" : "+v"(wf[0][s]), "+v"(wf[1][s]), "+v"(wf[2][s]) :: "memory");
    }
  }
#pragma unroll
  for (int c2 = 0; c2 < 2; ++c2)
#pragma unroll
    for (int s = 0; s < 4; ++s) {
      const int c = 2 * (w >> 2) + c2;
      bf16x8 f;
#pragma unroll
      for (int i = 0; i < 8; ++i) {
        const int d = 32 * s + ((i < 4) ? (4 * fq + i) : (16 + 4 * fq + i - 4));
        f[i] = (short)p.Wo[(size_t)(h * DH + d) * DM + 256 * j + 64 * (w & 3) + 16 * c + fr];
      }
      woS[(c * 4 + s) * 64] = f;
      asm volatile("" ::: "memory");
    }
  for (int e = t; e < 128 * LV; e += 512) vT[e] = 0;
  for (int e = t; e < 48 * LQ; e += 512) kS[e] = 0;
  u32x4 kpre, vpre;
  auto load_kv = [&](int seg) -> bool {
    if (seg == 0) {
      if (!wait_flag(p.kvflag + (512 + h) * 16, tmo)) return false;
      if (!wait_flag(p.kvflag + (512 + 32 + h) * 16, tmo)) return false;
    } else if ((seg & 7) == 0 || seg == 2) {
      if (!wait_flag(p.kvflag + ((seg >> 3) * 64 + h) * 16, tmo)) return false;
      if (!wait_flag(p.kvflag + ((seg >> 3) * 64 + 32 + h) * 16, tmo)) return false;
    }
    const int r = t >> 4, c = (t & 15) * 8;
    const u32 off = (u32)(((seg * SEG + r) * HD + h * DH + c) * 2);
    kpre = __builtin_bit_cast(u32x4, __builtin_amdgcn_raw_buffer_load_b128(rs_k, off, 0, 16));
    vpre = __builtin_bit_cast(u32x4, __builtin_amdgcn_raw_buffer_load_b128(rs_v, off, 0, 16));
    return true;
  };
  if (!load_kv(0)) return;
  __syncthreads();

  for (int s = 0; s < NSEG; ++s) {
    const int par = s & 1;
    const u32 epoch = (u32)s + 1u;
    STAMP(0);
    if (s == 0) {
#pragma unroll
      for (int i = 0; i < 2; ++i) {
        const int e = t + i * 512, r = e >> 7, c = (e & 127) * 8;
        *(u32x4*)(&memS[r * LM + c]) = *(const u32x4*)(p.mem0 + r * DM + c);
      }
    } else {
      if (w < H1W) {
        constexpr int NL = 32 / H1W;
        u32 v[2 * NL];
        const u32 gbase = (u32)(((par ^ 1) * NREP + (cb & (NREP - 1))) * NG_MEM + w * (4096 / H1W));
        __builtin_amdgcn_s_sleep(H1_DELAY);
        if (!sweep2<NL>(rs_mem, (gbase + lane * 2) * 8, 128 * 8, (u32)s, v, tmo)) return;
#pragma unroll
        for (int k = 0; k < NL; ++k) {
          const int u = lane + k * 64;
          *(uint2*)(&memS[((u >> 1) & 7) * LM + (w * (128 / H1W) + (u >> 4)) * 8 + (u & 1) * 4]) = make_uint2(v[2 * k], v[2 * k + 1]);
        }
      }
    }
    {
      const int r = t >> 4, c = (t & 15) * 8;
      *(u32x4*)(&kS[r * LQ + c]) = kpre;
      const u16* vp = (const u16*)&vpre;
      const int pos = (r < 16) ? (8 * (r >> 2) + (r & 3)) : (8 * ((r - 16) >> 2) + 4 + (r & 3));
#pragma unroll
      for (int q = 0; q < 8; ++q) vT[(c + q) * LV + pos] = vp[q];
    }
    __syncthreads();
    STAMP(1);
    {
      f32x4 acc[3] = {{0.f, 0.f, 0.f, 0.f}, {0.f, 0.f, 0.f, 0.f}, {0.f, 0.f, 0.f, 0.f}};
      bf16x8 a[8];
#pragma unroll
      for (int ks = 0; ks < 8; ++ks) a[ks] = *(const bf16x8*)(&memS[(lane & 7) * LM + 256 * wk + 32 * ks + 8 * fq]);
      __builtin_amdgcn_sched_barrier(0);
#pragma unroll
      for (int ks = 0; ks < 8; ++ks)
#pragma unroll
        for (int m = 0; m < 3; ++m) acc[m] = __builtin_amdgcn_mfma_f32_16x16x32_bf16(a[ks], wf[m][ks], acc[m], 0, 0, 0);
      if (fq < 2) {
#pragma unroll
        for (int m = 0; m < 3; ++m)
#pragma unroll
          for (int r = 0; r < 4; ++r) red[(w * 8 + fq * 4 + r) * 48 + m * 16 + fr] = acc[m][r];
      }
    }
    __syncthreads();
    if (t < NG_QKV / 2) {
      const int r = t / 24, cq = t % 24;
      const int m = cq >> 3, c = (cq & 7) * 4, g = c >> 4;
      float4 v4 = make_float4(0.f, 0.f, 0.f, 0.f);
#pragma unroll
      for (int ww = 0; ww < 4; ++ww) { const float4 q4 = *(const float4*)(&red[((g * 4 + ww) * 8 + r) * 48 + m * 16 + (c & 15)]); v4.x += q4.x; v4.y += q4.y; v4.z += q4.z; v4.w += q4.w; }
      const u32 pk0 = cvtpk(v4.x, v4.y), pk1 = cvtpk(v4.z, v4.w);
      if (s < NSEG - 1) st_gran2(p.qkvg + ((size_t)(par * NH + h) * 4 + j) * NG_QKV + 2 * t, epoch, pk0, pk1);
      if (m == 1) *(uint2*)(p.KM + (size_t)(s * MM + r) * HD + h * DH + 32 * j + c) = make_uint2(pk0, pk1);
      if (m == 2) *(uint2*)(p.VM + (size_t)(s * MM + r) * HD + h * DH + 32 * j + c) = make_uint2(pk0, pk1);
    }
    STAMP(2);
    if (s == NSEG - 1) break;
    bf16x8 kx[2][4], vx[8];
#pragma unroll
    for (int ks = 0; ks < 4; ++ks)
#pragma unroll
      for (int n = 0; n < 2; ++n) kx[n][ks] = *(const bf16x8*)(&kS[(n * 16 + fr) * LQ + 32 * ks + 8 * fq]);
    __builtin_amdgcn_sched_barrier(0);
    __builtin_amdgcn_s_barrier();
    __builtin_amdgcn_s_sleep(H2DELAY);
    if (w >= H2W0 && w < H2W0 + H2W) {
      constexpr int NL = 12 / H2W;
      u32 v[2 * NL];
      const int wv = w - H2W0;
      const u32 gbase = (u32)((par * NH + h) * 4 * NG_QKV + wv * NL * 128);
      if (!sweep2<NL>(rs_qkv, (gbase + lane * 2) * 8, 128 * 8, epoch, v, tmo)) return;
#pragma unroll
      for (int k = 0; k < NL; ++k) {
        const int ug = wv * NL * 64 + k * 64 + lane, pj = ug / 192, u = ug % 192;
        const int r = u / 24, cq = u % 24, m = cq >> 3, c = 32 * pj + (cq & 7) * 4;
        if (m == 0) *(uint2*)(&qS[r * LQ + c]) = make_uint2(v[2 * k], v[2 * k + 1]);
        else if (m == 1) *(uint2*)(&kS[(32 + r) * LQ + c]) = make_uint2(v[2 * k], v[2 * k + 1]);
        else {
          const int pos = 32 + 8 * (r >> 2) + (r & 3);
          vT[c * LV + pos] = (u16)(v[2 * k] & 0xffffu); vT[(c + 1) * LV + pos] = (u16)(v[2 * k] >> 16);
          vT[(c + 2) * LV + pos] = (u16)(v[2 * k + 1] & 0xffffu); vT[(c + 3) * LV + pos] = (u16)(v[2 * k + 1] >> 16);
        }
      }
    }
    __syncthreads();
    STAMP(3);
    if (w < 4) {
      f32x4 sc[3] = {{0.f, 0.f, 0.f, 0.f}, {0.f, 0.f, 0.f, 0.f}, {0.f, 0.f, 0.f, 0.f}};
      bf16x8 qf[4], km[4];
      uint2 vm[8];
#pragma unroll
      for (int ks = 0; ks < 4; ++ks) {
        qf[ks] = *(const bf16x8*)(&qS[(lane & 7) * LQ + 32 * ks + 8 * fq]);
        km[ks] = *(const bf16x8*)(&kS[(32 + fr) * LQ + 32 * ks + 8 * fq]);
      }
#pragma unroll
      for (int c = 0; c < 8; ++c) vm[c] = *(const uint2*)(&vT[(16 * c + fr) * LV + 32 + 8 * fq]);
      __builtin_amdgcn_sched_barrier(0);
#pragma unroll
      for (int ks = 0; ks < 4; ++ks) {
        sc[0] = __builtin_amdgcn_mfma_f32_16x16x32_bf16(kx[0][ks], qf[ks], sc[0], 0, 0, 0);
        sc[1] = __builtin_amdgcn_mfma_f32_16x16x32_bf16(kx[1][ks], qf[ks], sc[1], 0, 0, 0);
        sc[2] = __builtin_amdgcn_mfma_f32_16x16x32_bf16(km[ks], qf[ks], sc[2], 0, 0, 0);
      }
      __builtin_amdgcn_sched_barrier(0);
#pragma unroll
      for (int c = 0; c < 8; ++c) vx[c] = *(const bf16x8*)(&vT[(16 * c + fr) * LV + 8 * fq]);

      __builtin_amdgcn_sched_barrier(0);
      const float scale = 0.08838834764831845f;
      const bool live2 = fq < 2;
      float xs[3][4];
#pragma unroll
      for (int n = 0; n < 3; ++n)
#pragma unroll
        for (int r = 0; r < 4; r += 2) {
          const u32 a = cvtpk(sc[n][r], sc[n][r + 1]);
          const u32 c2 = cvtpk(lo2f(a) * scale, hi2f(a) * scale);
          xs[n][r] = lo2f(c2); xs[n][r + 1] = hi2f(c2);
        }
      if (!live2) { xs[2][0] = xs[2][1] = xs[2][2] = xs[2][3] = -3.0e38f; }
      float mx = fmaxf(fmaxf(fmaxf(xs[0][0], xs[0][1]), fmaxf(xs[0][2], xs[0][3])), fmaxf(fmaxf(xs[1][0], xs[1][1]), fmaxf(xs[1][2], xs[1][3])));
      mx = fmaxf(mx, fmaxf(fmaxf(xs[2][0], xs[2][1]), fmaxf(xs[2][2], xs[2][3])));
      mx = xrow_max(mx);
      float sm = 0.f;
#pragma unroll
      for (int n = 0; n < 3; ++n)
#pragma unroll
        for (int r = 0; r < 4; ++r) { xs[n][r] = __expf(xs[n][r] - mx); sm += xs[n][r]; }
      sm = xrow_sum(sm);
      const float inv = 1.0f / sm;
      u32x4 pw0, pw1;
      pw0[0] = cvtpk(xs[0][0] * inv, xs[0][1] * inv); pw0[1] = cvtpk(xs[0][2] * inv, xs[0][3] * inv);
      pw0[2] = cvtpk(xs[1][0] * inv, xs[1][1] * inv); pw0[3] = cvtpk(xs[1][2] * inv, xs[1][3] * inv);
      pw1[0] = cvtpk(xs[2][0] * inv, xs[2][1] * inv); pw1[1] = cvtpk(xs[2][2] * inv, xs[2][3] * inv);
      pw1[2] = 0u; pw1[3] = 0u;
      const bf16x8 pf0 = __builtin_bit_cast(bf16x8, pw0), pf1 = __builtin_bit_cast(bf16x8, pw1);
      u32x4 ow[4];
#pragma unroll
      for (int c = 0; c < 8; ++c) {
        const u32x4 vmw = {vm[c].x, vm[c].y, 0u, 0u};
        f32x4 oc = {0.f, 0.f, 0.f, 0.f};
        oc = __builtin_amdgcn_mfma_f32_16x16x32_bf16(vx[c], pf0, oc, 0, 0, 0);
        oc = __builtin_amdgcn_mfma_f32_16x16x32_bf16(__builtin_bit_cast(bf16x8, vmw), pf1, oc, 0, 0, 0);
        ow[c >> 1][(c & 1) * 2] = cvtpk(oc[0], oc[1]); ow[c >> 1][(c & 1) * 2 + 1] = cvtpk(oc[2], oc[3]);
      }
      f32x4 pc[4];
#pragma unroll
      for (int c = 0; c < 4; ++c) pc[c] = (f32x4){0.f, 0.f, 0.f, 0.f};
#pragma unroll
      for (int ks = 0; ks < 4; ++ks)
#pragma unroll
        for (int c = 0; c < 4; ++c) pc[c] = __builtin_amdgcn_mfma_f32_16x16x32_bf16(woS[(c * 4 + ks) * 64], __builtin_bit_cast(bf16x8, ow[ks]), pc[c], 0, 0, 0);
      if (fr < 8) {
#pragma unroll
        for (int c = 0; c < 4; ++c) {
          u64* dst = p.partg + ((size_t)(par * 128 + 32 * j + 8 * w + 2 * c + (fq >> 1)) * NH + h) * 64 + fr * 8 + 4 * (fq & 1);
          st_gran2(dst, epoch, __float_as_uint(pc[c][0]), __float_as_uint(pc[c][1]));
          st_gran2(dst + 2, epoch, __float_as_uint(pc[c][2]), __float_as_uint(pc[c][3]));
        }
      }
    }
    STAMP(4);
    __syncthreads();
    if (w >= 4 && w < 4 + H3W) {
      constexpr int UPW = 32 / H3W;
      constexpr int HG = 64 / UPW, NL = 32 / HG;
      u32 v[2 * NL];
      const int w4 = w - 4;
      const int hg = lane / UPW, u = lane % UPW;
      const u32 gbase = (u32)(((par * 128 + R) * NH + NL * hg) * 64 + (UPW * w4 + u) * 2);
      __builtin_amdgcn_s_sleep(H3DELAY);
      if (!sweep2<NL>(rs_part, gbase * 8, 64 * 8, epoch, v, tmo)) return;
      float f0 = 0.f, f1 = 0.f;
#pragma unroll
      for (int k = 0; k < NL; ++k) { f0 += __uint_as_float(v[2 * k]); f1 += __uint_as_float(v[2 * k + 1]); }
      if (UPW == 8) { f0 = ror8_sum(f0); f1 = ror8_sum(f1); }
      if (UPW <= 16) { f0 = xrow_sum(f0); f1 = xrow_sum(f1); }
      else {
        auto c0 = __builtin_amdgcn_permlane32_swap(__float_as_uint(f0), __float_as_uint(f0), false, false);
        auto c1 = __builtin_amdgcn_permlane32_swap(__float_as_uint(f1), __float_as_uint(f1), false, false);
        f0 = __uint_as_float(c0[0]) + __uint_as_float(c0[1]); f1 = __uint_as_float(c1[0]) + __uint_as_float(c1[1]);
      }
      const u32 pk = cvtpk(f0, f1);
#pragma unroll
      for (int q = 0; q < NREP / HG; ++q) st_gran(p.memg + (size_t)(par * NREP + q * HG + hg) * NG_MEM + R * 32 + UPW * w4 + u, epoch, pk);
      STAMP5;
    }
    if (!load_kv(s + 1)) return;
    __syncthreads();
  }
}

__device__ __forceinline__ void worker_role(const FusedP& p, char* smem, int gw) {
  const int t = threadIdx.x;
  u32* tmo = p.ctl;
  if (gw < 64) {
    const int n = gw;
    const u16* B = (n < 32 ? p.Wk : p.Wv) + (n & 31) * 128;
    u16* C = (n < 32 ? p.Kx : p.Vx) + (n & 31) * 128;
    gemm_tile<64, 128, 4, 2, 4>(smem, p.xn, DM, B, HD, DM, C, HD, nullptr, 0, true);
    asm volatile("s_waitcnt vmcnt(0)" ::: "memory");
    __syncthreads();
    if (t == 0) __hip_atomic_store((gu32*)(p.kvflag + (512 + n) * 16), 1u, RLX_AGENT);
  }
  for (int i = 0; i < 4; ++i) {
    const int id = gw + 128 * i, rb = id >> 6, n = id & 63;
    const u16* B = (n < 32 ? p.Wk : p.Wv) + (n & 31) * 128;
    u16* C = (n < 32 ? p.Kx : p.Vx) + (size_t)rb * 256 * HD + (n & 31) * 128;
    gemm_tile<256, 128, 4, 2, 3>(smem, p.xn + (size_t)rb * 256 * DM, DM, B, HD, DM, C, HD, nullptr, 0, true);
    asm volatile("s_waitcnt vmcnt(0)" ::: "memory");
    __syncthreads();
    if (t == 0) __hip_atomic_store((gu32*)(p.kvflag + (rb * 64 + n) * 16), 1u, RLX_AGENT);
  }
  for (int id = gw; id < 480; id += 128) {
    if (id < 224) {
      const int rb = id / 28, n = id % 28;
      gemm_tile<256, 128, 4, 2, 3>(smem, p.xn + (size_t)rb * 256 * DM, DM, p.w1 + n * 128, FF, DM, p.Hb + (size_t)rb * 256 * FF + n * 128, FF, p.b1 + n * 128, 2, false);
    } else {
      const int q = id - 224, rb = q >> 5, n = q & 31;
      gemm_tile<256, 128, 4, 2, 3>(smem, p.xn + (size_t)rb * 256 * DM, DM, p.Wq + n * 128, HD, DM, p.Qx + (size_t)rb * 256 * HD + n * 128, HD, nullptr, 0, false);
    }
  }
  asm volatile("s_waitcnt vmcnt(0)" ::: "memory");
  __syncthreads();
  if (t == 0) {
    __builtin_amdgcn_fence(__ATOMIC_RELEASE, "agent");
    asm volatile("s_waitcnt vmcnt(0)" ::: "memory");
    __hip_atomic_fetch_add((gu32*)(p.ctl + 64), 1u, RLX_AGENT);
    for (u32 spins = 0;;) {
      if (__hip_atomic_load((gu32*)(p.ctl + 64), RLX_AGENT) >= 128u) break;
      if (++spins > SPIN_CAP) { __hip_atomic_store((gu32*)tmo, 1u, RLX_AGENT); break; }
      __builtin_amdgcn_s_sleep(16);
    }
    __builtin_amdgcn_fence(__ATOMIC_ACQUIRE, "agent");
    asm volatile("s_waitcnt vmcnt(0)" ::: "memory");
  }
  __syncthreads();
  {
    const int mt = gw >> 3, nt = gw & 7;
    gemm_tile<128, 128, 4, 2, 4>(smem, p.Hb + (size_t)mt * 128 * FF, FF, p.w2 + nt * 128, DM, FF, p.Bm + (size_t)mt * 128 * DM + nt * 128, DM, p.b2 + nt * 128, 1, false);
  }
}

__global__ __launch_bounds__(512, 2) void fused_kernel(FusedP p) {
  extern __shared__ __attribute__((aligned(16))) char smem[];
  const int b = blockIdx.x, xcd = b & 7, ib = b >> 3;
  if (ib < 16) chain_role(p, smem, xcd * 16 + ib, xcd * 4 + (ib >> 2), ib & 3);
  else worker_role(p, smem, (ib - 16) * 8 + xcd);
}

__global__ __launch_bounds__(256) void attn_x_kernel(const u16* __restrict__ Q, const u16* __restrict__ Kx, const u16* __restrict__ Vx,
                                                     const u16* __restrict__ KM, const u16* __restrict__ VM, u16* __restrict__ O) {
  constexpr int LVR = 136;
  __shared__ __attribute__((aligned(16))) u16 vS[4][48 * LVR];
  const int t = threadIdx.x, lane = t & 63, w = __builtin_amdgcn_readfirstlane(t >> 6);
  const int item = blockIdx.x * 4 + w, s = item >> 5, h = item & 31;
  const int fr = lane & 15, fq = lane >> 4;
  u16* vw = vS[w];
  const size_t xoff = (size_t)(s * SEG) * HD + h * DH, moff = (size_t)(s * MM) * HD + h * DH;
#pragma unroll
  for (int i = 0; i < 12; ++i) {
    const int e = lane + i * 64, r = e >> 4, c = (e & 15) * 8;
    u32x4 v = {0u, 0u, 0u, 0u};
    if (r < 32) v = *(const u32x4*)(Vx + xoff + (size_t)r * HD + c);
    else if (r < 40) v = *(const u32x4*)(VM + moff + (size_t)(r - 32) * HD + c);
    *(u32x4*)(&vw[r * LVR + c]) = v;
  }
  bf16x8 qf[2][4], kf[3][4];
#pragma unroll
  for (int ks = 0; ks < 4; ++ks) {
#pragma unroll
    for (int n = 0; n < 2; ++n) {
      qf[n][ks] = *(const bf16x8*)(Q + xoff + (size_t)(16 * n + fr) * HD + 32 * ks + 8 * fq);
      kf[n][ks] = *(const bf16x8*)(Kx + xoff + (size_t)(16 * n + fr) * HD + 32 * ks + 8 * fq);
    }
    kf[2][ks] = *(const bf16x8*)(KM + moff + (size_t)(fr & 7) * HD + 32 * ks + 8 * fq);
  }
  asm volatile("s_waitcnt lgkmcnt(0)" ::: "memory");
  __builtin_amdgcn_wave_barrier();
  bf16x8 vf[8][2];
  const int tq = fr >> 2, tp = fr & 3;
#pragma unroll
  for (int c = 0; c < 8; ++c) {
    const s16x4 a0 = __builtin_amdgcn_ds_read_tr16_b64_v4i16((LAS s16x4*)(&vw[(4 * fq + tq) * LVR + 16 * c + 4 * tp]));
    const s16x4 a1 = __builtin_amdgcn_ds_read_tr16_b64_v4i16((LAS s16x4*)(&vw[(16 + 4 * fq + tq) * LVR + 16 * c + 4 * tp]));
    const s16x4 a2 = __builtin_amdgcn_ds_read_tr16_b64_v4i16((LAS s16x4*)(&vw[(32 + 4 * fq + tq) * LVR + 16 * c + 4 * tp]));
    vf[c][0] = (bf16x8){a0[0], a0[1], a0[2], a0[3], a1[0], a1[1], a1[2], a1[3]};
    vf[c][1] = (bf16x8){a2[0], a2[1], a2[2], a2[3], 0, 0, 0, 0};
  }
  const float scale = 0.08838834764831845f;
#pragma unroll
  for (int qt = 0; qt < 2; ++qt) {
    f32x4 sc[3] = {{0.f, 0.f, 0.f, 0.f}, {0.f, 0.f, 0.f, 0.f}, {0.f, 0.f, 0.f, 0.f}};
#pragma unroll
    for (int ks = 0; ks < 4; ++ks)
#pragma unroll
      for (int n = 0; n < 3; ++n) sc[n] = __builtin_amdgcn_mfma_f32_16x16x32_bf16(kf[n][ks], qf[qt][ks], sc[n], 0, 0, 0);
    const int qi = 16 * qt + fr;
    float xs[3][4];
#pragma unroll
    for (int n = 0; n < 3; ++n)
#pragma unroll
      for (int r = 0; r < 4; r += 2) {
        const u32 a = cvtpk(sc[n][r], sc[n][r + 1]);
        const u32 c2 = cvtpk(lo2f(a) * scale, hi2f(a) * scale);
        xs[n][r] = lo2f(c2); xs[n][r + 1] = hi2f(c2);
      }
    float mx = -3.0e38f;
#pragma unroll
    for (int n = 0; n < 3; ++n)
#pragma unroll
      for (int r = 0; r < 4; ++r) {
        const bool live = (n < 2) ? (16 * n + 4 * fq + r <= qi) : (fq < 2);
        if (!live) xs[n][r] = -3.0e38f;
        mx = fmaxf(mx, xs[n][r]);
      }
    mx = xrow_max(mx);
    float sm = 0.f;
#pragma unroll
    for (int n = 0; n < 3; ++n)
#pragma unroll
      for (int r = 0; r < 4; ++r) { xs[n][r] = __expf(xs[n][r] - mx); sm += xs[n][r]; }
    sm = xrow_sum(sm);
    const float inv = 1.0f / sm;
    u32x4 pw0, pw1;
    pw0[0] = cvtpk(xs[0][0] * inv, xs[0][1] * inv); pw0[1] = cvtpk(xs[0][2] * inv, xs[0][3] * inv);
    pw0[2] = cvtpk(xs[1][0] * inv, xs[1][1] * inv); pw0[3] = cvtpk(xs[1][2] * inv, xs[1][3] * inv);
    pw1[0] = cvtpk(xs[2][0] * inv, xs[2][1] * inv); pw1[1] = cvtpk(xs[2][2] * inv, xs[2][3] * inv);
    pw1[2] = 0u; pw1[3] = 0u;
    const bf16x8 pf0 = __builtin_bit_cast(bf16x8, pw0), pf1 = __builtin_bit_cast(bf16x8, pw1);
    u16* orow = O + xoff + (size_t)qi * HD + 4 * fq;
#pragma unroll
    for (int c = 0; c < 8; ++c) {
      f32x4 oc = {0.f, 0.f, 0.f, 0.f};
      oc = __builtin_amdgcn_mfma_f32_16x16x32_bf16(vf[c][0], pf0, oc, 0, 0, 0);
      oc = __builtin_amdgcn_mfma_f32_16x16x32_bf16(vf[c][1], pf1, oc, 0, 0, 0);
      *(uint2*)(orow + 16 * c) = make_uint2(cvtpk(oc[0], oc[1]), cvtpk(oc[2], oc[3]));
    }
  }
}

extern "C" void kernel_launch(void* const* d_in, const int* in_sizes, int n_in,
                              void* d_out, int out_size, void* d_ws, size_t ws_size,
                              hipStream_t stream) {
  const u16* x = (const u16*)d_in[0];
  const u16* mem0 = (const u16*)d_in[1];
  const u16* Wq = (const u16*)d_in[2];
  const u16* Wk = (const u16*)d_in[3];
  const u16* Wv = (const u16*)d_in[4];
  const u16* Wo = (const u16*)d_in[5];
  const u16* w1 = (const u16*)d_in[8];
  const u16* b1 = (const u16*)d_in[9];
  const u16* w2 = (const u16*)d_in[10];
  const u16* b2 = (const u16*)d_in[11];
  u16* out = (u16*)d_out;

  char* ws = (char*)d_ws;
  size_t off = 0;
  auto carve = [&](size_t bytes) { char* q = ws + off; off += (bytes + 255) & ~(size_t)255; return q; };
  u64* memg = (u64*)carve((size_t)2 * NREP * NG_MEM * 8);
  u64* qkvg = (u64*)carve((size_t)2 * NH * 4 * NG_QKV * 8);
  u64* partg = (u64*)carve((size_t)2 * 128 * NH * 64 * 8);
  u32* kvflag = (u32*)carve((size_t)576 * 64);
  u32* ctl = (u32*)carve(1024);
  const size_t sync_bytes = off;
  u16* xn = (u16*)carve((size_t)S_ * DM * 2);
  u16* Qx = (u16*)carve((size_t)S_ * HD * 2);
  u16* Kx = (u16*)carve((size_t)S_ * HD * 2);
  u16* Vx = (u16*)carve((size_t)S_ * HD * 2);
  u16* Hb = (u16*)carve((size_t)S_ * FF * 2);
  u16* Ox = (u16*)carve((size_t)S_ * HD * 2);
  u16* KM = (u16*)carve((size_t)NSEG * MM * HD * 2);
  u16* VM = (u16*)carve((size_t)NSEG * MM * HD * 2);
  u16* Bm = (u16*)carve((size_t)S_ * DM * 2);

  ln_kernel<<<S_, 256, 0, stream>>>(x, xn, (u32x4*)ws, (int)(sync_bytes / 16));
  {
    FusedP f{xn, mem0, Wq, Wk, Wv, Wo, w1, b1, w2, b2, Qx, Kx, Vx, Hb, Bm, KM, VM, memg, qkvg, partg, kvflag, ctl};
    (void)hipFuncSetAttribute((const void*)fused_kernel, hipFuncAttributeMaxDynamicSharedMemorySize, FUSED_LDS);
    fused_kernel<<<256, 512, FUSED_LDS, stream>>>(f);
  }
  attn_x_kernel<<<NSEG * NH / 4, 256, 0, stream>>>(Qx, Kx, Vx, KM, VM, Ox);
  {
    constexpr int LDSW = gemm_lds(WO_BM, WO_BN, WO_WM, WO_WN, WO_NS);
    (void)hipFuncSetAttribute((const void*)gemm_wo_kernel, hipFuncAttributeMaxDynamicSharedMemorySize, LDSW);
    gemm_wo_kernel<<<(S_ / WO_BM) * (DM / WO_BN), WO_WM * WO_WN * 64, LDSW, stream>>>(Ox, Wo, x, Bm, out);
  }
}
```
